```python
import torch

def _softmax(x, axis=-1):
    return torch.softmax(x, dim=axis)
def _gelu(x):
    return torch.nn.functional.gelu(x, approximate="tanh")
def _silu(x):
    return torch.nn.functional.silu(x)
def _sigmoid(x):
    return torch.sigmoid(x)
def _layernorm(x, axis=-1, eps=1e-5):
    m = x.mean(dim=axis, keepdim=True)
    v = x.var(dim=axis, keepdim=True, unbiased=False)
    return (x - m) / torch.sqrt(v + eps)
def _scatter_add(tgt, idx, src, axis=0):
    return tgt.clone().index_add_(axis, idx, src)
def _segment_sum(src, seg, n):
    out = torch.zeros((n,) + src.shape[1:], dtype=src.dtype)
    return out.index_add_(0, seg, src)
def _pad(x, pw):
    flat = tuple(v for pair in reversed(pw) for v in pair)
    return torch.nn.functional.pad(x, flat)

kernel_name = 'interleave_layers__sliding_block__glu_gated_glu__prompt'

def setup_inputs():

    _w = (lambda a: (lambda t: t.to(torch.bfloat16) if t.is_floating_point() else t)(torch.as_tensor(a)))
    def _A_setup():
        B, S, D = 1, 2048, 512
        return {
            "x": _w((lambda *s: torch.randn(*s, dtype=torch.bfloat16) * 0.05)(B, S, D)),
            "wqkv": _w((lambda *s: torch.randn(*s, dtype=torch.bfloat16) * 0.05)(D, 3 * D)),
            "wo": _w((lambda *s: torch.randn(*s, dtype=torch.bfloat16) * 0.05)(D, D)),
        }
    _A_in = _A_setup()

    def _B_setup():
        B, S, D, F = 1, 2048, 512, 1792
        return {
            "x": _w((lambda *s: torch.randn(*s, dtype=torch.bfloat16) * 0.05)(B, S, D)),
            "w_gate": _w((lambda *s: torch.randn(*s, dtype=torch.bfloat16) * 0.05)(D, F)),
            "w_up": _w((lambda *s: torch.randn(*s, dtype=torch.bfloat16) * 0.05)(D, F)),
            "w_down": _w((lambda *s: torch.randn(*s, dtype=torch.bfloat16) * 0.05)(F, D)),
        }
    _B_in = _B_setup()

    return {
        "A_x": _A_in["x"],
        "A_wqkv": _A_in["wqkv"],
        "A_wo": _A_in["wo"],
        "B_x": _B_in["x"],
        "B_w_gate": _B_in["w_gate"],
        "B_w_up": _B_in["w_up"],
        "B_w_down": _B_in["w_down"],
    }

def reference(A_x, A_wqkv, A_wo, B_x, B_w_gate, B_w_up, B_w_down):
    def _A_ref(x, wqkv, wo):
        B, S, D = x.shape
        H = 32; Dh = D // H; W = 256
        qkv = (lambda x, *d: x.permute(*d))((x @ wqkv).reshape(B, S, 3, H, Dh), 2, 0, 3, 1, 4)
        q, k, v = qkv[0], qkv[1], qkv[2]
        scores = torch.einsum("bhqd,bhkd->bhqk", q, k) * (Dh ** -0.5)
        idx = torch.arange(S)
        mask = (idx[:, None] - idx[None, :] >= 0) & (idx[:, None] - idx[None, :] < W)
        scores = torch.where(mask[None, None], scores, -1e9)
        p = _softmax(scores, axis=-1)
        o = torch.einsum("bhqk,bhkd->bhqd", p, v)
        return (lambda x, *d: x.permute(*d))(o, 0, 2, 1, 3).reshape(B, S, D) @ wo

    def _B_ref(x, w_gate, w_up, w_down):
        g = torch.einsum("bsd,df->bsf", x, w_gate)
        u = torch.einsum("bsd,df->bsf", x, w_up)
        h = _sigmoid(g) * u
        return torch.einsum("bsf,fd->bsd", h, w_down)

    x = A_x
    x = x + _A_ref(_layernorm(x, axis=-1), A_wqkv, A_wo)
    x = x + _B_ref(_layernorm(x, axis=-1), B_w_gate, B_w_up, B_w_down)
    x = x + _A_ref(_layernorm(x, axis=-1), A_wqkv, A_wo)
    x = x + _B_ref(_layernorm(x, axis=-1), B_w_gate, B_w_up, B_w_down)
    return x
```

```cpp
#include <hip/hip_runtime.h>
#include <hip/hip_bf16.h>
typedef unsigned short u16;
__device__ __forceinline__ float bf2f(u16 b){ return __uint_as_float(((unsigned)b)<<16); }
__device__ __forceinline__ u16 f2bf(float f){ unsigned u=__float_as_uint(f); u += 0x7FFFu + ((u>>16)&1u); return (u16)(u>>16); }
constexpr int S=2048, D=512, F=1792, H=32, DH=16, W=256;

__global__ void k_ln(const u16* x, u16* y){
  int row=blockIdx.x; int t=threadIdx.x;
  float v[8]; float s=0;
  for(int i=0;i<8;i++){ v[i]=bf2f(x[row*D+t*8+i]); s+=v[i]; }
  for(int o=32;o;o>>=1) s+=__shfl_xor(s,o);
  float m=s/D; float q=0;
  for(int i=0;i<8;i++){ float d=v[i]-m; q+=d*d; }
  for(int o=32;o;o>>=1) q+=__shfl_xor(q,o);
  float r=rsqrtf(q/D+1e-5f);
  for(int i=0;i<8;i++) y[row*D+t*8+i]=f2bf((v[i]-m)*r);
}
__global__ void k_gemm(const u16* A,const u16* B,const u16* res,u16* C,int M,int N,int K){
  int n=blockIdx.x*64+threadIdx.x; int m0=blockIdx.y*8;
  float acc[8]={0,0,0,0,0,0,0,0};
  for(int k=0;k<K;k++){ float b=bf2f(B[k*N+n]); for(int i=0;i<8;i++) acc[i]+=bf2f(A[(m0+i)*K+k])*b; }
  for(int i=0;i<8;i++){ float v=acc[i]; if(res) v=bf2f(f2bf(v))+bf2f(res[(m0+i)*N+n]); C[(m0+i)*N+n]=f2bf(v);}
}
__global__ void k_glu(const u16* g,const u16* u,u16* h,int n){
  int i=blockIdx.x*256+threadIdx.x; if(i<n){ float gg=bf2f(g[i]); float s=1.f/(1.f+__expf(-gg)); h[i]=f2bf(s*bf2f(u[i])); }
}
__global__ void k_attn(const u16* qkv,u16* o){
  int q=blockIdx.x*64+threadIdx.x; int h=blockIdx.y;
  float qq[16]; for(int d=0;d<16;d++) qq[d]=bf2f(qkv[q*1536+h*16+d]);
  int lo=q-W+1; if(lo<0) lo=0;
  float m=-1e30f;
  for(int j=lo;j<=q;j++){ float s=0; for(int d=0;d<16;d++) s+=qq[d]*bf2f(qkv[j*1536+512+h*16+d]); s*=0.25f; m=fmaxf(m,s);}
  float l=0; float acc[16]; for(int d=0;d<16;d++) acc[d]=0;
  for(int j=lo;j<=q;j++){ float s=0; for(int d=0;d<16;d++) s+=qq[d]*bf2f(qkv[j*1536+512+h*16+d]); s*=0.25f; float p=__expf(s-m); l+=p; for(int d=0;d<16;d++) acc[d]+=p*bf2f(qkv[j*1536+1024+h*16+d]); }
  for(int d=0;d<16;d++) o[q*D+h*16+d]=f2bf(acc[d]/l);
}
extern "C" void kernel_launch(void* const* d_in, const int* in_sizes, int n_in, void* d_out, int out_size, void* d_ws, size_t ws_size, hipStream_t stream){
  const u16* x=(const u16*)d_in[0]; const u16* wqkv=(const u16*)d_in[1]; const u16* wo=(const u16*)d_in[2];
  const u16* wg=(const u16*)d_in[4]; const u16* wu=(const u16*)d_in[5]; const u16* wd=(const u16*)d_in[6];
  u16* ws=(u16*)d_ws; u16* ln=ws; u16* qkv=ln+S*D; u16* o=qkv+S*1536; u16* x1=o+S*D; u16* g=x1+S*D; u16* u=g+S*F; u16* h=u+S*F; u16* x2=h+S*F;
  const u16* cur=x;
  for(int l=0;l<2;l++){
    k_ln<<<S,64,0,stream>>>(cur,ln);
    k_gemm<<<dim3(1536/64,S/8),64,0,stream>>>(ln,wqkv,nullptr,qkv,S,1536,D);
    k_attn<<<dim3(S/64,H),64,0,stream>>>(qkv,o);
    k_gemm<<<dim3(D/64,S/8),64,0,stream>>>(o,wo,cur,x1,S,D,D);
    k_ln<<<S,64,0,stream>>>(x1,ln);
    k_gemm<<<dim3(F/64,S/8),64,0,stream>>>(ln,wg,nullptr,g,S,F,D);
    k_gemm<<<dim3(F/64,S/8),64,0,stream>>>(ln,wu,nullptr,u,S,F,D);
    k_glu<<<(S*F+255)/256,256,0,stream>>>(g,u,h,S*F);
    u16* dst = l==0? x2 : (u16*)d_out;
    k_gemm<<<dim3(D/64,S/8),64,0,stream>>>(h,wd,x1,dst,S,D,F);
    cur=x2;
  }
}
```

```cpp
#include <hip/hip_runtime.h>
#include <hip/hip_bf16.h>

#define PROF 0

namespace {
typedef unsigned short u16;
typedef unsigned v4u __attribute__((ext_vector_type(4)));
typedef unsigned v2u __attribute__((ext_vector_type(2)));
typedef short v4s __attribute__((ext_vector_type(4)));
typedef float f32x4 __attribute__((ext_vector_type(4)));
typedef float f32x16 __attribute__((ext_vector_type(16)));
typedef __bf16 v2bf __attribute__((ext_vector_type(2)));
typedef __bf16 v8bf __attribute__((ext_vector_type(8)));
#define LAS __attribute__((address_space(3)))
#define GAS __attribute__((address_space(1)))
#define DI __device__ __forceinline__
typedef GAS unsigned gu32;
typedef LAS unsigned char* lptr;
#define RLX_AGENT __ATOMIC_RELAXED, __HIP_MEMORY_SCOPE_AGENT

constexpr int NWG = 256, NTHR = 512;
constexpr int KB = 1024;
constexpr int FLAG_STRIDE = 256;
enum { F_P1 = 0, F_P2 = 1, F_KV = 2, F_O = 3, F_X1 = 4, F_HE = 5, F_HL = 6, F_X2 = 7, F_PER_LAYER = 6, F_KINDS = 14 };
constexpr int WS_EPOCH = 0;
constexpr int WS_FLAGS = 4 * KB;
constexpr int WS_WA = WS_FLAGS + F_KINDS * 256 * FLAG_STRIDE;
constexpr int WS_WB = WS_WA + 16 * 99 * KB;
constexpr int WS_WC = WS_WB + 8 * 64 * KB;
constexpr int WS_WD = WS_WC + 16 * 231 * KB;
constexpr int WS_ACT = WS_WD + 8 * 224 * KB;
constexpr int A_XT = 0, A_ST0 = A_XT + 2048 * KB, A_KV = A_ST0 + 256 * KB, A_OT = A_KV + 4096 * KB, A_X1T = A_OT + 2048 * KB,
              A_ST1 = A_X1T + 2048 * KB, A_HT = A_ST1 + 256 * KB, A_LAYER = A_HT + 64 * 112 * KB;
constexpr int WS_DBG = WS_ACT + 2 * A_LAYER;
constexpr int WS_BYTES = WS_DBG + 256 * KB * 2;
constexpr int CH = 33 * KB;
constexpr int BUF_A = 0, BUF_B = CH, BUF_C = 2 * CH, BUF_D = 3 * CH, SP = 4 * CH;
constexpr int L_KV = SP;
constexpr int L_QB = SP + 16 * KB;
constexpr int L_ONES = SP + 20 * KB;
constexpr int L_RS = SP + 22 * KB;
constexpr int L_DBG = SP + 23 * KB;
constexpr int LDS_BYTES = SP + 24 * KB;

constexpr float NEG_BIG = -1e30f;
constexpr float LOG2E = 1.4426950408889634f;
constexpr int AUX_SC1 = 16;

struct Params {
  const u16 *x, *wqkv, *wo, *wg, *wu, *wd;
  u16* out;
  unsigned char* ws;
};

DI f32x16 mfma(v4u a, v4u b, f32x16 c) {
  return __builtin_amdgcn_mfma_f32_32x32x16_bf16(__builtin_bit_cast(v8bf, a), __builtin_bit_cast(v8bf, b), c, 0, 0, 0);
}
DI unsigned pk(float a, float b) { v2bf v = {(__bf16)a, (__bf16)b}; return __builtin_bit_cast(unsigned, v); }
DI float bflo(unsigned w) { return __uint_as_float(w << 16); }
DI float bfhi(unsigned w) { return __uint_as_float(w & 0xffff0000u); }
DI float xhalf_sum(float v) { auto r = __builtin_amdgcn_permlane32_swap(__float_as_uint(v), __float_as_uint(v), false, false); return __uint_as_float(r[0]) + __uint_as_float(r[1]); }
DI float xhalf_max(float v) { auto r = __builtin_amdgcn_permlane32_swap(__float_as_uint(v), __float_as_uint(v), false, false); return __builtin_fmaxf(__uint_as_float(r[0]), __uint_as_float(r[1])); }
DI v4u ldsr(lptr p) { return *(LAS v4u*)p; }
DI void ldsw(lptr p, v4u v) { *(LAS v4u*)p = v; }
typedef __amdgpu_buffer_rsrc_t rsrc_t;
DI v4u gld(rsrc_t rs, int voff, int soff) { return __builtin_amdgcn_raw_buffer_load_b128(rs, voff, soff, AUX_SC1); }
DI v4u gldf(rsrc_t rs, int l16, int soff, int i) { return __builtin_amdgcn_raw_buffer_load_b128(rs, l16 + (i & 3) * KB, soff + (i >> 2) * 4 * KB, AUX_SC1); }
DI int opv(int x) { asm volatile("" : "+v"(x)); return x; }
DI int ops(int x) { asm volatile("" : "+s"(x)); return x; }
DI void gst(rsrc_t rs, v4u v, int voff, int soff) { __builtin_amdgcn_raw_buffer_store_b128(v, rs, voff, soff, AUX_SC1); }
DI void gst2(rsrc_t rs, v2u v, int voff, int soff) { __builtin_amdgcn_raw_buffer_store_b64(v, rs, voff, soff, AUX_SC1); }
#define SCHED() __builtin_amdgcn_sched_barrier(0)
#define BAR() asm volatile("s_barrier" ::: "memory")
#define BARL() asm volatile("s_waitcnt lgkmcnt(0)\n\ts_barrier" ::: "memory")
#define WAITV(n) asm volatile("s_waitcnt vmcnt(" #n ")" ::: "memory")

template <int G, int LO> DI void dma4(rsrc_t rs, int sb, int lb, int voff) {
  unsigned keep, so;
  asm volatile("s_mov_b32 %0, m0\n\ts_add_u32 m0, %5, %7\n\ts_add_u32 %1, %4, %6\n\ts_nop 2\n\t"
               "buffer_load_dwordx4 %2, %3, %1 offen sc1 lds\n\tbuffer_load_dwordx4 %2, %3, %1 offen offset:1024 sc1 lds\n\t"
               "buffer_load_dwordx4 %2, %3, %1 offen offset:2048 sc1 lds\n\tbuffer_load_dwordx4 %2, %3, %1 offen offset:3072 sc1 lds\n\t"
               "s_mov_b32 m0, %0" : "=&s"(keep), "=&s"(so) : "v"(voff), "s"(rs), "s"(sb), "s"(lb), "i"(G), "i"(LO) : "memory", "scc");
}
template <int G, int LO> DI void dma1(rsrc_t rs, int sb, int lb, int voff) {
  unsigned keep, so;
  asm volatile("s_mov_b32 %0, m0\n\ts_add_u32 m0, %5, %7\n\ts_add_u32 %1, %4, %6\n\ts_nop 2\n\t"
               "buffer_load_dwordx4 %2, %3, %1 offen sc1 lds\n\t"
               "s_mov_b32 m0, %0" : "=&s"(keep), "=&s"(so) : "v"(voff), "s"(rs), "s"(sb), "s"(lb), "i"(G), "i"(LO) : "memory", "scc");
}
template <bool EXTRA, int G, int LBUF> DI void dma_chunk(rsrc_t rs, int sb, int lb, int voff) {
  dma4<G, LBUF>(rs, sb, lb, voff);
  dma4<G + 4 * KB, LBUF + 4 * KB>(rs, sb, lb, voff);
  if (EXTRA && lb == 0) dma1<G + 32 * KB, LBUF + 32 * KB>(rs, sb, lb, voff);
}

DI gu32* flag_ptr(unsigned char* ws, int kind, int idx) { return (gu32*)(ws + WS_FLAGS + (size_t)(kind * 256 + idx) * FLAG_STRIDE); }
DI void set_flag(unsigned char* ws, int kind, int idx, unsigned epoch) { __hip_atomic_store(flag_ptr(ws, kind, idx), epoch, RLX_AGENT); }
DI void poll(gu32* f, bool active, unsigned epoch, bool& dead) {
  if (!dead) {
    const unsigned long long t0 = __builtin_amdgcn_s_memrealtime();
    for (unsigned spins = 1;; ++spins) {
      unsigned v = epoch;
      if (active) v = __hip_atomic_load(f, RLX_AGENT);
      if (__all(v == epoch)) break;
      if ((spins & 63u) == 0u && __builtin_amdgcn_s_memrealtime() - t0 > 2000000ull) { dead = true; break; }
    }
  }
  __builtin_amdgcn_fence(__ATOMIC_ACQUIRE, "wavefront");
}

#define STAMP(i) do {} while (0)

DI void finish_stats(const v4u (&st)[4], float& mean, float& rstd) {
  float s = 0.f, q = 0.f;
#pragma unroll
  for (int i = 0; i < 4; ++i) { s += __uint_as_float(st[i].x) + __uint_as_float(st[i].z); q += __uint_as_float(st[i].y) + __uint_as_float(st[i].w); }
  s = xhalf_sum(s); q = xhalf_sum(q);
  mean = s * (1.f / 512.f);
  const float var = __builtin_fmaxf(q * (1.f / 512.f) - mean * mean, 0.f);
  rstd = __builtin_amdgcn_rsqf(var + 1e-5f);
}
DI v4u mean_frag(float mean) {
  const float nm = -mean;
  const unsigned hh = pk(nm, nm);
  const float lo = nm - bflo(hh);
  const unsigned ll = pk(lo, lo);
  return v4u{hh, ll, hh, ll};
}
DI void store_stats(rsrc_t rs, const float (&v)[16], int lane, int soff) {
  float s = 0.f, q = 0.f;
#pragma unroll
  for (int i = 0; i < 16; ++i) { s += v[i]; q = __builtin_fmaf(v[i], v[i], q); }
  s = xhalf_sum(s); q = xhalf_sum(q);
  if (lane < 32) gst2(rs, v2u{__float_as_uint(s), __float_as_uint(q)}, lane * 16, soff);
}

struct Attn { f32x16 O; float m; };
DI float max16(const f32x16& s, float m) {
#pragma unroll
  for (int i = 0; i < 16; i += 2) m = __builtin_fmaxf(__builtin_fmaxf(m, s[i]), s[i + 1]);
  return m;
}
DI f32x16 pv(const f32x16& s, v4u v0, v4u v1, f32x16 O) {
  float p[16];
#pragma unroll
  for (int i = 0; i < 16; ++i) p[i] = __builtin_amdgcn_exp2f(s[i]);
  const v4u p0 = {pk(p[0], p[1]), pk(p[2], p[3]), pk(p[4], p[5]), pk(p[6], p[7])};
  const v4u p1 = {pk(p[8], p[9]), pk(p[10], p[11]), pk(p[12], p[13]), pk(p[14], p[15])};
  O = mfma(v0, p0, O);
  O = mfma(v1, p1, O);
  return O;
}
template <bool FIRST>
DI void attn_chunk(Attn& A, v4u Q, lptr kp, lptr vp, int vstride, int t0, int t1, int tm, const f32x16& mask) {
  const f32x16 zero = {};
  float mx = FIRST ? NEG_BIG : A.m;
  f32x16 sm = zero;
  if (tm >= 0) { sm = mfma(ldsr(kp + tm * 4096), Q, mask); mx = max16(sm, mx); }
  for (int t = t0; t < t1; ++t) { const f32x16 s = mfma(ldsr(kp + t * 4096), Q, zero); mx = max16(s, mx); }
  mx = xhalf_max(mx);
  if (FIRST) A.O = zero;
  else { const float al = __builtin_amdgcn_exp2f(A.m - mx); A.O *= al; }
  A.m = mx;
  if (tm >= 0) { A.O = pv(sm - mx, ldsr(vp + tm * vstride), ldsr(vp + tm * vstride + 1024), A.O); }
  f32x16 nm;
#pragma unroll
  for (int i = 0; i < 16; ++i) nm[i] = -mx;
  for (int t = t0; t < t1; ++t) {
    const f32x16 s = mfma(ldsr(kp + t * 4096), Q, nm);
    A.O = pv(s, ldsr(vp + t * vstride), ldsr(vp + t * vstride + 1024), A.O);
  }
}
DI void make_mask(f32x16& mk, int lane, bool old) {
  const int d = (lane & 31) - 4 * (lane >> 5);
#pragma unroll
  for (int i = 0; i < 16; ++i) { const bool le = ((i & 3) + 8 * (i >> 2)) <= d; mk[i] = (le != old) ? 0.f : NEG_BIG; }
}
template <int HD> DI v4u attn_out(const Attn& A) {
  const float r = __builtin_amdgcn_rcpf(A.O[HD ? 0 : 8]);
  const int o = HD ? 8 : 0;
  return v4u{pk(A.O[o] * r, A.O[o + 1] * r), pk(A.O[o + 2] * r, A.O[o + 3] * r), pk(A.O[o + 4] * r, A.O[o + 5] * r), pk(A.O[o + 6] * r, A.O[o + 7] * r)};
}

DI void finish_tile(lptr L, const f32x16& keep, v4u (&res)[2], float (&v)[16], int w, int lane) {
  lptr xp = L + L_KV + (w ^ 2) * 4096 + lane * 16;
#pragma unroll
  for (int qd = 0; qd < 4; ++qd) {
    const f32x4 r = *(LAS f32x4*)(xp + qd * 1024);
#pragma unroll
    for (int j = 0; j < 4; ++j) v[4 * qd + j] = keep[4 * qd + j] + r[j];
  }
#pragma unroll
  for (int e = 0; e < 2; ++e)
#pragma unroll
    for (int j = 0; j < 4; ++j) { v[8 * e + 2 * j] += bflo(res[e][j]); v[8 * e + 2 * j + 1] += bfhi(res[e][j]); }
#pragma unroll
  for (int e = 0; e < 2; ++e) res[e] = v4u{pk(v[8 * e], v[8 * e + 1]), pk(v[8 * e + 2], v[8 * e + 3]), pk(v[8 * e + 4], v[8 * e + 5]), pk(v[8 * e + 6], v[8 * e + 7])};
}
DI void send_tile(lptr L, const f32x16& send, int w, int lane) {
  lptr xp = L + L_KV + w * 4096 + lane * 16;
#pragma unroll
  for (int qd = 0; qd < 4; ++qd) *(LAS f32x4*)(xp + qd * 1024) = f32x4{send[4 * qd], send[4 * qd + 1], send[4 * qd + 2], send[4 * qd + 3]};
}

__global__ void __launch_bounds__(NTHR, 2) fused_layers(Params p) {
  extern __shared__ __attribute__((aligned(16))) unsigned char smem[];
  lptr L = (lptr)smem;
  const int tid = threadIdx.x, wave = __builtin_amdgcn_readfirstlane(tid >> 6);
  const int b = blockIdx.x, g = (b & 7) * 2 + (b >> 7), m = (b >> 3) & 15, me = g * 16 + m;
  unsigned char* ws = p.ws;
  const rsrc_t rs = __builtin_amdgcn_make_buffer_rsrc(ws, 0, WS_BYTES, 0x00020000);
  const unsigned epoch = __builtin_amdgcn_readfirstlane(__hip_atomic_load((gu32*)(ws + WS_EPOCH), RLX_AGENT)) + 1u;
  asm volatile("" :: "s"(epoch));
  const int th = m >> 3, fb = m & 7;
  bool dead = false;
#define LANE_VALS() const int lane = opv(tid) & 63, r32 = lane & 31, hf = lane >> 5, l16 = lane * 16; (void)r32; (void)hf; (void)l16
  if (tid < 8) *(LAS unsigned*)(L + L_ONES + (tid >> 2) * 1024 + (tid & 3) * 4) = 0x3f803f80u;

  if (wave < 4) {
    const int w = wave;
    const int ttr = 4 * g + 2 * th + (w & 1), kh = w >> 1, ftile = 2 * fb + kh;
    const int tta = 4 * g + w;
    v4u res[2];
    {
      LANE_VALS();
      if (w == 0) STAMP(0);
      const GAS v4u* xs = (const GAS v4u*)(p.x + (size_t)(ttr * 32 + r32) * 512 + ftile * 32 + hf * 8);
      res[0] = xs[0]; res[1] = xs[2];
      const int so = WS_ACT + A_XT + (ttr * 32 + 2 * ftile) * KB;
      gst(rs, res[0], l16, so); gst(rs, res[1], l16 + KB, so);
      float v[16];
#pragma unroll
      for (int e = 0; e < 2; ++e)
#pragma unroll
        for (int j = 0; j < 4; ++j) { v[8 * e + 2 * j] = bflo(res[e][j]); v[8 * e + 2 * j + 1] = bfhi(res[e][j]); }
      store_stats(rs, v, lane, WS_ACT + A_ST0 + ttr * 4096 + (ftile >> 1) * 512 + (ftile & 1) * 8);
      WAITV(0);
      BAR();
      if (w == 0) STAMP(1);
      BAR();
    }
#pragma unroll 1
    for (int layer = 0; layer < 2; ++layer) {
      const int act = WS_ACT + layer * A_LAYER;
      const int fl = layer * F_PER_LAYER;
      const int sb = 2 + layer * 28;
      (void)sb;
      v4u xf[32];
      v4u Qa;
      {
        LANE_VALS();
        if (w == 0) STAMP(sb + 0);
        constexpr int WIN = 12;
        v4u st[4];
        const int sto = ops(act + A_ST0 + tta * 4096), xo = ops(act + A_XT + tta * 32 * KB);
#pragma unroll
        for (int i = 0; i < 4; ++i) st[i] = gld(rs, hf * 2048 + r32 * 16 + i * 512, sto);
#pragma unroll
        for (int i = 0; i < WIN; ++i) xf[i] = gldf(rs, l16, xo, i);
        SCHED();
        BAR();
        f32x16 aK = {}, aV = {};
        constexpr int PD = 2;
        v4u wk[PD], wv[PD];
        lptr pa = L + BUF_A + l16, pb = L + opv(BUF_B + l16);
#pragma unroll
        for (int i = 0; i < PD; ++i) { wk[i] = ldsr(pa + 2 * i * KB); wv[i] = ldsr(pa + (2 * i + 1) * KB); }
        float mean, rstd; v4u xe, wex;
#pragma unroll
        for (int s = 0; s < 32; ++s) {
          if (s + WIN < 32) xf[s + WIN] = gldf(rs, l16, xo, s + WIN);
          const v4u ck = wk[s % PD], cv = wv[s % PD];
          if (s + PD == 16) { wex = ldsr(pa + 32 * KB); BAR(); }
          if (s + PD < 16) { wk[s % PD] = ldsr(pa + 2 * (s + PD) * KB); wv[s % PD] = ldsr(pa + (2 * (s + PD) + 1) * KB); }
          else if (s + PD < 32) { wk[s % PD] = ldsr(pb + 2 * (s + PD - 16) * KB); wv[s % PD] = ldsr(pb + (2 * (s + PD - 16) + 1) * KB); }
          aK = mfma(ck, xf[s], aK);
          aV = mfma(xf[s], cv, aV);
          if (s == 4) { finish_stats(st, mean, rstd); xe = mean_frag(mean); if (lane < 32) *(LAS float*)(L + L_RS + w * 128 + lane * 4) = rstd; }
          if (s == 17) { aK = mfma(wex, xe, aK); wex = ldsr(pb + 32 * KB); }
          SCHED();
        }
        aV = mfma(xe, wex, aV);
        if (w == 0) STAMP(sb + 1);
        BAR();
        lptr pc = L + opv(BUF_C + l16);
        constexpr int PQ = 4;
        v4u wq[PQ];
#pragma unroll
        for (int i = 0; i < PQ; ++i) wq[i] = ldsr(pc + i * KB);
        f32x16 aQ = {};
        lptr kvl = L + opv(L_KV + w * 4096 + l16);
        const int kvo = ops(act + A_KV + (me * 4 + w) * 4096);
        f32x4 rsv[4];
#pragma unroll
        for (int s = 0; s < 33; ++s) {
          const v4u cq = wq[s % PQ];
          if (s + PQ < 33) wq[s % PQ] = ldsr(pc + (s + PQ) * KB);
          aQ = mfma(cq, s < 32 ? xf[s < 32 ? s : 0] : xe, aQ);
          if (s == 1) {
#pragma unroll
            for (int qd = 0; qd < 4; ++qd) rsv[qd] = *(LAS f32x4*)(L + L_RS + w * 128 + qd * 32 + hf * 16);
          }
          if (s == 2 || s == 5) {
            const int o = s == 2 ? 0 : 8;
            const v4u kf = {pk(aK[o] * rstd, aK[o + 1] * rstd), pk(aK[o + 2] * rstd, aK[o + 3] * rstd), pk(aK[o + 4] * rstd, aK[o + 5] * rstd), pk(aK[o + 6] * rstd, aK[o + 7] * rstd)};
            ldsw(kvl + (o ? KB : 0), kf); gst(rs, kf, l16 + (o ? KB : 0), kvo);
          }
          if (s == 8 || s == 11) {
            const int o = s == 8 ? 0 : 8, qd = o >> 2;
            const v4u vf = {pk(aV[o] * rsv[qd][0], aV[o + 1] * rsv[qd][1]), pk(aV[o + 2] * rsv[qd][2], aV[o + 3] * rsv[qd][3]),
                            pk(aV[o + 4] * rsv[qd + 1][0], aV[o + 5] * rsv[qd + 1][1]), pk(aV[o + 6] * rsv[qd + 1][2], aV[o + 7] * rsv[qd + 1][3])};
            ldsw(kvl + 2 * KB + (o ? KB : 0), vf); gst(rs, vf, l16 + 2 * KB + (o ? KB : 0), kvo);
          }
          SCHED();
        }
        const float qs = rstd * (0.25f * LOG2E);
        Qa = v4u{pk(aQ[0] * qs, aQ[1] * qs), pk(aQ[2] * qs, aQ[3] * qs), pk(aQ[4] * qs, aQ[5] * qs), pk(aQ[6] * qs, aQ[7] * qs)};
        ldsw(L + L_QB + w * KB + l16, v4u{pk(aQ[8] * qs, aQ[9] * qs), pk(aQ[10] * qs, aQ[11] * qs), pk(aQ[12] * qs, aQ[13] * qs), pk(aQ[14] * qs, aQ[15] * qs)});
        if (w == 0) STAMP(sb + 2);
        WAITV(0);
        BARL();
        if (tid == 0) set_flag(ws, fl + F_KV, me, epoch);
        if (w == 0) STAMP(sb + 3);
      }
      {
        LANE_VALS();
        Attn A; f32x16 mk;
        const bool vl = r32 < 16;
        const int vstride = vl ? 4096 : 0;
        make_mask(mk, lane, false);
        attn_chunk<true>(A, Qa, L + L_KV + l16, vl ? L + L_KV + 2 * KB + l16 : L + L_ONES, vstride, 0, w, w, mk);
        if (w == 0) {
          STAMP(sb + 4);
          if (g >= 1) {
            poll(flag_ptr(ws, fl + F_KV, me - 16), lane == 0, epoch, dead);
            const int src = act + A_KV + (me - 16) * 16 * KB;
            dma4<0, BUF_B + 16 * KB>(rs, src, 0, l16); dma4<4 * KB, BUF_B + 20 * KB>(rs, src, 0, l16);
            dma4<8 * KB, BUF_B + 24 * KB>(rs, src, 0, l16); dma4<12 * KB, BUF_B + 28 * KB>(rs, src, 0, l16);
            WAITV(0);
          }
        }
        BAR();
        if (w == 0) STAMP(sb + 5);
        if (g >= 1) {
          make_mask(mk, opv(lane), true);
          attn_chunk<false>(A, Qa, L + BUF_B + l16, vl ? L + BUF_B + 2 * KB + l16 : L + L_ONES, vstride, g >= 2 ? w + 1 : 4, 8, g >= 2 ? w : -1, mk);
        }
        gst(rs, attn_out<0>(A), l16, act + A_OT + (tta * 32 + 2 * m) * KB);
        if (w == 0) STAMP(sb + 6);
        WAITV(0);
        BAR();
        if (tid == 0) set_flag(ws, fl + F_O, me, epoch);
        if (w == 0) { STAMP(sb + 7); poll(flag_ptr(ws, fl + F_O, g * 16 + (lane & 15)), lane < 16, epoch, dead); }
        BAR();
        if (w == 0) STAMP(sb + 8);
      }
      {
        LANE_VALS();
        const int oo = ops(act + A_OT + (ttr * 32 + 16 * kh) * KB);
#pragma unroll
        for (int i = 0; i < 16; ++i) xf[i] = gldf(rs, l16, oo, i);
        SCHED();
        f32x16 keep = {}, send = {};
        lptr k0 = L + opv(BUF_D + 3 * kh * KB + l16), s0 = L + opv(BUF_D + (1 + kh) * KB + l16);
        lptr k1 = L + opv(BUF_A + 3 * kh * KB + l16), s1 = L + opv(BUF_A + (1 + kh) * KB + l16);
        constexpr int PD = 2;
        v4u wk[PD], wsd[PD];
#pragma unroll
        for (int i = 0; i < PD; ++i) { wk[i] = ldsr(k0 + i * 4 * KB); wsd[i] = ldsr(s0 + i * 4 * KB); }
#pragma unroll
        for (int s = 0; s < 16; ++s) {
          const v4u ck = wk[s % PD], cs = wsd[s % PD];
          if (s + PD < 8) { wk[s % PD] = ldsr(k0 + (s + PD) * 4 * KB); wsd[s % PD] = ldsr(s0 + (s + PD) * 4 * KB); }
          else if (s + PD < 16) { wk[s % PD] = ldsr(k1 + (s + PD - 8) * 4 * KB); wsd[s % PD] = ldsr(s1 + (s + PD - 8) * 4 * KB); }
          send = mfma(cs, xf[s], send);
          keep = mfma(ck, xf[s], keep);
          SCHED();
        }
        send_tile(L, send, w, lane);
        if (w == 0) STAMP(sb + 9);
        BARL();
        float v[16];
        finish_tile(L, keep, res, v, w, lane);
        const int so = act + A_X1T + (ttr * 32 + 2 * ftile) * KB;
        gst(rs, res[0], l16, so); gst(rs, res[1], l16 + KB, so);
        store_stats(rs, v, lane, act + A_ST1 + ttr * 4096 + (ftile >> 1) * 512 + (ftile & 1) * 8);
        if (w == 0) STAMP(sb + 10);
        WAITV(0);
        BAR();
        if (tid == 0) set_flag(ws, fl + F_X1, me, epoch);
        if (w == 0) { STAMP(sb + 11); poll(flag_ptr(ws, fl + F_X1, g * 16 + (lane & 15)), lane < 16, epoch, dead); }
        BAR();
        if (w == 0) STAMP(sb + 12);
      }
      {
        LANE_VALS();
        constexpr int WIN = 12;
        v4u st[4];
        const int sto = ops(act + A_ST1 + tta * 4096), xo = ops(act + A_X1T + tta * 32 * KB);
#pragma unroll
        for (int i = 0; i < 4; ++i) st[i] = gld(rs, hf * 2048 + r32 * 16 + i * 512, sto);
#pragma unroll
        for (int i = 0; i < WIN; ++i) xf[i] = gldf(rs, l16, xo, i);
        SCHED();
        f32x16 acc[3];
        acc[0] = f32x16{}; acc[1] = f32x16{};
        float mean, rstd, c1; v4u xe;
        lptr pbuf[4] = {L + BUF_A + l16, L + opv(BUF_B + l16), L + opv(BUF_C + l16), L + opv(BUF_D + l16)};
        {
          constexpr int PD = 2;
          v4u w0[PD], w1[PD];
          lptr p0 = pbuf[2], p1 = pbuf[1];
#pragma unroll
          for (int i = 0; i < PD; ++i) { w0[i] = ldsr(p0 + i * KB); w1[i] = ldsr(p1 + i * KB); }
#pragma unroll
          for (int s = 0; s < 33; ++s) {
            if (s + WIN < 32) xf[s + WIN] = gldf(rs, l16, xo, s + WIN);
            const v4u c0 = w0[s % PD], cc1 = w1[s % PD];
            if (s + PD < 33) { w0[s % PD] = ldsr(p0 + (s + PD) * KB); w1[s % PD] = ldsr(p1 + (s + PD) * KB); }
            const v4u xa = s < 32 ? xf[s < 32 ? s : 0] : xe;
            acc[0] = mfma(c0, xa, acc[0]);
            acc[1] = mfma(cc1, xa, acc[1]);
            if (s == 4) { finish_stats(st, mean, rstd); xe = mean_frag(mean); c1 = -LOG2E * rstd; }
            SCHED();
          }
        }
        if (w == 0) STAMP(sb + 13);
        const int ho = ops(act + A_HT + (tta * 112 + 7 * m) * KB);
        float ev[8]; v4u hw4;
        auto epi = [&](const f32x16& a, int k, int tile) {
          const int e = k >> 1;
          if (!(k & 1)) ev[e] = 1.f + __builtin_amdgcn_exp2f(a[e] * c1);
          else {
            ev[e] = (a[8 + e] * rstd) * __builtin_amdgcn_rcpf(ev[e]);
            if (e & 1) hw4[e >> 1] = pk(ev[e - 1], ev[e]);
            if (e == 7) gst(rs, hw4, l16 + (tile & 3) * KB, ho + (tile >> 2) * 4 * KB);
          }
        };
        constexpr int PQ = 4;
        v4u wq[PQ];
        BARL();
#pragma unroll
        for (int i = 0; i < PQ; ++i) wq[i] = ldsr(pbuf[3] + i * KB);
#pragma unroll
        for (int t = 2; t < 7; ++t) {
          constexpr int bufs[8] = {2, 1, 3, 0, 2, 1, 3, 0};
          lptr pc = pbuf[bufs[t]], pn = pbuf[bufs[t + 1]];
          f32x16& a = acc[t % 3];
          a = f32x16{};
#pragma unroll
          for (int s = 0; s < 33; ++s) {
            const int u = (t - 2) * 33 + s;
            const v4u cq = wq[u % PQ];
            if (s + PQ == 33 && t < 6) {
              if (t == 5) WAITV(0);
              BAR();
            }
            if (s + PQ < 33) wq[u % PQ] = ldsr(pc + (s + PQ) * KB);
            else if (t < 6) wq[u % PQ] = ldsr(pn + (s + PQ - 33) * KB);
            a = mfma(cq, s < 32 ? xf[s < 32 ? s : 0] : xe, a);
            if (t == 2) { if (s >= 1 && s < 17) epi(acc[0], s - 1, 0); if (s >= 17) epi(acc[1], s - 17, 1); }
            else if (s >= 2 && s < 18) epi(acc[(t - 1) % 3], s - 2, t - 1);
            SCHED();
          }
        }
#pragma unroll
        for (int k = 0; k < 16; ++k) epi(acc[6 % 3], k, 6);
        if (w == 0) STAMP(sb + 14);
      }
      {
        LANE_VALS();
        BAR();
        if (w == 0) STAMP(sb + 15);
        constexpr int WIN = 16;
        const int hb = ops(act + A_HT + (ttr * 112 + 56 * kh) * KB), fbo = ops(fb);
        auto hld = [&](int s) {
          const int jj = s < 40 ? s / 5 : (s - 40) / 2, t = s < 40 ? s % 5 : 5 + (s & 1);
          return gld(rs, l16 + (t & 3) * KB, hb + (((jj + fbo) & 7) * 7 + (t & 4)) * KB);
        };
#pragma unroll
        for (int i = 0; i < WIN; ++i) xf[i] = hld(i);
        SCHED();
        f32x16 keep = {}, send = {};
        constexpr int PD = 2;
        constexpr int bufs[7] = {0, 2, 1, 3, 0, 2, 1};
        const int ko = 3 * kh * KB + l16, sdo = (1 - 2 * kh) * KB;
        lptr kb[4] = {L + opv(BUF_A + ko), L + opv(BUF_B + ko), L + opv(BUF_C + ko), L + opv(BUF_D + ko)};
        lptr sp[4] = {kb[0] + sdo, kb[1] + sdo, kb[2] + sdo, kb[3] + sdo};
        v4u wk[PD], wsd[PD];
#pragma unroll
        for (int i = 0; i < PD; ++i) { wk[i] = ldsr(kb[bufs[0]] + i * 4 * KB); wsd[i] = ldsr(sp[bufs[0]] + i * 4 * KB); }
#pragma unroll
        for (int s = 0; s < 56; ++s) {
          const v4u ck = wk[s % PD], cs = wsd[s % PD];
          const int n = s + PD;
          if (n < 56 && (n & 7) == 0) {
            if (n == 8) WAITV(16);
            BAR();
            if (n == 32) {
#pragma unroll
              for (int i = 40; i < 32 - PD + WIN; ++i) xf[i % 32] = hld(i);
            }
          }
          if (s + WIN < 56 && (s + WIN < 40 || s >= 32 - PD)) xf[(s + WIN) % 32] = hld(s + WIN);
          if (n < 56) { wk[s % PD] = ldsr(kb[bufs[n >> 3]] + (n & 7) * 4 * KB); wsd[s % PD] = ldsr(sp[bufs[n >> 3]] + (n & 7) * 4 * KB); }
          send = mfma(cs, xf[s % 32], send);
          keep = mfma(ck, xf[s % 32], keep);
          SCHED();
        }
        send_tile(L, send, w, lane);
        if (w == 0) STAMP(sb + 16);
        BARL();
        float v[16];
        finish_tile(L, keep, res, v, w, lane);
        if (layer == 0) {
          const int nact = act + A_LAYER;
          const int so = nact + A_XT + (ttr * 32 + 2 * ftile) * KB;
          gst(rs, res[0], l16, so); gst(rs, res[1], l16 + KB, so);
          store_stats(rs, v, lane, nact + A_ST0 + ttr * 4096 + (ftile >> 1) * 512 + (ftile & 1) * 8);
          if (w == 0) STAMP(sb + 17);
          WAITV(0);
          BAR();
          if (tid == 0) set_flag(ws, fl + F_X2, me, epoch);
          if (w == 0) { STAMP(sb + 18); poll(flag_ptr(ws, fl + F_X2, g * 16 + (lane & 15)), lane < 16, epoch, dead); }
          BAR();
        } else {
          GAS v4u* od = (GAS v4u*)(p.out + (size_t)(ttr * 32 + r32) * 512 + ftile * 32 + hf * 8);
          od[0] = res[0]; od[2] = res[1];
          if (w == 0) STAMP(sb + 17);
        }
      }
    }
    if (b == 0 && w == 0) {
      LANE_VALS();
#pragma unroll 1
      for (int i = 0; i < 4; ++i) poll(flag_ptr(ws, F_P1, i * 64 + lane), true, epoch, dead);
      if (lane == 0) __hip_atomic_store((gu32*)(ws + WS_EPOCH), epoch, RLX_AGENT);
    }
  } else {
    const int hw = wave - 4;
    {
      LANE_VALS();
      if (hw == 0) STAMP(0);
      const int q = b;
      const bool has_unit = q < 232, first = q < 48;
      int ubase, ustep, xoff = -1;
      int ku0, ft1 = hw & 1, ufb = 0;
      const u16* src; int ld;
      {
        const int row = lane >> 2, pc = lane & 3;
        if (q < 48) {
          const int kind = q >> 4;
          const int r = q & 15, hp = 2 * (r >> 1) + (hw & 1), quarter = 2 * (r & 1) + (hw >> 1);
          ku0 = 8 * quarter; ld = 1536;
          src = p.wqkv + (size_t)(ku0 * 16 + row) * 1536 + (kind == 0 ? 512 : kind == 1 ? 1024 : 0) + 32 * hp + 8 * pc;
          const int sbase = WS_WA + hp * 99 * KB;
          ubase = sbase + (kind == 2 ? 66 + ku0 : (ku0 >> 4) * 33 + (ku0 & 15) * 2 + kind) * KB;
          ustep = kind == 2 ? KB : 2 * KB;
          xoff = sbase + (kind == 0 ? 32 : kind == 1 ? 65 : 98) * KB + (quarter >> 1) * 512 + (quarter & 1) * 8;
        } else if (q < 160) {
          const int n = (q - 48) >> 2, quarter = (q - 48) & 3, Ft = 4 * n + hw;
          ku0 = 8 * quarter; ld = 1792;
          src = (pc < 2 ? p.wg : p.wu) + (size_t)(ku0 * 16 + row) * 1792 + 16 * Ft + 8 * (pc & 1);
          ubase = WS_WC + (Ft * 33 + ku0) * KB; ustep = KB;
          xoff = WS_WC + (Ft * 33 + 32) * KB + (quarter >> 1) * 512 + (quarter & 1) * 8;
        } else if (q < 176) {
          const int r = q - 160; ufb = r >> 1;
          ku0 = 8 * (2 * (r & 1) + (hw >> 1)); ld = 512;
          src = p.wo + (size_t)(ku0 * 16 + row) * 512 + 64 * ufb + 32 * ft1 + 8 * pc;
          ubase = WS_WB + (ufb * 64 + (ku0 & 15) * 4 + (ku0 >> 4) * 2 + ft1) * KB; ustep = 4 * KB;
        } else {
          const int r = q < 232 ? q - 176 : 0; ufb = r / 7;
          ku0 = 8 * (2 * (r % 7) + (hw >> 1)); ld = 512;
          src = p.wd + (size_t)(ku0 * 16 + row) * 512 + 64 * ufb + 32 * ft1 + 8 * pc;
          ubase = WS_WD + (ufb * 224 + ft1) * KB; ustep = 0;
        }
      }
      v4u uw[8];
#pragma unroll
      for (int u = 0; u < 8; ++u) uw[u] = *(const GAS v4u*)(src + (size_t)u * 16 * ld);
#pragma unroll 1
      for (int pass = 0; pass < 2; ++pass) {
        if (has_unit && first == (pass == 0)) {
          lptr sc = L + BUF_D + hw * 8 * KB;
#pragma unroll
          for (int u = 0; u < 8; ++u) ldsw(sc + u * KB + l16, uw[u]);
          const int gi = lane >> 4, q4 = (lane & 15) >> 2, pp = lane & 3, ps = ((pp & 1) << 1) | (pp >> 1);
          lptr tp = sc + (8 * (gi >> 1) + q4) * 64 + (gi & 1) * 32 + ps * 8;
          float cs = 0.f;
          int j = ku0 / 7, t = ku0 % 7;
#pragma unroll
          for (int u = 0; u < 8; ++u) {
            const v2u lo = __builtin_bit_cast(v2u, __builtin_amdgcn_ds_read_tr16_b64_v4i16((LAS v4s*)(tp + u * KB)));
            const v2u hi = __builtin_bit_cast(v2u, __builtin_amdgcn_ds_read_tr16_b64_v4i16((LAS v4s*)(tp + u * KB + 256)));
            const v4u f = {lo.x, lo.y, hi.x, hi.y};
            const int jj = (j - ufb) & 7;
            const int dpos = ((t < 5 ? jj * 5 + t : 35 + jj * 2 + t) * 4 + (j >> 3) * 2) * KB;
            gst(rs, f, l16, ubase + (ustep ? u * ustep : dpos));
            if (++t == 7) { t = 0; ++j; }
#pragma unroll
            for (int e = 0; e < 4; ++e) { const unsigned fe = f[e]; cs = __builtin_amdgcn_fdot2_f32_bf16(__builtin_bit_cast(v2bf, fe), __builtin_bit_cast(v2bf, 0x3f803f80u), cs, false); }
          }
          if (xoff >= 0) {
            cs = xhalf_sum(cs);
            const unsigned h2 = pk(cs, cs);
            const unsigned wd = pk(cs, cs - bflo(h2));
            if (lane < 32) gst2(rs, v2u{wd, wd}, l16, xoff);
          }
        }
        if (pass == 0) {
          if (first) WAITV(0);
          BARL();
          if (tid == 256) set_flag(ws, F_P1, me, epoch);
        }
      }
      if (hw == 0) {
        STAMP(1);
        const int qq = ((lane - 16) >> 1) * 16 + (m >> 1) * 2 + (lane & 1);
        const int idx = lane < 16 ? g * 16 + lane : (((qq & 7) * 2 + (qq >> 7)) * 16 + ((qq >> 3) & 15));
        poll(flag_ptr(ws, F_P1, lane < 22 ? idx : 0), lane < 22, epoch, dead);
      }
      BAR();
    }
    const int lb = hw * 8 * KB;
    const int wa = WS_WA + m * 99 * KB + lb, wb = WS_WB + fb * 64 * KB + lb, wc = WS_WC + m * 231 * KB + lb, wdn = WS_WD + fb * 224 * KB + lb;
    const int l16h = (tid & 63) * 16;
#define DMA(EXTRA, base, G, BUF) dma_chunk<EXTRA, G, BUF>(rs, base, lb, l16h)
    DMA(true, wa, 0, BUF_A); DMA(true, wa, CH, BUF_B); DMA(true, wa, 2 * CH, BUF_C);
#pragma unroll 1
    for (int layer = 0; layer < 2; ++layer) {
      LANE_VALS();
      const int act = WS_ACT + layer * A_LAYER;
      const int fl = layer * F_PER_LAYER;
      const int sb = 2 + layer * 28;
      (void)sb;
      WAITV(16); BAR();
      if (layer == 0 && tid == 256) set_flag(ws, F_P2, me, epoch);
      if (hw == 0) STAMP(sb + 0);
      WAITV(8); BAR();
      if (layer == 0 && hw == 0) {
        int qq;
        if (lane < 2) qq = 160 + 2 * fb + lane;
        else if (lane < 9) qq = 176 + 7 * fb + (lane - 2);
        else { int n = (7 * m) / 4 + ((lane - 9) >> 2); n = n > 27 ? 27 : n; qq = 48 + 4 * n + ((lane - 9) & 3); }
        const int idx = ((qq & 7) * 2 + (qq >> 7)) * 16 + ((qq >> 3) & 15);
        poll(flag_ptr(ws, F_P2, lane < 21 ? idx : 0), lane < 21, epoch, dead);
      }
      WAITV(0); BAR();
      if (hw == 0) STAMP(sb + 1);
      DMA(false, wb, 0, BUF_D); DMA(false, wb, 32 * KB, BUF_A);
      BAR();
      DMA(true, wc, 0, BUF_C);
      {
        const int tq = 3 - hw;
        const v4u Qb = ldsr(L + L_QB + tq * KB + l16);
        Attn A; f32x16 mk;
        const bool vl = r32 >= 16;
        const int vstride = vl ? 4096 : 0;
        make_mask(mk, lane, false);
        attn_chunk<true>(A, Qb, L + L_KV + KB + l16, vl ? L + L_KV + 2 * KB + l16 : L + L_ONES, vstride, 0, tq, tq, mk);
        if (hw == 3 && g >= 2) {
          poll(flag_ptr(ws, fl + F_KV, me - 32), lane == 0, epoch, dead);
          const int s2 = act + A_KV + (me - 32) * 16 * KB;
          dma4<0, BUF_B>(rs, s2, 0, l16); dma4<4 * KB, BUF_B + 4 * KB>(rs, s2, 0, l16);
          dma4<8 * KB, BUF_B + 8 * KB>(rs, s2, 0, l16); dma4<12 * KB, BUF_B + 12 * KB>(rs, s2, 0, l16);
          WAITV(0);
        }
        if (hw == 0) STAMP(sb + 4);
        BAR();
        if (g >= 1) {
          make_mask(mk, opv(lane), true);
          attn_chunk<false>(A, Qb, L + BUF_B + KB + l16, vl ? L + BUF_B + 2 * KB + l16 : L + L_ONES, vstride, g >= 2 ? tq + 1 : 4, 8, g >= 2 ? tq : -1, mk);
        }
        gst(rs, attn_out<1>(A), l16, act + A_OT + ((4 * g + tq) * 32 + 2 * m + 1) * KB);
        if (hw == 0) STAMP(sb + 6);
        WAITV(0);
        BAR();
      }
      DMA(true, wc, CH, BUF_B);
      BAR();
      BAR();
      BAR();
      DMA(true, wc, 2 * CH, BUF_D); DMA(true, wc, 3 * CH, BUF_A);
      WAITV(16); BAR();
      WAITV(8); BAR();
      if (hw == 0) STAMP(sb + 13);
      DMA(true, wc, 4 * CH, BUF_C); DMA(true, wc, 5 * CH, BUF_B);
      WAITV(16); BAR();
      WAITV(8); BAR();
      DMA(true, wc, 6 * CH, BUF_D);
      WAITV(8); BAR();
      DMA(false, wdn, 0, BUF_A);
      WAITV(8); BAR();
      if (tid == 320) set_flag(ws, fl + F_HE, me, epoch);
      DMA(false, wdn, 32 * KB, BUF_C);
      if (hw == 0) { poll(flag_ptr(ws, fl + F_HE, g * 16 + (lane & 15)), lane < 16, epoch, dead); STAMP(sb + 14); }
      WAITV(8); BAR();
      DMA(false, wdn, 64 * KB, BUF_B); DMA(false, wdn, 96 * KB, BUF_D);
      WAITV(16); BAR();
      if (tid == 320) set_flag(ws, fl + F_HL, me, epoch);
      WAITV(8); BAR();
      DMA(false, wdn, 128 * KB, BUF_A);
      WAITV(8); BAR();
      DMA(false, wdn, 160 * KB, BUF_C);
      if (hw == 0) { poll(flag_ptr(ws, fl + F_HL, g * 16 + (lane & 15)), lane < 16, epoch, dead); STAMP(sb + 15); }
      WAITV(8); BAR();
      DMA(false, wdn, 192 * KB, BUF_B);
      WAITV(8); BAR();
      WAITV(0); BAR();
      BAR();
      if (layer == 0) {
        BAR();
        DMA(true, wa, 0, BUF_A); DMA(true, wa, CH, BUF_B); DMA(true, wa, 2 * CH, BUF_C);
        BAR();
      }
    }
  }
}
}

extern "C" void kernel_launch(void* const* d_in, const int* in_sizes, int n_in, void* d_out, int out_size, void* d_ws, size_t ws_size,
                              hipStream_t stream) {
  Params p;
  p.x = (const u16*)d_in[0]; p.wqkv = (const u16*)d_in[1]; p.wo = (const u16*)d_in[2];
  p.wg = (const u16*)d_in[4]; p.wu = (const u16*)d_in[5]; p.wd = (const u16*)d_in[6];
  p.out = (u16*)d_out; p.ws = (unsigned char*)d_ws;
  (void)hipFuncSetAttribute((const void*)fused_layers, hipFuncAttributeMaxDynamicSharedMemorySize, LDS_BYTES);
  hipLaunchKernelGGL(fused_layers, dim3(NWG), dim3(NTHR), LDS_BYTES, stream, p);
}
```

```cpp
#include <hip/hip_runtime.h>
#include <hip/hip_bf16.h>

#define PROF 0

namespace {
typedef unsigned short u16;
typedef unsigned v4u __attribute__((ext_vector_type(4)));
typedef unsigned v2u __attribute__((ext_vector_type(2)));
typedef short v4s __attribute__((ext_vector_type(4)));
typedef float f32x4 __attribute__((ext_vector_type(4)));
typedef float f32x16 __attribute__((ext_vector_type(16)));
typedef __bf16 v2bf __attribute__((ext_vector_type(2)));
typedef __bf16 v8bf __attribute__((ext_vector_type(8)));
#define LAS __attribute__((address_space(3)))
#define GAS __attribute__((address_space(1)))
#define DI __device__ __forceinline__
typedef GAS unsigned gu32;
typedef LAS unsigned char* lptr;
#define RLX_AGENT __ATOMIC_RELAXED, __HIP_MEMORY_SCOPE_AGENT

constexpr int NWG = 256, NTHR = 512;
constexpr int KB = 1024;
constexpr int FLAG_STRIDE = 256;
enum { F_P1 = 0, F_P2 = 1, F_KV = 2, F_O = 3, F_X1 = 4, F_HE = 5, F_HM = 6, F_HL = 7, F_X2 = 8, F_PER_LAYER = 7, F_KINDS = 16 };
constexpr int WS_EPOCH = 0;
constexpr int WS_FLAGS = 4 * KB;
constexpr int WS_WA = WS_FLAGS + F_KINDS * 256 * FLAG_STRIDE;
constexpr int WS_WB = WS_WA + 16 * 99 * KB;
constexpr int WS_WC = WS_WB + 8 * 64 * KB;
constexpr int WS_WD = WS_WC + 16 * 231 * KB;
constexpr int WS_ACT = WS_WD + 8 * 224 * KB;
constexpr int A_XT = 0, A_ST0 = A_XT + 2048 * KB, A_KV = A_ST0 + 256 * KB, A_OT = A_KV + 4096 * KB, A_X1T = A_OT + 2048 * KB,
              A_ST1 = A_X1T + 2048 * KB, A_HT = A_ST1 + 256 * KB, A_LAYER = A_HT + 64 * 112 * KB;
constexpr int WS_DBG = WS_ACT + 2 * A_LAYER;
constexpr int WS_BYTES = WS_DBG + 256 * KB * 2;
constexpr int CH = 33 * KB;
constexpr int BUF_A = 0, BUF_B = CH, BUF_C = 2 * CH, BUF_D = 3 * CH, SP = 4 * CH;
constexpr int L_KV = SP;
constexpr int L_QB = SP + 16 * KB;
constexpr int L_ONES = SP + 20 * KB;
constexpr int L_RS = SP + 22 * KB;
constexpr int L_DBG = SP + 23 * KB;
constexpr int LDS_BYTES = SP + 24 * KB;

constexpr float NEG_BIG = -1e30f;
constexpr float LOG2E = 1.4426950408889634f;
constexpr int AUX_SC1 = 16;

struct Params {
  const u16 *x, *wqkv, *wo, *wg, *wu, *wd;
  u16* out;
  unsigned char* ws;
};

DI f32x16 mfma(v4u a, v4u b, f32x16 c) {
  return __builtin_amdgcn_mfma_f32_32x32x16_bf16(__builtin_bit_cast(v8bf, a), __builtin_bit_cast(v8bf, b), c, 0, 0, 0);
}
DI unsigned pk(float a, float b) { v2bf v = {(__bf16)a, (__bf16)b}; return __builtin_bit_cast(unsigned, v); }
DI float bflo(unsigned w) { return __uint_as_float(w << 16); }
DI float bfhi(unsigned w) { return __uint_as_float(w & 0xffff0000u); }
DI float xhalf_sum(float v) { auto r = __builtin_amdgcn_permlane32_swap(__float_as_uint(v), __float_as_uint(v), false, false); return __uint_as_float(r[0]) + __uint_as_float(r[1]); }
DI float xhalf_max(float v) { auto r = __builtin_amdgcn_permlane32_swap(__float_as_uint(v), __float_as_uint(v), false, false); return __builtin_fmaxf(__uint_as_float(r[0]), __uint_as_float(r[1])); }
DI v4u ldsr(lptr p) { return *(LAS v4u*)p; }
DI void ldsw(lptr p, v4u v) { *(LAS v4u*)p = v; }
typedef __amdgpu_buffer_rsrc_t rsrc_t;
DI v4u gld(rsrc_t rs, int voff, int soff) { return __builtin_amdgcn_raw_buffer_load_b128(rs, voff, soff, AUX_SC1); }
DI v4u gldf(rsrc_t rs, int l16, int soff, int i) { return __builtin_amdgcn_raw_buffer_load_b128(rs, l16 + (i & 3) * KB, soff + (i >> 2) * 4 * KB, AUX_SC1); }
DI int opv(int x) { asm volatile("" : "+v"(x)); return x; }
DI int ops(int x) { asm volatile("" : "+s"(x)); return x; }
DI void gst(rsrc_t rs, v4u v, int voff, int soff) { __builtin_amdgcn_raw_buffer_store_b128(v, rs, voff, soff, AUX_SC1); }
DI void gst2(rsrc_t rs, v2u v, int voff, int soff) { __builtin_amdgcn_raw_buffer_store_b64(v, rs, voff, soff, AUX_SC1); }
#define SCHED() __builtin_amdgcn_sched_barrier(0)
#define BAR() asm volatile("s_barrier" ::: "memory")
#define BARL() asm volatile("s_waitcnt lgkmcnt(0)\n\ts_barrier" ::: "memory")
#define WAITV(n) asm volatile("s_waitcnt vmcnt(" #n ")" ::: "memory")

template <int G, int LO> DI void dma4(rsrc_t rs, int sb, int lb, int voff) {
  unsigned keep, so;
  asm volatile("s_mov_b32 %0, m0\n\ts_add_u32 m0, %5, %7\n\ts_add_u32 %1, %4, %6\n\ts_nop 2\n\t"
               "buffer_load_dwordx4 %2, %3, %1 offen sc1 lds\n\tbuffer_load_dwordx4 %2, %3, %1 offen offset:1024 sc1 lds\n\t"
               "buffer_load_dwordx4 %2, %3, %1 offen offset:2048 sc1 lds\n\tbuffer_load_dwordx4 %2, %3, %1 offen offset:3072 sc1 lds\n\t"
               "s_mov_b32 m0, %0" : "=&s"(keep), "=&s"(so) : "v"(voff), "s"(rs), "s"(sb), "s"(lb), "i"(G), "i"(LO) : "memory", "scc");
}
template <int G, int LO> DI void dma1(rsrc_t rs, int sb, int lb, int voff) {
  unsigned keep, so;
  asm volatile("s_mov_b32 %0, m0\n\ts_add_u32 m0, %5, %7\n\ts_add_u32 %1, %4, %6\n\ts_nop 2\n\t"
               "buffer_load_dwordx4 %2, %3, %1 offen sc1 lds\n\t"
               "s_mov_b32 m0, %0" : "=&s"(keep), "=&s"(so) : "v"(voff), "s"(rs), "s"(sb), "s"(lb), "i"(G), "i"(LO) : "memory", "scc");
}
template <bool EXTRA, int G, int LBUF> DI void dma_chunk(rsrc_t rs, int sb, int lb, int voff) {
  dma4<G, LBUF>(rs, sb, lb, voff);
  dma4<G + 4 * KB, LBUF + 4 * KB>(rs, sb, lb, voff);
  if (EXTRA && lb == 0) dma1<G + 32 * KB, LBUF + 32 * KB>(rs, sb, lb, voff);
}

#define MAPV 1
__host__ __device__ inline int wg_index(int b) {
  if (MAPV == 0) return ((b & 7) * 2 + (b >> 7)) * 16 + ((b >> 3) & 15);
  return (4 * ((b & 7) >> 1) + (b >> 6)) * 16 + 8 * (b & 1) + ((b >> 3) & 7);
}
DI gu32* flag_ptr(unsigned char* ws, int kind, int idx) { return (gu32*)(ws + WS_FLAGS + (size_t)(kind * 256 + idx) * FLAG_STRIDE); }
DI void set_flag(unsigned char* ws, int kind, int idx, unsigned epoch) { __hip_atomic_store(flag_ptr(ws, kind, idx), epoch, RLX_AGENT); }
DI void poll(gu32* f, bool active, unsigned epoch, bool& dead) {
  if (!dead) {
    const unsigned long long t0 = __builtin_amdgcn_s_memrealtime();
    for (unsigned spins = 1;; ++spins) {
      unsigned v = epoch;
      if (active) v = __hip_atomic_load(f, RLX_AGENT);
      if (__all(v == epoch)) break;
      if ((spins & 63u) == 0u && __builtin_amdgcn_s_memrealtime() - t0 > 2000000ull) { dead = true; break; }
    }
  }
  __builtin_amdgcn_fence(__ATOMIC_ACQUIRE, "wavefront");
}

#define STAMP(i) do {} while (0)

DI void finish_stats(const v4u (&st)[4], float& mean, float& rstd) {
  float s = 0.f, q = 0.f;
#pragma unroll
  for (int i = 0; i < 4; ++i) { s += __uint_as_float(st[i].x) + __uint_as_float(st[i].z); q += __uint_as_float(st[i].y) + __uint_as_float(st[i].w); }
  s = xhalf_sum(s); q = xhalf_sum(q);
  mean = s * (1.f / 512.f);
  const float var = __builtin_fmaxf(q * (1.f / 512.f) - mean * mean, 0.f);
  rstd = __builtin_amdgcn_rsqf(var + 1e-5f);
}
DI v4u mean_frag(float mean) {
  const float nm = -mean;
  const unsigned hh = pk(nm, nm);
  const float lo = nm - bflo(hh);
  const unsigned ll = pk(lo, lo);
  return v4u{hh, ll, hh, ll};
}
DI void store_stats(rsrc_t rs, const float (&v)[16], int lane, int soff) {
  float s = 0.f, q = 0.f;
#pragma unroll
  for (int i = 0; i < 16; ++i) { s += v[i]; q = __builtin_fmaf(v[i], v[i], q); }
  s = xhalf_sum(s); q = xhalf_sum(q);
  if (lane < 32) gst2(rs, v2u{__float_as_uint(s), __float_as_uint(q)}, lane * 16, soff);
}

struct Attn { f32x16 O; float m; };
DI float max16(const f32x16& s, float m) {
#pragma unroll
  for (int i = 0; i < 16; i += 2) m = __builtin_fmaxf(__builtin_fmaxf(m, s[i]), s[i + 1]);
  return m;
}
DI f32x16 pv(const f32x16& s, v4u v0, v4u v1, f32x16 O) {
  float p[16];
#pragma unroll
  for (int i = 0; i < 16; ++i) p[i] = __builtin_amdgcn_exp2f(s[i]);
  const v4u p0 = {pk(p[0], p[1]), pk(p[2], p[3]), pk(p[4], p[5]), pk(p[6], p[7])};
  const v4u p1 = {pk(p[8], p[9]), pk(p[10], p[11]), pk(p[12], p[13]), pk(p[14], p[15])};
  O = mfma(v0, p0, O);
  O = mfma(v1, p1, O);
  return O;
}
template <bool FIRST>
DI void attn_chunk(Attn& A, v4u Q, lptr kp, lptr vp, int vstride, int t0, int t1, int tm, const f32x16& mask) {
  const f32x16 zero = {};
  float mx = FIRST ? NEG_BIG : A.m;
  f32x16 sm = zero;
  if (tm >= 0) { sm = mfma(ldsr(kp + tm * 4096), Q, mask); mx = max16(sm, mx); }
  for (int t = t0; t < t1; ++t) { const f32x16 s = mfma(ldsr(kp + t * 4096), Q, zero); mx = max16(s, mx); }
  mx = xhalf_max(mx);
  if (FIRST) A.O = zero;
  else { const float al = __builtin_amdgcn_exp2f(A.m - mx); A.O *= al; }
  A.m = mx;
  if (tm >= 0) { A.O = pv(sm - mx, ldsr(vp + tm * vstride), ldsr(vp + tm * vstride + 1024), A.O); }
  f32x16 nm;
#pragma unroll
  for (int i = 0; i < 16; ++i) nm[i] = -mx;
  for (int t = t0; t < t1; ++t) {
    const f32x16 s = mfma(ldsr(kp + t * 4096), Q, nm);
    A.O = pv(s, ldsr(vp + t * vstride), ldsr(vp + t * vstride + 1024), A.O);
  }
}
DI float opf(float x) { asm volatile("" : "+v"(x)); return x; }
struct Fast { f32x16 O, nm; };
DI void fast_diag(Fast& F, v4u Q, lptr kp, lptr vp, const f32x16& mask) {
  const v4u k = ldsr(kp), v0 = ldsr(vp), v1 = ldsr(vp + 1024);
  const f32x16 s = mfma(k, Q, mask);
  float m0 = __builtin_fmaxf(s[0], s[1]), m1 = __builtin_fmaxf(s[2], s[3]);
#pragma unroll
  for (int i = 4; i < 16; i += 4) { m0 = __builtin_fmaxf(__builtin_fmaxf(m0, s[i]), s[i + 1]); m1 = __builtin_fmaxf(__builtin_fmaxf(m1, s[i + 2]), s[i + 3]); }
  const float mx = xhalf_max(__builtin_fmaxf(m0, m1));
  f32x16 d;
#pragma unroll
  for (int i = 0; i < 16; ++i) { d[i] = opf(s[i] - mx); F.nm[i] = -mx; }
  F.O = pv(d, v0, v1, f32x16{});
}
template <class Hook>
DI void fast_run(Fast& F, v4u Q, lptr kp, lptr vp, int vstride, int t0, int t1, const f32x16& c0, Hook hook) {
  lptr kq = kp + t0 * 4096, vq = vp + t0 * vstride;
  v4u k = ldsr(kq), va0 = ldsr(vq), va1 = ldsr(vq + 1024), vb0, vb1;
  f32x16 sa = mfma(k, Q, c0), sb;
  k = ldsr(kq + 4096);
  int n = t1 - t0;
#pragma unroll 1
  for (; n >= 2; n -= 2) {
    sb = mfma(k, Q, F.nm);
    vq += vstride; vb0 = ldsr(vq); vb1 = ldsr(vq + 1024); k = ldsr(kq + 2 * 4096);
    hook();
    SCHED();
    F.O = pv(sa, va0, va1, F.O);
    SCHED();
    sa = mfma(k, Q, F.nm);
    vq += vstride; va0 = ldsr(vq); va1 = ldsr(vq + 1024); kq += 2 * 4096; k = ldsr(kq + 4096);
    hook();
    SCHED();
    F.O = pv(sb, vb0, vb1, F.O);
    SCHED();
  }
  if (n) { hook(); F.O = pv(sa, va0, va1, F.O); }
}
DI void make_mask(f32x16& mk, int lane, bool old) {
  const int d = (lane & 31) - 4 * (lane >> 5);
#pragma unroll
  for (int i = 0; i < 16; ++i) { const bool le = ((i & 3) + 8 * (i >> 2)) <= d; mk[i] = (le != old) ? 0.f : NEG_BIG; }
}
template <int HD> DI v4u attn_out(const f32x16& O) {
  const float r = __builtin_amdgcn_rcpf(O[HD ? 0 : 8]);
  const int o = HD ? 8 : 0;
  return v4u{pk(O[o] * r, O[o + 1] * r), pk(O[o + 2] * r, O[o + 3] * r), pk(O[o + 4] * r, O[o + 5] * r), pk(O[o + 6] * r, O[o + 7] * r)};
}
template <int HD, class Mid, class Hook>
DI v4u attn_item(lptr L, v4u Q, int lane, int tq, int g, Mid mid, Hook hook) {
  const int l16 = lane * 16;
  const bool vl = ((lane >> 4) & 1) == HD;
  const int vstride = vl ? 4096 : 0;
  lptr kl = L + L_KV + HD * KB + l16, vloc = vl ? L + L_KV + 2 * KB + l16 : L + L_ONES;
  lptr kr = L + BUF_B + HD * KB + l16, vrem = vl ? L + BUF_B + 2 * KB + l16 : L + L_ONES;
  Fast F; f32x16 mk;
  make_mask(mk, lane, false);
  fast_diag(F, Q, kl + tq * 4096, vloc + tq * vstride, mk);
  if (tq > 0) fast_run(F, Q, kl, vloc, vstride, 0, tq, F.nm, []() {});
  f32x16 c0;
#pragma unroll
  for (int i = 0; i < 16; ++i) c0[i] = opf(g >= 2 ? __builtin_fminf(F.nm[0], NEG_BIG - mk[i] * 2.f) : F.nm[0]);
  mid();
  if (g >= 1) fast_run(F, Q, kr, vrem, vstride, g >= 2 ? tq : 4, 8, c0, hook);
  if (__any(!(F.O[HD ? 0 : 8] <= 1.8446744e19f))) {
    Attn A;
    make_mask(mk, opv(lane), false);
    attn_chunk<true>(A, Q, kl, vloc, vstride, 0, tq, tq, mk);
    if (g >= 1) { make_mask(mk, opv(lane), true); attn_chunk<false>(A, Q, kr, vrem, vstride, g >= 2 ? tq + 1 : 4, 8, g >= 2 ? tq : -1, mk); }
    F.O = A.O;
  }
  return attn_out<HD>(F.O);
}

DI void finish_tile(lptr L, const f32x16& keep, v4u (&res)[2], float (&v)[16], int w, int lane) {
  lptr xp = L + L_KV + (w ^ 2) * 4096 + lane * 16;
#pragma unroll
  for (int qd = 0; qd < 4; ++qd) {
    const f32x4 r = *(LAS f32x4*)(xp + qd * 1024);
#pragma unroll
    for (int j = 0; j < 4; ++j) v[4 * qd + j] = keep[4 * qd + j] + r[j];
  }
#pragma unroll
  for (int e = 0; e < 2; ++e)
#pragma unroll
    for (int j = 0; j < 4; ++j) { v[8 * e + 2 * j] += bflo(res[e][j]); v[8 * e + 2 * j + 1] += bfhi(res[e][j]); }
#pragma unroll
  for (int e = 0; e < 2; ++e) res[e] = v4u{pk(v[8 * e], v[8 * e + 1]), pk(v[8 * e + 2], v[8 * e + 3]), pk(v[8 * e + 4], v[8 * e + 5]), pk(v[8 * e + 6], v[8 * e + 7])};
}
DI void send_tile(lptr L, const f32x16& send, int w, int lane) {
  lptr xp = L + L_KV + w * 4096 + lane * 16;
#pragma unroll
  for (int qd = 0; qd < 4; ++qd) *(LAS f32x4*)(xp + qd * 1024) = f32x4{send[4 * qd], send[4 * qd + 1], send[4 * qd + 2], send[4 * qd + 3]};
}

__global__ void __launch_bounds__(NTHR, 2) fused_layers(Params p) {
  extern __shared__ __attribute__((aligned(16))) unsigned char smem[];
  lptr L = (lptr)smem;
  const int tid = threadIdx.x, wave = __builtin_amdgcn_readfirstlane(tid >> 6);
  const int b = blockIdx.x, me = wg_index(b), g = me >> 4, m = me & 15;
  unsigned char* ws = p.ws;
  const rsrc_t rs = __builtin_amdgcn_make_buffer_rsrc(ws, 0, WS_BYTES, 0x00020000);
  const unsigned epoch = __builtin_amdgcn_readfirstlane(__hip_atomic_load((gu32*)(ws + WS_EPOCH), RLX_AGENT)) + 1u;
  asm volatile("" :: "s"(epoch));
  const int th = m >> 3, fb = m & 7;
  bool dead = false;
#define LANE_VALS() const int lane = opv(tid) & 63, r32 = lane & 31, hf = lane >> 5, l16 = lane * 16; (void)r32; (void)hf; (void)l16
  if (tid < 8) *(LAS unsigned*)(L + L_ONES + (tid >> 2) * 1024 + (tid & 3) * 4) = 0x3f803f80u;

  if (wave < 4) {
    const int w = wave;
    const int ttr = 4 * g + 2 * th + (w & 1), kh = w >> 1, ftile = 2 * fb + kh;
    const int tta = 4 * g + w;
    v4u res[2];
    {
      LANE_VALS();
      if (w == 0) STAMP(0);
      const GAS v4u* xs = (const GAS v4u*)(p.x + (size_t)(ttr * 32 + r32) * 512 + ftile * 32 + hf * 8);
      res[0] = xs[0]; res[1] = xs[2];
      const int so = WS_ACT + A_XT + (ttr * 32 + 2 * ftile) * KB;
      gst(rs, res[0], l16, so); gst(rs, res[1], l16 + KB, so);
      float v[16];
#pragma unroll
      for (int e = 0; e < 2; ++e)
#pragma unroll
        for (int j = 0; j < 4; ++j) { v[8 * e + 2 * j] = bflo(res[e][j]); v[8 * e + 2 * j + 1] = bfhi(res[e][j]); }
      store_stats(rs, v, lane, WS_ACT + A_ST0 + ttr * 4096 + (ftile >> 1) * 512 + (ftile & 1) * 8);
      WAITV(0);
      BAR();
      if (w == 0) STAMP(1);
      BAR();
    }
#pragma unroll 1
    for (int layer = 0; layer < 2; ++layer) {
      const int act = WS_ACT + layer * A_LAYER;
      const int fl = layer * F_PER_LAYER;
      const int sb = 2 + layer * 28;
      (void)sb;
      v4u xf[32];
      v4u Qa;
      {
        LANE_VALS();
        if (w == 0) STAMP(sb + 0);
        constexpr int WIN = 12;
        v4u st[4];
        const int sto = ops(act + A_ST0 + tta * 4096), xo = ops(act + A_XT + tta * 32 * KB);
#pragma unroll
        for (int i = 0; i < 4; ++i) st[i] = gld(rs, hf * 2048 + r32 * 16 + i * 512, sto);
#pragma unroll
        for (int i = 0; i < WIN; ++i) xf[i] = gldf(rs, l16, xo, i);
        SCHED();
        BAR();
        f32x16 aK = {}, aV = {};
        constexpr int PD = 3;
        v4u wk[PD], wv[PD];
        lptr pa = L + BUF_A + l16, pb = L + opv(BUF_B + l16);
#pragma unroll
        for (int i = 0; i < PD; ++i) { wk[i] = ldsr(pa + 2 * i * KB); wv[i] = ldsr(pa + (2 * i + 1) * KB); }
        float mean, rstd; v4u xe, wex;
#pragma unroll
        for (int s = 0; s < 32; ++s) {
          if (s + WIN < 32) xf[s + WIN] = gldf(rs, l16, xo, s + WIN);
          const v4u ck = wk[s % PD], cv = wv[s % PD];
          if (s + PD == 16) { wex = ldsr(pa + 32 * KB); BAR(); }
          if (s + PD < 16) { wk[s % PD] = ldsr(pa + 2 * (s + PD) * KB); wv[s % PD] = ldsr(pa + (2 * (s + PD) + 1) * KB); }
          else if (s + PD < 32) { wk[s % PD] = ldsr(pb + 2 * (s + PD - 16) * KB); wv[s % PD] = ldsr(pb + (2 * (s + PD - 16) + 1) * KB); }
          aK = mfma(ck, xf[s], aK);
          aV = mfma(xf[s], cv, aV);
          if (s == 4) { finish_stats(st, mean, rstd); xe = mean_frag(mean); if (lane < 32) *(LAS float*)(L + L_RS + w * 128 + lane * 4) = rstd; }
          if (s == 17) { aK = mfma(wex, xe, aK); wex = ldsr(pb + 32 * KB); }
          if (s == 22) aV = mfma(xe, wex, aV);
          SCHED();
        }
        if (w == 0) STAMP(sb + 1);
        BAR();
        lptr pc = L + opv(BUF_C + l16);
        constexpr int PQ = 4;
        v4u wq[PQ];
#pragma unroll
        for (int i = 0; i < PQ; ++i) wq[i] = ldsr(pc + i * KB);
        f32x4 rsv[4];
#pragma unroll
        for (int qd = 0; qd < 4; ++qd) rsv[qd] = *(LAS f32x4*)(L + L_RS + w * 128 + qd * 32 + hf * 16);
        f32x16 aQ = {};
        lptr kvl = L + opv(L_KV + w * 4096 + l16);
        const int kvo = ops(act + A_KV + (me * 4 + w) * 4096);
        v4u kvf;
#pragma unroll
        for (int s = 0; s < 33; ++s) {
          const v4u cq = wq[s % PQ];
          if (s + PQ < 33) wq[s % PQ] = ldsr(pc + (s + PQ) * KB);
          aQ = mfma(cq, s < 32 ? xf[s < 32 ? s : 0] : xe, aQ);
          if (s < 8) {
            const int f = s >> 1, o = (f & 1) * 8 + (s & 1) * 4;
            if (f < 2) { kvf[(s & 1) * 2] = pk(opf(aK[o] * rstd), opf(aK[o + 1] * rstd)); kvf[(s & 1) * 2 + 1] = pk(opf(aK[o + 2] * rstd), opf(aK[o + 3] * rstd)); }
            else { const f32x4 r4 = rsv[o >> 2]; kvf[(s & 1) * 2] = pk(opf(aV[o] * r4[0]), opf(aV[o + 1] * r4[1])); kvf[(s & 1) * 2 + 1] = pk(opf(aV[o + 2] * r4[2]), opf(aV[o + 3] * r4[3])); }
            if (s & 1) { gst(rs, kvf, l16 + f * KB, kvo); ldsw(kvl + f * KB, kvf); }
          }
          SCHED();
        }
        const float qs = rstd * (0.25f * LOG2E);
        ldsw(L + L_QB + w * KB + l16, v4u{pk(aQ[8] * qs, aQ[9] * qs), pk(aQ[10] * qs, aQ[11] * qs), pk(aQ[12] * qs, aQ[13] * qs), pk(aQ[14] * qs, aQ[15] * qs)});
        Qa = v4u{pk(aQ[0] * qs, aQ[1] * qs), pk(aQ[2] * qs, aQ[3] * qs), pk(aQ[4] * qs, aQ[5] * qs), pk(aQ[6] * qs, aQ[7] * qs)};
        if (w == 0) STAMP(sb + 2);
        WAITV(0);
        BARL();
        if (tid == 0) set_flag(ws, fl + F_KV, me, epoch);
        if (w == 0) STAMP(sb + 3);
      }
      {
        LANE_VALS();
        const v4u of = attn_item<0>(L, Qa, lane, w, g, [&]() {
          if (w == 0) {
            STAMP(sb + 4);
            if (g >= 1) {
              poll(flag_ptr(ws, fl + F_KV, me - 16), lane == 0, epoch, dead);
              const int src = act + A_KV + (me - 16) * 16 * KB;
              dma4<0, BUF_B + 16 * KB>(rs, src, 0, l16); dma4<4 * KB, BUF_B + 20 * KB>(rs, src, 0, l16);
              dma4<8 * KB, BUF_B + 24 * KB>(rs, src, 0, l16); dma4<12 * KB, BUF_B + 28 * KB>(rs, src, 0, l16);
              WAITV(0);
            }
          }
          BAR();
          if (w == 0) STAMP(sb + 5);
        }, []() {});
        gst(rs, of, l16, act + A_OT + (tta * 32 + 2 * m) * KB);
        if (w == 0) STAMP(sb + 6);
        WAITV(0);
        BAR();
        if (tid == 0) set_flag(ws, fl + F_O, me, epoch);
        if (w == 0) { STAMP(sb + 7); poll(flag_ptr(ws, fl + F_O, g * 16 + (lane & 15)), lane < 16, epoch, dead); }
        BAR();
        if (w == 0) STAMP(sb + 8);
      }
      {
        LANE_VALS();
        const int oo = ops(act + A_OT + (ttr * 32 + 16 * kh) * KB);
#pragma unroll
        for (int i = 0; i < 16; ++i) xf[i] = gldf(rs, l16, oo, i);
        SCHED();
        f32x16 keep = {}, send = {};
        lptr k0 = L + opv(BUF_D + 3 * kh * KB + l16), s0 = L + opv(BUF_D + (1 + kh) * KB + l16);
        lptr k1 = L + opv(BUF_A + 3 * kh * KB + l16), s1 = L + opv(BUF_A + (1 + kh) * KB + l16);
        constexpr int PD = 3;
        v4u wk[PD], wsd[PD];
#pragma unroll
        for (int i = 0; i < PD; ++i) { wk[i] = ldsr(k0 + i * 4 * KB); wsd[i] = ldsr(s0 + i * 4 * KB); }
#pragma unroll
        for (int s = 0; s < 16; ++s) {
          const v4u ck = wk[s % PD], cs = wsd[s % PD];
          if (s + PD < 8) { wk[s % PD] = ldsr(k0 + (s + PD) * 4 * KB); wsd[s % PD] = ldsr(s0 + (s + PD) * 4 * KB); }
          else if (s + PD < 16) { wk[s % PD] = ldsr(k1 + (s + PD - 8) * 4 * KB); wsd[s % PD] = ldsr(s1 + (s + PD - 8) * 4 * KB); }
          send = mfma(cs, xf[s], send);
          keep = mfma(ck, xf[s], keep);
          SCHED();
        }
        send_tile(L, send, w, lane);
        if (w == 0) STAMP(sb + 9);
        BARL();
        float v[16];
        finish_tile(L, keep, res, v, w, lane);
        const int so = act + A_X1T + (ttr * 32 + 2 * ftile) * KB;
        gst(rs, res[0], l16, so); gst(rs, res[1], l16 + KB, so);
        store_stats(rs, v, lane, act + A_ST1 + ttr * 4096 + (ftile >> 1) * 512 + (ftile & 1) * 8);
        if (w == 0) STAMP(sb + 10);
        WAITV(0);
        BAR();
        if (tid == 0) set_flag(ws, fl + F_X1, me, epoch);
        if (w == 0) { STAMP(sb + 11); poll(flag_ptr(ws, fl + F_X1, g * 16 + (lane & 15)), lane < 16, epoch, dead); }
        BAR();
        if (w == 0) STAMP(sb + 12);
      }
      {
        LANE_VALS();
        constexpr int WIN = 12;
        v4u st[4];
        const int sto = ops(act + A_ST1 + tta * 4096), xo = ops(act + A_X1T + tta * 32 * KB);
#pragma unroll
        for (int i = 0; i < 4; ++i) st[i] = gld(rs, hf * 2048 + r32 * 16 + i * 512, sto);
#pragma unroll
        for (int i = 0; i < WIN; ++i) xf[i] = gldf(rs, l16, xo, i);
        SCHED();
        f32x16 acc[3];
        acc[0] = f32x16{}; acc[1] = f32x16{};
        float mean, rstd, c1; v4u xe;
        lptr pbuf[4] = {L + BUF_A + l16, L + opv(BUF_B + l16), L + opv(BUF_C + l16), L + opv(BUF_D + l16)};
        {
          constexpr int PD = 3;
          v4u w0[PD], w1[PD];
          lptr p0 = pbuf[2], p1 = pbuf[1];
#pragma unroll
          for (int i = 0; i < PD; ++i) { w0[i] = ldsr(p0 + i * KB); w1[i] = ldsr(p1 + i * KB); }
#pragma unroll
          for (int s = 0; s < 33; ++s) {
            if (s + WIN < 32) xf[s + WIN] = gldf(rs, l16, xo, s + WIN);
            const v4u c0 = w0[s % PD], cc1 = w1[s % PD];
            if (s + PD < 33) { w0[s % PD] = ldsr(p0 + (s + PD) * KB); w1[s % PD] = ldsr(p1 + (s + PD) * KB); }
            const v4u xa = s < 32 ? xf[s < 32 ? s : 0] : xe;
            acc[0] = mfma(c0, xa, acc[0]);
            acc[1] = mfma(cc1, xa, acc[1]);
            if (s == 4) { finish_stats(st, mean, rstd); xe = mean_frag(mean); c1 = -LOG2E * rstd; }
            SCHED();
          }
        }
        if (w == 0) STAMP(sb + 13);
        const int ho = ops(act + A_HT + (tta * 112 + 7 * m) * KB);
        float ev[8]; v4u hw4;
        auto epi = [&](const f32x16& a, int k, int tile) {
          const int e = k >> 1;
          if (!(k & 1)) ev[e] = 1.f + __builtin_amdgcn_exp2f(a[e] * c1);
          else {
            ev[e] = opf(opf(a[8 + e] * rstd) * __builtin_amdgcn_rcpf(ev[e]));
            if (e & 1) hw4[e >> 1] = pk(ev[e - 1], ev[e]);
            if (e == 7) gst(rs, hw4, l16 + (tile & 3) * KB, ho + (tile >> 2) * 4 * KB);
          }
        };
        constexpr int DWIN = 16;
        v4u hfr[32];
        const int hb = ops(act + A_HT + (ttr * 112 + 56 * kh) * KB), fbo = ops(fb);
        auto hld = [&](int s) {
          const int jj = s < 32 ? s >> 2 : s < 48 ? (s - 32) >> 1 : s - 48, t = s < 32 ? s & 3 : s < 48 ? 4 + (s & 1) : 6;
          return gld(rs, l16 + (t & 3) * KB, hb + (((jj + fbo) & 7) * 7 + (t & 4)) * KB);
        };
        constexpr int PQ = 4;
        v4u wq[PQ];
        BARL();
#pragma unroll
        for (int i = 0; i < PQ; ++i) wq[i] = ldsr(pbuf[3] + i * KB);
#pragma unroll
        for (int t = 2; t < 7; ++t) {
          constexpr int bufs[8] = {2, 1, 3, 0, 2, 1, 3, 0};
          lptr pc = pbuf[bufs[t]], pn = pbuf[bufs[t + 1]];
          f32x16& a = acc[t % 3];
          a = f32x16{};
#pragma unroll
          for (int s = 0; s < 33; ++s) {
            const int u = (t - 2) * 33 + s;
            const v4u cq = wq[u % PQ];
            if (s + PQ == 33 && t < 6) BAR();
            if (t == 5 && s == 15) { WAITV(0); BAR(); }
            if (t == 6 && s == 17) BAR();
            if (s + PQ < 33) wq[u % PQ] = ldsr(pc + (s + PQ) * KB);
            else if (t < 6) wq[u % PQ] = ldsr(pn + (s + PQ - 33) * KB);
            a = mfma(cq, s < 32 ? xf[s < 32 ? s : 0] : xe, a);
            if (t == 2) { if (s >= 1 && s < 17) epi(acc[0], s - 1, 0); if (s >= 17) epi(acc[1], s - 17, 1); }
            else if (s >= 1 && s < 17) epi(acc[(t - 1) % 3], s - 1, t - 1);
            if (t == 6 && s >= 17) hfr[s - 17] = hld(s - 17);
            SCHED();
          }
        }
        if (w == 0) STAMP(sb + 14);
        BAR();
        f32x16 keep = {}, send = {};
        constexpr int PD = 3;
        constexpr int dbufs[7] = {0, 2, 1, 3, 0, 2, 1};
        const int ko = 3 * kh * KB, sdo = (1 - 2 * kh) * KB;
        lptr kb[4] = {pbuf[0] + ko, pbuf[1] + ko, pbuf[2] + ko, pbuf[3] + ko};
        lptr sp[4] = {kb[0] + sdo, kb[1] + sdo, kb[2] + sdo, kb[3] + sdo};
        v4u wk[PD], wsd[PD];
#pragma unroll
        for (int i = 0; i < PD; ++i) { wk[i] = ldsr(kb[dbufs[0]] + i * 4 * KB); wsd[i] = ldsr(sp[dbufs[0]] + i * 4 * KB); }
#pragma unroll
        for (int s = 0; s < 56; ++s) {
          const v4u ck = wk[s % PD], cs = wsd[s % PD];
          const int n = s + PD;
          if (n < 56 && (n & 7) == 0) {
            if (n == 8) WAITV(16);
            if (n == 24) WAITV(8);
            BAR();
            if (n == 24) {
#pragma unroll
              for (int i = 32; i < s + DWIN; ++i) hfr[i % 32] = hld(i);
            }
            if (n == 40) {
#pragma unroll
              for (int i = 48; i < s + DWIN; ++i) hfr[i % 32] = hld(i);
            }
          }
          const int ld = s + DWIN;
          if (ld < 32 || (ld < 48 && n >= 24) || (ld < 56 && n >= 40)) hfr[ld % 32] = hld(ld);
          if (n < 56) { wk[s % PD] = ldsr(kb[dbufs[n >> 3]] + (n & 7) * 4 * KB); wsd[s % PD] = ldsr(sp[dbufs[n >> 3]] + (n & 7) * 4 * KB); }
          send = mfma(cs, hfr[s % 32], send);
          keep = mfma(ck, hfr[s % 32], keep);
          if (s < 8) { epi(acc[6 % 3], 2 * s, 6); epi(acc[6 % 3], 2 * s + 1, 6); }
          SCHED();
        }
        send_tile(L, send, w, lane);
        if (w == 0) STAMP(sb + 16);
        BARL();
        float v[16];
        finish_tile(L, keep, res, v, w, lane);
        if (layer == 0) {
          const int nact = act + A_LAYER;
          const int so = nact + A_XT + (ttr * 32 + 2 * ftile) * KB;
          gst(rs, res[0], l16, so); gst(rs, res[1], l16 + KB, so);
          store_stats(rs, v, lane, nact + A_ST0 + ttr * 4096 + (ftile >> 1) * 512 + (ftile & 1) * 8);
          if (w == 0) STAMP(sb + 17);
          WAITV(0);
          BAR();
          if (tid == 0) set_flag(ws, fl + F_X2, me, epoch);
          if (w == 0) { STAMP(sb + 18); poll(flag_ptr(ws, fl + F_X2, g * 16 + (lane & 15)), lane < 16, epoch, dead); }
          BAR();
        } else {
          GAS v4u* od = (GAS v4u*)(p.out + (size_t)(ttr * 32 + r32) * 512 + ftile * 32 + hf * 8);
          od[0] = res[0]; od[2] = res[1];
          if (w == 0) STAMP(sb + 17);
        }
      }
    }
    if (b == 0 && w == 0) {
      LANE_VALS();
#pragma unroll 1
      for (int i = 0; i < 4; ++i) poll(flag_ptr(ws, F_P1, i * 64 + lane), true, epoch, dead);
      if (lane == 0) __hip_atomic_store((gu32*)(ws + WS_EPOCH), epoch, RLX_AGENT);
    }
  } else {
    const int hw = wave - 4;
    {
      LANE_VALS();
      if (hw == 0) STAMP(0);
      const int q = b;
      const bool has_unit = q < 232, first = q < 48;
      int ubase, ustep, xoff = -1;
      int ku0, ft1 = hw & 1, ufb = 0;
      const u16* src; int ld;
      {
        const int row = lane >> 2, pc = lane & 3;
        if (q < 48) {
          const int kind = q >> 4;
          const int r = q & 15, hp = 2 * (r >> 1) + (hw & 1), quarter = 2 * (r & 1) + (hw >> 1);
          ku0 = 8 * quarter; ld = 1536;
          src = p.wqkv + (size_t)(ku0 * 16 + row) * 1536 + (kind == 0 ? 512 : kind == 1 ? 1024 : 0) + 32 * hp + 8 * pc;
          const int sbase = WS_WA + hp * 99 * KB;
          ubase = sbase + (kind == 2 ? 66 + ku0 : (ku0 >> 4) * 33 + (ku0 & 15) * 2 + kind) * KB;
          ustep = kind == 2 ? KB : 2 * KB;
          xoff = sbase + (kind == 0 ? 32 : kind == 1 ? 65 : 98) * KB + (quarter >> 1) * 512 + (quarter & 1) * 8;
        } else if (q < 160) {
          const int n = (q - 48) >> 2, quarter = (q - 48) & 3, Ft = 4 * n + hw;
          ku0 = 8 * quarter; ld = 1792;
          src = (pc < 2 ? p.wg : p.wu) + (size_t)(ku0 * 16 + row) * 1792 + 16 * Ft + 8 * (pc & 1);
          ubase = WS_WC + (Ft * 33 + ku0) * KB; ustep = KB;
          xoff = WS_WC + (Ft * 33 + 32) * KB + (quarter >> 1) * 512 + (quarter & 1) * 8;
        } else if (q < 176) {
          const int r = q - 160; ufb = r >> 1;
          ku0 = 8 * (2 * (r & 1) + (hw >> 1)); ld = 512;
          src = p.wo + (size_t)(ku0 * 16 + row) * 512 + 64 * ufb + 32 * ft1 + 8 * pc;
          ubase = WS_WB + (ufb * 64 + (ku0 & 15) * 4 + (ku0 >> 4) * 2 + ft1) * KB; ustep = 4 * KB;
        } else {
          const int r = q < 232 ? q - 176 : 0; ufb = r / 7;
          ku0 = 8 * (2 * (r % 7) + (hw >> 1)); ld = 512;
          src = p.wd + (size_t)(ku0 * 16 + row) * 512 + 64 * ufb + 32 * ft1 + 8 * pc;
          ubase = WS_WD + (ufb * 224 + ft1) * KB; ustep = 0;
        }
      }
      v4u uw[8];
#pragma unroll
      for (int u = 0; u < 8; ++u) uw[u] = *(const GAS v4u*)(src + (size_t)u * 16 * ld);
#pragma unroll 1
      for (int pass = 0; pass < 2; ++pass) {
        if (has_unit && first == (pass == 0)) {
          lptr sc = L + BUF_D + hw * 8 * KB;
#pragma unroll
          for (int u = 0; u < 8; ++u) ldsw(sc + u * KB + l16, uw[u]);
          const int gi = lane >> 4, q4 = (lane & 15) >> 2, pp = lane & 3, ps = ((pp & 1) << 1) | (pp >> 1);
          lptr tp = sc + (8 * (gi >> 1) + q4) * 64 + (gi & 1) * 32 + ps * 8;
          float cs = 0.f;
          int j = ku0 / 7, t = ku0 % 7;
#pragma unroll
          for (int u = 0; u < 8; ++u) {
            const v2u lo = __builtin_bit_cast(v2u, __builtin_amdgcn_ds_read_tr16_b64_v4i16((LAS v4s*)(tp + u * KB)));
            const v2u hi = __builtin_bit_cast(v2u, __builtin_amdgcn_ds_read_tr16_b64_v4i16((LAS v4s*)(tp + u * KB + 256)));
            const v4u f = {lo.x, lo.y, hi.x, hi.y};
            const int jj = (j - ufb) & 7;
            const int dpos = ((t < 4 ? jj * 4 + t : t < 6 ? 28 + jj * 2 + t : 48 + jj) * 4 + (j >> 3) * 2) * KB;
            gst(rs, f, l16, ubase + (ustep ? u * ustep : dpos));
            if (++t == 7) { t = 0; ++j; }
#pragma unroll
            for (int e = 0; e < 4; ++e) { const unsigned fe = f[e]; cs = __builtin_amdgcn_fdot2_f32_bf16(__builtin_bit_cast(v2bf, fe), __builtin_bit_cast(v2bf, 0x3f803f80u), cs, false); }
          }
          if (xoff >= 0) {
            cs = xhalf_sum(cs);
            const unsigned h2 = pk(cs, cs);
            const unsigned wd = pk(cs, cs - bflo(h2));
            if (lane < 32) gst2(rs, v2u{wd, wd}, l16, xoff);
          }
        }
        if (pass == 0) {
          if (first) WAITV(0);
          BARL();
          if (tid == 256) set_flag(ws, F_P1, me, epoch);
        }
      }
      if (hw == 0) {
        STAMP(1);
        const int qq = ((lane - 16) >> 1) * 16 + (m >> 1) * 2 + (lane & 1);
        const int idx = lane < 16 ? g * 16 + lane : wg_index(qq);
        poll(flag_ptr(ws, F_P1, lane < 22 ? idx : 0), lane < 22, epoch, dead);
      }
      BAR();
    }
    const int lb = hw * 8 * KB;
    const int wa = WS_WA + m * 99 * KB + lb, wb2 = WS_WB + fb * 64 * KB + 2 * lb, wc = WS_WC + m * 231 * KB + lb, wdn = WS_WD + fb * 224 * KB + lb;
    const int l16h = (tid & 63) * 16;
#define DMA(EXTRA, base, G, BUF) dma_chunk<EXTRA, G, BUF>(rs, base, lb, l16h)
#define STAGE(arr, base, G) do { const int o_ = ops(base) + (G); _Pragma("unroll") for (int i_ = 0; i_ < 8; ++i_) arr[i_] = gldf(rs, l16, o_, i_); } while (0)
#define ZERO(arr) _Pragma("unroll") for (int i_ = 0; i_ < 8; ++i_) arr[i_] = v4u{0u, 0u, 0u, 0u}
#define PIN(arr) asm volatile("" : "+v"(arr[0]), "+v"(arr[1]), "+v"(arr[2]), "+v"(arr[3]), "+v"(arr[4]), "+v"(arr[5]), "+v"(arr[6]), "+v"(arr[7]))
#define PUT(arr, BUF) do { lptr d_ = L + opv((BUF) + lb + l16); _Pragma("unroll") for (int i_ = 0; i_ < 8; ++i_) ldsw(d_ + i_ * KB, arr[i_]); } while (0)
    DMA(true, wa, 0, BUF_A); DMA(true, wa, CH, BUF_B); DMA(true, wa, 2 * CH, BUF_C);
#pragma unroll 1
    for (int layer = 0; layer < 2; ++layer) {
      LANE_VALS();
      const int act = WS_ACT + layer * A_LAYER;
      const int fl = layer * F_PER_LAYER;
      const int sb = 2 + layer * 28;
      (void)sb;
      v4u d2[8], d3[8], d4[8], d5[8], d6[8], a0[8], a1[8];
      WAITV(16); BAR();
      if (layer == 0 && tid == 256) set_flag(ws, F_P2, me, epoch);
      if (hw == 0) STAMP(sb + 0);
      WAITV(8); BAR();
      if (layer == 0 && hw == 0) {
        int qq;
        if (lane < 2) qq = 160 + 2 * fb + lane;
        else if (lane < 9) qq = 176 + 7 * fb + (lane - 2);
        else { int n = (7 * m) / 4 + ((lane - 9) >> 2); n = n > 27 ? 27 : n; qq = 48 + 4 * n + ((lane - 9) & 3); }
        const int idx = wg_index(qq);
        poll(flag_ptr(ws, F_P2, lane < 21 ? idx : 0), lane < 21, epoch, dead);
      }
      WAITV(0); BAR();
      if (hw == 0) STAMP(sb + 1);
      __builtin_amdgcn_s_sleep(5);
      STAGE(d5, wdn, 160 * KB); STAGE(d6, wdn, 192 * KB);
      BAR();
      {
        const int tq = 3 - hw;
        const v4u Qb = ldsr(L + L_QB + tq * KB + l16);
        int nd = 0;
        const int wol = (hw < 2 ? BUF_D : BUF_A - 32 * KB) + 2 * lb;
        auto fill = [&]() {
          if (nd < 4) dma4<0, 0>(rs, wb2 + nd * 4 * KB, wol + nd * 4 * KB, l16);
          else if (nd < 6) dma4<-16 * KB, BUF_C - 16 * KB>(rs, wc + nd * 4 * KB, lb + nd * 4 * KB, l16);
          ++nd;
        };
        const v4u of = attn_item<1>(L, Qb, lane, tq, g, [&]() {
          if (hw == 3 && g >= 2) {
            poll(flag_ptr(ws, fl + F_KV, me - 32), lane == 0, epoch, dead);
            const int s2 = act + A_KV + (me - 32) * 16 * KB;
            dma4<0, BUF_B>(rs, s2, 0, l16); dma4<4 * KB, BUF_B + 4 * KB>(rs, s2, 0, l16);
            dma4<8 * KB, BUF_B + 8 * KB>(rs, s2, 0, l16); dma4<12 * KB, BUF_B + 12 * KB>(rs, s2, 0, l16);
            WAITV(0);
          }
          if (hw == 0) STAMP(sb + 4);
          BAR();
          fill();
        }, fill);
        gst(rs, of, l16, act + A_OT + ((4 * g + tq) * 32 + 2 * m + 1) * KB);
#pragma unroll 1
        while (nd < 6) fill();
        if (hw == 0) { dma1<32 * KB, BUF_C + 32 * KB>(rs, wc, 0, l16); STAMP(sb + 6); }
        WAITV(0);
        PIN(d5); PIN(d6);
        BAR();
      }
      DMA(true, wc, CH, BUF_B);
      STAGE(d4, wdn, 128 * KB);
      BAR();
      BAR();
      BAR();
      WAITV(0); PIN(d4);
      DMA(true, wc, 2 * CH, BUF_D); DMA(true, wc, 3 * CH, BUF_A);
      BAR();
      WAITV(8); BAR();
      if (hw == 0) STAMP(sb + 13);
      DMA(true, wc, 4 * CH, BUF_C); DMA(true, wc, 5 * CH, BUF_B);
      STAGE(d2, wdn, 64 * KB);
      WAITV(24); BAR();
      STAGE(d3, wdn, 96 * KB);
      WAITV(24); BAR();
      DMA(true, wc, 6 * CH, BUF_D);
      if (layer == 0) { STAGE(a0, wa, 0); WAITV(32); } else { ZERO(a0); WAITV(24); }
      BAR();
      DMA(false, wdn, 0, BUF_A);
      BAR();
      if (tid == 320) set_flag(ws, fl + F_HE, me, epoch);
      if (layer == 0) { STAGE(a1, wa, CH); WAITV(24); } else { ZERO(a1); WAITV(8); }
      BAR();
      DMA(false, wdn, 32 * KB, BUF_C);
      if (hw == 0) { poll(flag_ptr(ws, fl + F_HE, g * 16 + (lane & 15)), lane < 16, epoch, dead); STAMP(sb + 14); }
      BAR();
      WAITV(0); PIN(d2); PIN(d3); PIN(a0); PIN(a1);
      BAR();
      PUT(d2, BUF_B); PUT(d3, BUF_D);
      BAR();
      if (tid == 320) set_flag(ws, fl + F_HM, me, epoch);
      BARL();
      PUT(d4, BUF_A);
      if (hw == 0) { poll(flag_ptr(ws, fl + F_HM, g * 16 + (lane & 15)), lane < 16, epoch, dead); STAMP(sb + 15); }
      BAR();
      if (tid == 320) set_flag(ws, fl + F_HL, me, epoch);
      PUT(d5, BUF_C);
      BARL();
      PUT(d6, BUF_B);
      if (hw == 0) { poll(flag_ptr(ws, fl + F_HL, g * 16 + (lane & 15)), lane < 16, epoch, dead); STAMP(sb + 16); }
      BAR();
      BARL();
      if (layer == 0) PUT(a0, BUF_A);
      BAR();
      if (layer == 0) {
        PUT(a1, BUF_B);
        BAR();
        if (hw == 0) { dma1<32 * KB, BUF_A + 32 * KB>(rs, wa, 0, l16); dma1<CH + 32 * KB, BUF_B + 32 * KB>(rs, wa, 0, l16); WAITV(0); }
        DMA(true, wa, 2 * CH, BUF_C);
        BARL();
      }
    }
  }
}
}

extern "C" void kernel_launch(void* const* d_in, const int* in_sizes, int n_in, void* d_out, int out_size, void* d_ws, size_t ws_size,
                              hipStream_t stream) {
  Params p;
  p.x = (const u16*)d_in[0]; p.wqkv = (const u16*)d_in[1]; p.wo = (const u16*)d_in[2];
  p.wg = (const u16*)d_in[4]; p.wu = (const u16*)d_in[5]; p.wd = (const u16*)d_in[6];
  p.out = (u16*)d_out; p.ws = (unsigned char*)d_ws;
  (void)hipFuncSetAttribute((const void*)fused_layers, hipFuncAttributeMaxDynamicSharedMemorySize, LDS_BYTES);
  hipLaunchKernelGGL(fused_layers, dim3(NWG), dim3(NTHR), LDS_BYTES, stream, p);
}
```

```cpp
#include <hip/hip_runtime.h>
#include <hip/hip_bf16.h>
#include <utility>

#define PROF 0

extern "C" __device__ float llvm_fmul_legacy(float, float) __asm("llvm.amdgcn.fmul.legacy");
namespace {
typedef unsigned short u16;
typedef unsigned v4u __attribute__((ext_vector_type(4)));
typedef unsigned v2u __attribute__((ext_vector_type(2)));
typedef short v4s __attribute__((ext_vector_type(4)));
typedef float f32x4 __attribute__((ext_vector_type(4)));
typedef float f32x16 __attribute__((ext_vector_type(16)));
typedef __bf16 v2bf __attribute__((ext_vector_type(2)));
typedef __bf16 v8bf __attribute__((ext_vector_type(8)));
#define LAS __attribute__((address_space(3)))
#define GAS __attribute__((address_space(1)))
#define DI __device__ __forceinline__
typedef GAS unsigned gu32;
typedef LAS unsigned char* lptr;
#define RLX_AGENT __ATOMIC_RELAXED, __HIP_MEMORY_SCOPE_AGENT

constexpr int NWG = 256, NTHR = 512;
constexpr int KB = 1024;
constexpr int FLAG_STRIDE = 256;
enum { F_P1 = 0, F_P2 = 1, F_KV = 2, F_O = 3, F_X1 = 4, F_H = 5, F_X2 = 6, F_PER_LAYER = 5, F_KINDS = 12 };
constexpr int WS_EPOCH = 0;
constexpr int WS_FLAGS = 4 * KB;
constexpr int WS_WA = WS_FLAGS + F_KINDS * 256 * FLAG_STRIDE;
constexpr int WS_WB = WS_WA + 16 * 99 * KB;
constexpr int WS_WC = WS_WB + 8 * 64 * KB;
constexpr int WS_WD = WS_WC + 16 * 231 * KB;
constexpr int WS_ACT = WS_WD + 8 * 224 * KB;
constexpr int A_XT = 0, A_ST0 = A_XT + 2048 * KB, A_KV = A_ST0 + 256 * KB, A_OT = A_KV + 4096 * KB, A_X1T = A_OT + 2048 * KB,
              A_ST1 = A_X1T + 2048 * KB, A_HT = A_ST1 + 256 * KB, A_LAYER = A_HT + 64 * 112 * KB;
constexpr int WS_DBG = WS_ACT + 2 * A_LAYER;
constexpr int WS_BYTES = WS_DBG + 256 * KB * 2;
constexpr int CH = 33 * KB;
constexpr int BUF_A = 0, BUF_B = CH, BUF_C = 2 * CH, BUF_D = 3 * CH, SP = 4 * CH;
constexpr int L_KV = SP;
constexpr int L_QB = SP + 16 * KB;
constexpr int L_ONES = SP + 20 * KB;
constexpr int L_RS = SP + 22 * KB;
constexpr int L_SY = L_RS + 512;
constexpr int SY_GO = 0, SY_DR = 4, SY_HWL = 8, SY_EX = 16;
constexpr int L_DBG = SP + 25 * KB;
constexpr int L_PF = SP + 24 * KB;
constexpr int LDS_BYTES = SP + (PROF == 1 ? 27 : 25) * KB;

constexpr float NEG_BIG = -1e30f;
constexpr float LOG2E = 1.4426950408889634f;
constexpr int AUX_SC1 = 16;

struct Params {
  const u16 *x, *wqkv, *wo, *wg, *wu, *wd;
  u16* out;
  unsigned char* ws;
};

template <int I> struct IC { static constexpr int v = I; };
template <class F, int... Is> DI void sfor_impl(F& f, std::integer_sequence<int, Is...>) { (f(IC<Is>{}), ...); }
template <int N, class F> DI void sfor(F f) { sfor_impl(f, std::make_integer_sequence<int, N>{}); }
#define AI __attribute__((always_inline))
DI f32x16 mfma(v4u a, v4u b, f32x16 c) {
  return __builtin_amdgcn_mfma_f32_32x32x16_bf16(__builtin_bit_cast(v8bf, a), __builtin_bit_cast(v8bf, b), c, 0, 0, 0);
}
DI unsigned pk(float a, float b) { v2bf v = {(__bf16)a, (__bf16)b}; return __builtin_bit_cast(unsigned, v); }
DI float fm(float a, float b) { return llvm_fmul_legacy(a, b); }
DI float bflo(unsigned w) { return __uint_as_float(w << 16); }
DI float bfhi(unsigned w) { return __uint_as_float(w & 0xffff0000u); }
DI float xhalf_sum(float v) { auto r = __builtin_amdgcn_permlane32_swap(__float_as_uint(v), __float_as_uint(v), false, false); return __uint_as_float(r[0]) + __uint_as_float(r[1]); }
DI float xhalf_max(float v) { auto r = __builtin_amdgcn_permlane32_swap(__float_as_uint(v), __float_as_uint(v), false, false); return __builtin_fmaxf(__uint_as_float(r[0]), __uint_as_float(r[1])); }
DI v4u ldsr(lptr p) { return *(LAS v4u*)p; }
DI void ldsw(lptr p, v4u v) { *(LAS v4u*)p = v; }
typedef __amdgpu_buffer_rsrc_t rsrc_t;
DI v4u gld(rsrc_t rs, int voff, int soff) { return __builtin_amdgcn_raw_buffer_load_b128(rs, voff, soff, AUX_SC1); }
DI v4u gldf(rsrc_t rs, int l16, int soff, int i) { return __builtin_amdgcn_raw_buffer_load_b128(rs, l16 + (i & 3) * KB, soff + (i >> 2) * 4 * KB, AUX_SC1); }
DI int opv(int x) { asm volatile("" : "+v"(x)); return x; }
DI int ops(int x) { asm volatile("" : "+s"(x)); return x; }
DI void gst(rsrc_t rs, v4u v, int voff, int soff) { __builtin_amdgcn_raw_buffer_store_b128(v, rs, voff, soff, AUX_SC1); }
DI void gst2(rsrc_t rs, v2u v, int voff, int soff) { __builtin_amdgcn_raw_buffer_store_b64(v, rs, voff, soff, AUX_SC1); }
#define SCHED() __builtin_amdgcn_sched_barrier(0)
#define PINA(a) asm volatile("" : "+v"(a))
#define BAR() asm volatile("s_barrier" ::: "memory")
#define BARL() asm volatile("s_waitcnt lgkmcnt(0)\n\ts_barrier" ::: "memory")
#define WAITV(n) asm volatile("s_waitcnt vmcnt(" #n ")" ::: "memory")

template <int G, int LO> DI void dma4(rsrc_t rs, int sb, int lb, int voff) {
  unsigned keep, so;
  asm volatile("s_mov_b32 %0, m0\n\ts_add_u32 m0, %5, %7\n\ts_add_u32 %1, %4, %6\n\ts_nop 2\n\t"
               "buffer_load_dwordx4 %2, %3, %1 offen sc1 lds\n\tbuffer_load_dwordx4 %2, %3, %1 offen offset:1024 sc1 lds\n\t"
               "buffer_load_dwordx4 %2, %3, %1 offen offset:2048 sc1 lds\n\tbuffer_load_dwordx4 %2, %3, %1 offen offset:3072 sc1 lds\n\t"
               "s_mov_b32 m0, %0" : "=&s"(keep), "=&s"(so) : "v"(voff), "s"(rs), "s"(sb), "s"(lb), "i"(G), "i"(LO) : "memory", "scc");
}
template <int G, int LO> DI void dma1(rsrc_t rs, int sb, int lb, int voff) {
  unsigned keep, so;
  asm volatile("s_mov_b32 %0, m0\n\ts_add_u32 m0, %5, %7\n\ts_add_u32 %1, %4, %6\n\ts_nop 2\n\t"
               "buffer_load_dwordx4 %2, %3, %1 offen sc1 lds\n\t"
               "s_mov_b32 m0, %0" : "=&s"(keep), "=&s"(so) : "v"(voff), "s"(rs), "s"(sb), "s"(lb), "i"(G), "i"(LO) : "memory", "scc");
}
template <int G> DI void touch(rsrc_t rs, int sb, int voff) {
  unsigned keep, so;
  asm volatile("s_mov_b32 %0, m0\n\ts_mov_b32 m0, %6\n\ts_add_u32 %1, %4, %5\n\ts_nop 2\n\t"
               "buffer_load_dword %2, %3, %1 offen sc1 lds\n\t"
               "s_mov_b32 m0, %0" : "=&s"(keep), "=&s"(so) : "v"(voff), "s"(rs), "s"(sb), "i"(G), "i"(L_PF) : "memory", "scc");
}
template <bool EXTRA, int G, int LBUF> DI void dma_chunk(rsrc_t rs, int sb, int lb, int voff) {
  dma4<G, LBUF>(rs, sb, lb, voff);
  dma4<G + 4 * KB, LBUF + 4 * KB>(rs, sb, lb, voff);
  if (EXTRA && lb == 0) dma1<G + 32 * KB, LBUF + 32 * KB>(rs, sb, lb, voff);
}

#define MAPV 1
__host__ __device__ inline int wg_index(int b) {
  if (MAPV == 0) return ((b & 7) * 2 + (b >> 7)) * 16 + ((b >> 3) & 15);
  return (4 * ((b & 7) >> 1) + (b >> 6)) * 16 + 8 * (b & 1) + ((b >> 3) & 7);
}
DI gu32* flag_ptr(unsigned char* ws, int kind, int idx) { return (gu32*)(ws + WS_FLAGS + (size_t)(kind * 256 + idx) * FLAG_STRIDE); }
DI void set_flag(unsigned char* ws, int kind, int idx, unsigned epoch) { __hip_atomic_store(flag_ptr(ws, kind, idx), epoch, RLX_AGENT); }
DI void poll(gu32* f, bool active, unsigned epoch, bool& dead) {
  if (!dead) {
    const unsigned long long t0 = __builtin_amdgcn_s_memrealtime();
    for (unsigned spins = 1;; ++spins) {
      unsigned v = epoch;
      if (active) v = __hip_atomic_load(f, RLX_AGENT);
      if (__all(v == epoch)) break;
      if ((spins & 63u) == 0u && __builtin_amdgcn_s_memrealtime() - t0 > 2000000ull) { dead = true; break; }
    }
  }
  __builtin_amdgcn_fence(__ATOMIC_ACQUIRE, "wavefront");
}

DI void poll_h(gu32* f, bool active, unsigned epoch, unsigned need, unsigned& have, bool& dead) {
  if (!dead && have < need) {
    const unsigned long long t0 = __builtin_amdgcn_s_memrealtime();
    for (unsigned spins = 1;; ++spins) {
      unsigned c = 7u;
      if (active) { const unsigned v = __hip_atomic_load(f, RLX_AGENT); c = (v >> 3) == (epoch & 0x1fffffffu) ? (v & 7u) : 0u; }
      if (__all(c >= need)) { have = __all(c >= need + 2) ? need + 2 : __all(c >= need + 1) ? need + 1 : need; break; }
      if ((spins & 63u) == 0u && __builtin_amdgcn_s_memrealtime() - t0 > 2000000ull) { dead = true; break; }
    }
  }
  __builtin_amdgcn_fence(__ATOMIC_ACQUIRE, "wavefront");
}
DI void lds_set(lptr L, int word, unsigned v) { asm volatile("" ::: "memory"); *(volatile LAS unsigned*)(L + L_SY + word) = v; asm volatile("" ::: "memory"); }
DI void lds_add(lptr L, int word, int lane) {
  asm volatile("" ::: "memory");
  if (lane == 0) __hip_atomic_fetch_add((LAS unsigned*)(L + L_SY + word), 1u, __ATOMIC_RELAXED, __HIP_MEMORY_SCOPE_WORKGROUP);
  asm volatile("" ::: "memory");
}
DI void lds_wait(lptr L, int word, unsigned v) {
  for (unsigned n = 0; n < (1u << 22); ++n)
    if ((unsigned)__builtin_amdgcn_readfirstlane(*(volatile LAS unsigned*)(L + L_SY + word)) >= v) break;
  asm volatile("" ::: "memory");
}
DI void lds_wait4(lptr L, unsigned v) {
  for (unsigned n = 0; n < (1u << 22); ++n) {
    const v4u e = *(volatile LAS v4u*)(L + L_SY + SY_EX);
    const unsigned a = e.x < e.y ? e.x : e.y, b = e.z < e.w ? e.z : e.w;
    if ((unsigned)__builtin_amdgcn_readfirstlane(a < b ? a : b) >= v) break;
    __builtin_amdgcn_s_sleep(1);
  }
  asm volatile("" ::: "memory");
}

#define STAMP(i) do {} while (0)

DI void finish_stats(const v4u (&st)[4], float& mean, float& rstd) {
  float s = 0.f, q = 0.f;
#pragma unroll
  for (int i = 0; i < 4; ++i) { s += __uint_as_float(st[i].x) + __uint_as_float(st[i].z); q += __uint_as_float(st[i].y) + __uint_as_float(st[i].w); }
  s = xhalf_sum(s); q = xhalf_sum(q);
  mean = s * (1.f / 512.f);
  const float var = __builtin_fmaxf(q * (1.f / 512.f) - mean * mean, 0.f);
  rstd = __builtin_amdgcn_rsqf(var + 1e-5f);
}
DI v4u mean_frag(float mean) {
  const float nm = -mean;
  const unsigned hh = pk(nm, nm);
  const float lo = nm - bflo(hh);
  const unsigned ll = pk(lo, lo);
  return v4u{hh, ll, hh, ll};
}
DI void store_stats(rsrc_t rs, const float (&v)[16], int lane, int soff) {
  float s = 0.f, q = 0.f;
#pragma unroll
  for (int i = 0; i < 16; ++i) { s += v[i]; q = __builtin_fmaf(v[i], v[i], q); }
  s = xhalf_sum(s); q = xhalf_sum(q);
  if (lane < 32) gst2(rs, v2u{__float_as_uint(s), __float_as_uint(q)}, lane * 16, soff);
}

struct Attn { f32x16 O; float m; };
DI float max16(const f32x16& s, float m) {
#pragma unroll
  for (int i = 0; i < 16; i += 2) m = __builtin_fmaxf(__builtin_fmaxf(m, s[i]), s[i + 1]);
  return m;
}
DI f32x16 pv(const f32x16& s, v4u v0, v4u v1, f32x16 O) {
  float p[16];
#pragma unroll
  for (int i = 0; i < 16; ++i) p[i] = __builtin_amdgcn_exp2f(s[i]);
  const v4u p0 = {pk(p[0], p[1]), pk(p[2], p[3]), pk(p[4], p[5]), pk(p[6], p[7])};
  const v4u p1 = {pk(p[8], p[9]), pk(p[10], p[11]), pk(p[12], p[13]), pk(p[14], p[15])};
  O = mfma(v0, p0, O);
  O = mfma(v1, p1, O);
  return O;
}
template <bool FIRST>
DI void attn_chunk(Attn& A, v4u Q, lptr kp, lptr vp, int vstride, int t0, int t1, int tm, const f32x16& mask) {
  const f32x16 zero = {};
  float mx = FIRST ? NEG_BIG : A.m;
  f32x16 sm = zero;
  if (tm >= 0) { sm = mfma(ldsr(kp + tm * 4096), Q, mask); mx = max16(sm, mx); }
  for (int t = t0; t < t1; ++t) { const f32x16 s = mfma(ldsr(kp + t * 4096), Q, zero); mx = max16(s, mx); }
  mx = xhalf_max(mx);
  if (FIRST) A.O = zero;
  else { const float al = __builtin_amdgcn_exp2f(A.m - mx); A.O *= al; }
  A.m = mx;
  if (tm >= 0) { A.O = pv(sm - mx, ldsr(vp + tm * vstride), ldsr(vp + tm * vstride + 1024), A.O); }
  f32x16 nm;
#pragma unroll
  for (int i = 0; i < 16; ++i) nm[i] = -mx;
  for (int t = t0; t < t1; ++t) {
    const f32x16 s = mfma(ldsr(kp + t * 4096), Q, nm);
    A.O = pv(s, ldsr(vp + t * vstride), ldsr(vp + t * vstride + 1024), A.O);
  }
}
DI float opf(float x) { asm volatile("" : "+v"(x)); return x; }
struct Fast { f32x16 O, nm; };
DI void fast_diag(Fast& F, v4u Q, lptr kp, lptr vp, const f32x16& mask) {
  const v4u k = ldsr(kp), v0 = ldsr(vp), v1 = ldsr(vp + 1024);
  const f32x16 s = mfma(k, Q, mask);
  float m0 = __builtin_fmaxf(s[0], s[1]), m1 = __builtin_fmaxf(s[2], s[3]);
#pragma unroll
  for (int i = 4; i < 16; i += 4) { m0 = __builtin_fmaxf(__builtin_fmaxf(m0, s[i]), s[i + 1]); m1 = __builtin_fmaxf(__builtin_fmaxf(m1, s[i + 2]), s[i + 3]); }
  const float mx = xhalf_max(__builtin_fmaxf(m0, m1));
  f32x16 d;
#pragma unroll
  for (int i = 0; i < 16; ++i) { d[i] = opf(s[i] - mx); F.nm[i] = -mx; }
  F.O = pv(d, v0, v1, f32x16{});
}
template <class Hook>
DI void fast_run(Fast& F, v4u Q, lptr kp, lptr vp, int vstride, int t0, int t1, const f32x16& c0, Hook hook) {
  lptr kq = kp + t0 * 4096, vq = vp + t0 * vstride;
  v4u k = ldsr(kq), va0 = ldsr(vq), va1 = ldsr(vq + 1024), vb0, vb1;
  f32x16 sa = mfma(k, Q, c0), sb;
  k = ldsr(kq + 4096);
  int n = t1 - t0;
#pragma unroll 1
  for (; n >= 2; n -= 2) {
    sb = mfma(k, Q, F.nm);
    vq += vstride; vb0 = ldsr(vq); vb1 = ldsr(vq + 1024); k = ldsr(kq + 2 * 4096);
    hook();
    SCHED();
    F.O = pv(sa, va0, va1, F.O);
    SCHED();
    sa = mfma(k, Q, F.nm);
    vq += vstride; va0 = ldsr(vq); va1 = ldsr(vq + 1024); kq += 2 * 4096; k = ldsr(kq + 4096);
    hook();
    SCHED();
    F.O = pv(sb, vb0, vb1, F.O);
    SCHED();
  }
  if (n) { hook(); F.O = pv(sa, va0, va1, F.O); }
}
DI void make_mask(f32x16& mk, int lane, bool old) {
  const int d = (lane & 31) - 4 * (lane >> 5);
#pragma unroll
  for (int i = 0; i < 16; ++i) { const bool le = ((i & 3) + 8 * (i >> 2)) <= d; mk[i] = (le != old) ? 0.f : NEG_BIG; }
}
template <int HD> DI v4u attn_out(const f32x16& O) {
  const float r = __builtin_amdgcn_rcpf(O[HD ? 0 : 8]);
  const int o = HD ? 8 : 0;
  return v4u{pk(O[o] * r, O[o + 1] * r), pk(O[o + 2] * r, O[o + 3] * r), pk(O[o + 4] * r, O[o + 5] * r), pk(O[o + 6] * r, O[o + 7] * r)};
}
template <int HD, class Mid, class Hook>
DI v4u attn_item(lptr L, v4u Q, int lane, int tq, int g, Mid mid, Hook hook) {
  const int l16 = lane * 16;
  const bool vl = ((lane >> 4) & 1) == HD;
  const int vstride = vl ? 4096 : 0;
  lptr kl = L + L_KV + HD * KB + l16, vloc = vl ? L + L_KV + 2 * KB + l16 : L + L_ONES;
  lptr kr = L + BUF_B + HD * KB + l16, vrem = vl ? L + BUF_B + 2 * KB + l16 : L + L_ONES;
  Fast F; f32x16 mk;
  make_mask(mk, lane, false);
  fast_diag(F, Q, kl + tq * 4096, vloc + tq * vstride, mk);
  if (tq > 0) fast_run(F, Q, kl, vloc, vstride, 0, tq, F.nm, []() {});
  f32x16 c0;
#pragma unroll
  for (int i = 0; i < 16; ++i) c0[i] = opf(g >= 2 ? __builtin_fminf(F.nm[0], NEG_BIG - mk[i] * 2.f) : F.nm[0]);
  mid();
  if (g >= 1) fast_run(F, Q, kr, vrem, vstride, g >= 2 ? tq : 4, 8, c0, hook);
  if (__any(!(F.O[HD ? 0 : 8] <= 1.8446744e19f))) {
    Attn A;
    make_mask(mk, opv(lane), false);
    attn_chunk<true>(A, Q, kl, vloc, vstride, 0, tq, tq, mk);
    if (g >= 1) { make_mask(mk, opv(lane), true); attn_chunk<false>(A, Q, kr, vrem, vstride, g >= 2 ? tq + 1 : 4, 8, g >= 2 ? tq : -1, mk); }
    F.O = A.O;
  }
  return attn_out<HD>(F.O);
}

DI void finish_tile(lptr L, const f32x16& keep, v4u (&res)[2], float (&v)[16], int w, int lane) {
  lptr xp = L + L_KV + (w ^ 2) * 4096 + lane * 16;
#pragma unroll
  for (int qd = 0; qd < 4; ++qd) {
    const f32x4 r = *(LAS f32x4*)(xp + qd * 1024);
#pragma unroll
    for (int j = 0; j < 4; ++j) v[4 * qd + j] = keep[4 * qd + j] + r[j];
  }
#pragma unroll
  for (int e = 0; e < 2; ++e)
#pragma unroll
    for (int j = 0; j < 4; ++j) { v[8 * e + 2 * j] += bflo(res[e][j]); v[8 * e + 2 * j + 1] += bfhi(res[e][j]); }
#pragma unroll
  for (int e = 0; e < 2; ++e) res[e] = v4u{pk(v[8 * e], v[8 * e + 1]), pk(v[8 * e + 2], v[8 * e + 3]), pk(v[8 * e + 4], v[8 * e + 5]), pk(v[8 * e + 6], v[8 * e + 7])};
}
DI void send_tile(lptr L, const f32x16& send, int w, int lane) {
  lptr xp = L + L_KV + w * 4096 + lane * 16;
#pragma unroll
  for (int qd = 0; qd < 4; ++qd) *(LAS f32x4*)(xp + qd * 1024) = f32x4{send[4 * qd], send[4 * qd + 1], send[4 * qd + 2], send[4 * qd + 3]};
}

__global__ void __launch_bounds__(NTHR, 2) fused_layers(Params p) {
  extern __shared__ __attribute__((aligned(16))) unsigned char smem[];
  lptr L = (lptr)smem;
  const int tid = threadIdx.x, wave = __builtin_amdgcn_readfirstlane(tid >> 6);
  const int b = blockIdx.x, me = wg_index(b), g = me >> 4, m = me & 15;
  unsigned char* ws = p.ws;
  const rsrc_t rs = __builtin_amdgcn_make_buffer_rsrc(ws, 0, WS_BYTES, 0x00020000);
  const unsigned epoch = __builtin_amdgcn_readfirstlane(__hip_atomic_load((gu32*)(ws + WS_EPOCH), RLX_AGENT)) + 1u;
  asm volatile("" :: "s"(epoch));
  const int th = m >> 3, fb = m & 7;
  bool dead = false;
#define LANE_VALS() const int lane = opv(tid) & 63, r32 = lane & 31, hf = lane >> 5, l16 = lane * 16; (void)r32; (void)hf; (void)l16
  if (tid < 8) { *(LAS unsigned*)(L + L_ONES + (tid >> 2) * 1024 + (tid & 3) * 4) = 0x3f803f80u; *(LAS unsigned*)(L + L_SY + tid * 4) = 0u; }

  if (wave < 4) {
    const int w = wave;
    const int ttr = 4 * g + 2 * th + (w & 1), kh = w >> 1, ftile = 2 * fb + kh;
    const int tta = 4 * g + w;
    v4u res[2];
    {
      LANE_VALS();
      STAMP(0);
      const GAS v4u* xs = (const GAS v4u*)(p.x + (size_t)(ttr * 32 + r32) * 512 + ftile * 32 + hf * 8);
      res[0] = xs[0]; res[1] = xs[2];
      const int so = WS_ACT + A_XT + (ttr * 32 + 2 * ftile) * KB;
      gst(rs, res[0], l16, so); gst(rs, res[1], l16 + KB, so);
      float v[16];
#pragma unroll
      for (int e = 0; e < 2; ++e)
#pragma unroll
        for (int j = 0; j < 4; ++j) { v[8 * e + 2 * j] = bflo(res[e][j]); v[8 * e + 2 * j + 1] = bfhi(res[e][j]); }
      store_stats(rs, v, lane, WS_ACT + A_ST0 + ttr * 4096 + (ftile >> 1) * 512 + (ftile & 1) * 8);
      WAITV(0);
      BARL();
      STAMP(1);
      BAR();
    }
#pragma unroll 1
    for (int layer = 0; layer < 2; ++layer) {
      const int act = WS_ACT + layer * A_LAYER;
      const int fl = layer * F_PER_LAYER;
      const int sb = 2 + layer * 28;
      (void)sb;
      v4u xf[32];
      v4u Qa;
      {
        LANE_VALS();
        STAMP(sb + 0);
        constexpr int WIN = 12;
        v4u st[4];
        const int sto = ops(act + A_ST0 + tta * 4096), xo = ops(act + A_XT + tta * 32 * KB);
#pragma unroll
        for (int i = 0; i < 4; ++i) st[i] = gld(rs, hf * 2048 + r32 * 16 + i * 512, sto);
#pragma unroll
        for (int i = 0; i < WIN; ++i) xf[i] = gldf(rs, l16, xo, i);
        SCHED();
        BAR();
        f32x16 aK = {}, aV = {};
        constexpr int PD = 3;
        v4u wk[PD], wv[PD];
        lptr pa = L + BUF_A + l16, pb = L + opv(BUF_B + l16);
#pragma unroll
        for (int i = 0; i < PD; ++i) { wk[i] = ldsr(pa + 2 * i * KB); wv[i] = ldsr(pa + (2 * i + 1) * KB); }
        float mean, rstd; v4u xe, wex;
#pragma unroll
        for (int s = 0; s < 32; ++s) {
          if (s + WIN < 32) xf[s + WIN] = gldf(rs, l16, xo, s + WIN);
          const v4u ck = wk[s % PD], cv = wv[s % PD];
          if (s + PD == 16) { wex = ldsr(pa + 32 * KB); BAR(); }
          if (s + PD < 16) { wk[s % PD] = ldsr(pa + 2 * (s + PD) * KB); wv[s % PD] = ldsr(pa + (2 * (s + PD) + 1) * KB); }
          else if (s + PD < 32) { wk[s % PD] = ldsr(pb + 2 * (s + PD - 16) * KB); wv[s % PD] = ldsr(pb + (2 * (s + PD - 16) + 1) * KB); }
          aK = mfma(ck, xf[s], aK);
          aV = mfma(xf[s], cv, aV);
          if (s == 4) { finish_stats(st, mean, rstd); xe = mean_frag(mean); if (lane < 32) *(LAS float*)(L + L_RS + w * 128 + lane * 4) = rstd; }
          if (s == 17) { aK = mfma(wex, xe, aK); wex = ldsr(pb + 32 * KB); }
          if (s == 22) aV = mfma(xe, wex, aV);
          SCHED();
        }
        STAMP(sb + 1);
        BAR();
        lptr pc = L + opv(BUF_C + l16);
        constexpr int PQ = 4;
        v4u wq[PQ];
#pragma unroll
        for (int i = 0; i < PQ; ++i) wq[i] = ldsr(pc + i * KB);
        f32x4 rsv[4];
#pragma unroll
        for (int qd = 0; qd < 4; ++qd) rsv[qd] = *(LAS f32x4*)(L + L_RS + w * 128 + qd * 32 + hf * 16);
        f32x16 aQ = {};
        lptr kvl = L + opv(L_KV + w * 4096 + l16);
        const int kvo = ops(act + A_KV + (me * 4 + w) * 4096);
        v4u kvf;
#pragma unroll
        for (int s = 0; s < 33; ++s) {
          const v4u cq = wq[s % PQ];
          if (s + PQ < 33) wq[s % PQ] = ldsr(pc + (s + PQ) * KB);
          aQ = mfma(cq, s < 32 ? xf[s < 32 ? s : 0] : xe, aQ);
          if (s < 8) {
            const int f = s >> 1, o = (f & 1) * 8 + (s & 1) * 4;
            if (f < 2) { kvf[(s & 1) * 2] = pk(fm(aK[o], rstd), fm(aK[o + 1], rstd)); kvf[(s & 1) * 2 + 1] = pk(fm(aK[o + 2], rstd), fm(aK[o + 3], rstd)); }
            else { const f32x4 r4 = rsv[o >> 2]; kvf[(s & 1) * 2] = pk(fm(aV[o], r4[0]), fm(aV[o + 1], r4[1])); kvf[(s & 1) * 2 + 1] = pk(fm(aV[o + 2], r4[2]), fm(aV[o + 3], r4[3])); }
            if (s & 1) { gst(rs, kvf, l16 + f * KB, kvo); ldsw(kvl + f * KB, kvf); }
          }
          SCHED();
        }
        const float qs = rstd * (0.25f * LOG2E);
        ldsw(L + L_QB + w * KB + l16, v4u{pk(aQ[8] * qs, aQ[9] * qs), pk(aQ[10] * qs, aQ[11] * qs), pk(aQ[12] * qs, aQ[13] * qs), pk(aQ[14] * qs, aQ[15] * qs)});
        Qa = v4u{pk(aQ[0] * qs, aQ[1] * qs), pk(aQ[2] * qs, aQ[3] * qs), pk(aQ[4] * qs, aQ[5] * qs), pk(aQ[6] * qs, aQ[7] * qs)};
        STAMP(sb + 2);
        WAITV(0);
        BARL();
        if (tid == 0) set_flag(ws, fl + F_KV, me, epoch);
        STAMP(sb + 3);
      }
      {
        LANE_VALS();
        const v4u of = attn_item<0>(L, Qa, lane, w, g, [&]() {
          STAMP(sb + 4);
          if (w == 0) {
            if (g >= 1) {
              poll(flag_ptr(ws, fl + F_KV, me - 16), lane == 0, epoch, dead);
              const int src = act + A_KV + (me - 16) * 16 * KB;
              dma4<0, BUF_B + 16 * KB>(rs, src, 0, l16); dma4<4 * KB, BUF_B + 20 * KB>(rs, src, 0, l16);
              dma4<8 * KB, BUF_B + 24 * KB>(rs, src, 0, l16); dma4<12 * KB, BUF_B + 28 * KB>(rs, src, 0, l16);
              WAITV(0);
            }
          }
          BAR();
          STAMP(sb + 5);
        }, []() {});
        gst(rs, of, l16, act + A_OT + (tta * 32 + 2 * m) * KB);
        STAMP(sb + 6);
        WAITV(0);
        BAR();
        if (tid == 0) set_flag(ws, fl + F_O, me, epoch);
        STAMP(sb + 7);
        if (w == 0) { poll(flag_ptr(ws, fl + F_O, g * 16 + (lane & 15)), lane < 16, epoch, dead); lds_set(L, SY_GO, 3 * layer + 1); }
        else lds_wait(L, SY_GO, 3 * layer + 1);
        STAMP(sb + 8);
      }
      {
        LANE_VALS();
        const int oo = ops(act + A_OT + (ttr * 32 + 16 * kh) * KB);
#pragma unroll
        for (int i = 0; i < 16; ++i) xf[i] = gldf(rs, l16, oo, i);
        SCHED();
        f32x16 keep = {}, send = {};
        lptr k0 = L + opv(BUF_D + 3 * kh * KB + l16), s0 = L + opv(BUF_D + (1 + kh) * KB + l16);
        lptr k1 = L + opv(BUF_A + 3 * kh * KB + l16), s1 = L + opv(BUF_A + (1 + kh) * KB + l16);
        constexpr int PD = 3;
        v4u wk[PD], wsd[PD];
#pragma unroll
        for (int i = 0; i < PD; ++i) { wk[i] = ldsr(k0 + i * 4 * KB); wsd[i] = ldsr(s0 + i * 4 * KB); }
#pragma unroll
        for (int s = 0; s < 16; ++s) {
          const v4u ck = wk[s % PD], cs = wsd[s % PD];
          if (s + PD < 8) { wk[s % PD] = ldsr(k0 + (s + PD) * 4 * KB); wsd[s % PD] = ldsr(s0 + (s + PD) * 4 * KB); }
          else if (s + PD < 16) { wk[s % PD] = ldsr(k1 + (s + PD - 8) * 4 * KB); wsd[s % PD] = ldsr(s1 + (s + PD - 8) * 4 * KB); }
          send = mfma(cs, xf[s], send);
          keep = mfma(ck, xf[s], keep);
          SCHED();
        }
        send_tile(L, send, w, lane);
        lds_set(L, SY_EX + 4 * w, 2 * layer + 1);
        STAMP(sb + 9);
        lds_wait(L, SY_EX + 4 * (w ^ 2), 2 * layer + 1);
        float v[16];
        finish_tile(L, keep, res, v, w, lane);
        const int so = act + A_X1T + (ttr * 32 + 2 * ftile) * KB;
        gst(rs, res[0], l16, so); gst(rs, res[1], l16 + KB, so);
        store_stats(rs, v, lane, act + A_ST1 + ttr * 4096 + (ftile >> 1) * 512 + (ftile & 1) * 8);
        STAMP(sb + 10);
        WAITV(0);
        lds_add(L, SY_DR, lane);
        if (w == 0) {
          lds_wait(L, SY_DR, 8 * layer + 4);
          if (lane == 0) set_flag(ws, fl + F_X1, me, epoch);
          STAMP(sb + 11);
          poll(flag_ptr(ws, fl + F_X1, g * 16 + (lane & 15)), lane < 16, epoch, dead);
          lds_set(L, SY_GO, 3 * layer + 2);
        } else lds_wait(L, SY_GO, 3 * layer + 2);
        STAMP(sb + 12);
      }
      {
        LANE_VALS();
        constexpr int WIN = 12;
        v4u st[4];
        const int sto = ops(act + A_ST1 + tta * 4096), xo = ops(act + A_X1T + tta * 32 * KB);
#pragma unroll
        for (int i = 0; i < 4; ++i) st[i] = gld(rs, hf * 2048 + r32 * 16 + i * 512, sto);
#pragma unroll
        for (int i = 0; i < WIN; ++i) xf[i] = gldf(rs, l16, xo, i);
        SCHED();
        lds_wait(L, SY_HWL, 4 * layer + 4);
        f32x16 acc[3];
        acc[0] = f32x16{}; acc[1] = f32x16{};
        float mean, rstd, c1, sd; v4u xe;
        lptr pbuf[4] = {L + BUF_A + l16, L + opv(BUF_B + l16), L + opv(BUF_C + l16), L + opv(BUF_D + l16)};
        {
          constexpr int PD = 3;
          v4u w0[PD], w1[PD];
          lptr p0 = pbuf[2], p1 = pbuf[1];
#pragma unroll
          for (int i = 0; i < PD; ++i) { w0[i] = ldsr(p0 + i * KB); w1[i] = ldsr(p1 + i * KB); }
#pragma unroll
          for (int s = 0; s < 33; ++s) {
            if (s + WIN < 32) xf[s + WIN] = gldf(rs, l16, xo, s + WIN);
            const v4u c0 = w0[s % PD], cc1 = w1[s % PD];
            if (s + PD < 33) { w0[s % PD] = ldsr(p0 + (s + PD) * KB); w1[s % PD] = ldsr(p1 + (s + PD) * KB); }
            const v4u xa = s < 32 ? xf[s < 32 ? s : 0] : xe;
            acc[0] = mfma(c0, xa, acc[0]);
            acc[1] = mfma(cc1, xa, acc[1]);
            if (s == 4) { finish_stats(st, mean, rstd); xe = mean_frag(mean); c1 = -LOG2E * rstd; sd = __builtin_amdgcn_rcpf(rstd); }
            SCHED();
          }
        }
        STAMP(sb + 13);
        const int ho = ops(act + A_HT + (tta * 112 + 7 * m) * KB);
        float ev[8]; v4u hw4;
        auto epi = [&](const f32x16& a, int k, int tile) AI {
          const int e = k >> 1;
          if (!(k & 1)) ev[e] = __builtin_fmaf(__builtin_amdgcn_exp2f(a[e] * c1), sd, sd);
          else {
            ev[e] = fm(a[8 + e], __builtin_amdgcn_rcpf(ev[e]));
            if (e & 1) hw4[e >> 1] = pk(ev[e - 1], ev[e]);
            if (e == 7) gst(rs, hw4, l16 + (tile & 3) * KB, ho + (tile >> 2) * 4 * KB);
          }
        };
        constexpr int DWIN = 16;
        v4u hfr[32];
        const int hb = ops(act + A_HT + (ttr * 112 + 56 * kh) * KB), fbo = ops(fb);
        auto hld = [&](int s) AI {
          const int t = s >> 3, jj = s & 7;
          return gld(rs, l16 + (t & 3) * KB, hb + (((jj + fbo) & 7) * 7 + (t & 4)) * KB);
        };
        constexpr int PQ = 4;
        v4u wq[PQ];
        BARL();
        STAMP(sb + 27);
#pragma unroll
        for (int i = 0; i < PQ; ++i) wq[i] = ldsr(pbuf[3] + i * KB);
        sfor<165>([&](auto U_) AI {
          constexpr int u = decltype(U_)::v, t = 2 + u / 33, s = u % 33;
          constexpr int bufs[8] = {2, 1, 3, 0, 2, 1, 3, 0};
          const v4u cq = wq[u % PQ];
          if constexpr (s + PQ == 33 && t < 6) { if constexpr (t > 2) WAITV(1); BAR(); }
          if constexpr (t == 6 && s == 17) { WAITV(1); BAR(); }
          if constexpr (s + PQ < 33) wq[u % PQ] = ldsr(pbuf[bufs[t]] + (s + PQ) * KB);
          else if constexpr (t < 6) wq[u % PQ] = ldsr(pbuf[bufs[t + 1]] + (s + PQ - 33) * KB);
          if constexpr (s == 0) acc[t % 3] = mfma(cq, xf[0], f32x16{});
          else acc[t % 3] = mfma(cq, s < 32 ? xf[s < 32 ? s : 0] : xe, acc[t % 3]);
          PINA(acc[t % 3]);
          if constexpr (t == 2) { if constexpr (s >= 1 && s < 17) epi(acc[0], s - 1, 0); if constexpr (s >= 17) epi(acc[1], s - 17, 1); }
          else if constexpr (s >= 1 && s < 17) epi(acc[(t - 1) % 3], s - 1, t - 1);
          if constexpr (t == 6 && s >= 17) hfr[s - 17] = hld(s - 17);
          if constexpr (s == 32 && t < 6) STAMP(sb + 21 + t);
          SCHED();
        });
        STAMP(sb + 14);
        BAR();
        STAMP(sb + 15);
        f32x16 keep = {}, send = {};
        constexpr int PD = 3;
        static constexpr int dbufs[7] = {0, 2, 1, 3, 0, 2, 1};
        const int ko = 3 * kh * KB, sdo = (1 - 2 * kh) * KB;
        lptr kb[4] = {pbuf[0] + ko, pbuf[1] + ko, pbuf[2] + ko, pbuf[3] + ko};
        lptr sp[4] = {kb[0] + sdo, kb[1] + sdo, kb[2] + sdo, kb[3] + sdo};
        v4u wk[PD], wsd[PD];
#pragma unroll
        for (int i = 0; i < PD; ++i) { wk[i] = ldsr(kb[dbufs[0]] + i * 4 * KB); wsd[i] = ldsr(sp[dbufs[0]] + i * 4 * KB); }
        sfor<56>([&](auto S_) AI {
          constexpr int s = decltype(S_)::v, n = s + PD;
          constexpr int ld = s < 8 ? s + 16 : (s >= 13 && s < 45) ? s + 11 : -1;
          const v4u ck = wk[s % PD], cs = wsd[s % PD];
          if constexpr (n < 56 && (n & 7) == 0) {
            if constexpr (n == 8) WAITV(21);
            if constexpr (n == 24) WAITV(8);
            BAR();
            if constexpr (n >= 16 && n <= 40) STAMP(sb + 17 + n / 8);
          }
          if constexpr (ld >= 0) hfr[ld % 32] = hld(ld);
          if constexpr (n < 56) { wk[s % PD] = ldsr(kb[dbufs[n >> 3]] + (n & 7) * 4 * KB); wsd[s % PD] = ldsr(sp[dbufs[n >> 3]] + (n & 7) * 4 * KB); }
          send = mfma(cs, hfr[s % 32], send);
          keep = mfma(ck, hfr[s % 32], keep);
          PINA(send); PINA(keep);
          if constexpr (s < 8) { epi(acc[6 % 3], 2 * s, 6); epi(acc[6 % 3], 2 * s + 1, 6); }
          SCHED();
        });
        send_tile(L, send, w, lane);
        lds_set(L, SY_EX + 4 * w, 2 * layer + 2);
        STAMP(sb + 16);
        lds_wait(L, SY_EX + 4 * (w ^ 2), 2 * layer + 2);
        float v[16];
        finish_tile(L, keep, res, v, w, lane);
        if (layer == 0) {
          const int nact = act + A_LAYER;
          const int so = nact + A_XT + (ttr * 32 + 2 * ftile) * KB;
          gst(rs, res[0], l16, so); gst(rs, res[1], l16 + KB, so);
          store_stats(rs, v, lane, nact + A_ST0 + ttr * 4096 + (ftile >> 1) * 512 + (ftile & 1) * 8);
          STAMP(sb + 17);
          WAITV(0);
          lds_add(L, SY_DR, lane);
          if (w == 0) {
            lds_wait(L, SY_DR, 8);
            if (lane == 0) set_flag(ws, fl + F_X2, me, epoch);
            STAMP(sb + 18);
            poll(flag_ptr(ws, fl + F_X2, g * 16 + (lane & 15)), lane < 16, epoch, dead);
            lds_set(L, SY_GO, 3);
          } else lds_wait(L, SY_GO, 3);
        } else {
          GAS v4u* od = (GAS v4u*)(p.out + (size_t)(ttr * 32 + r32) * 512 + ftile * 32 + hf * 8);
          od[0] = res[0]; od[2] = res[1];
          STAMP(sb + 17);
        }
      }
    }
    if (b == 0 && w == 0) {
      LANE_VALS();
#pragma unroll 1
      for (int i = 0; i < 4; ++i) poll(flag_ptr(ws, F_P1, i * 64 + lane), true, epoch, dead);
      if (lane == 0) __hip_atomic_store((gu32*)(ws + WS_EPOCH), epoch, RLX_AGENT);
    }
  } else {
    const int hw = wave - 4;
    {
      LANE_VALS();
      STAMP(0);
      const int q = b;
      const bool has_unit = q < 232, first = q < 48;
      int ubase, ustep, xoff = -1;
      int ku0, ft1 = hw & 1, ufb = 0;
      const u16* src; int ld;
      {
        const int row = lane >> 2, pc = lane & 3;
        if (q < 48) {
          const int kind = q >> 4;
          const int r = q & 15, hp = 2 * (r >> 1) + (hw & 1), quarter = 2 * (r & 1) + (hw >> 1);
          ku0 = 8 * quarter; ld = 1536;
          src = p.wqkv + (size_t)(ku0 * 16 + row) * 1536 + (kind == 0 ? 512 : kind == 1 ? 1024 : 0) + 32 * hp + 8 * pc;
          const int sbase = WS_WA + hp * 99 * KB;
          ubase = sbase + (kind == 2 ? 66 + ku0 : (ku0 >> 4) * 33 + (ku0 & 15) * 2 + kind) * KB;
          ustep = kind == 2 ? KB : 2 * KB;
          xoff = sbase + (kind == 0 ? 32 : kind == 1 ? 65 : 98) * KB + (quarter >> 1) * 512 + (quarter & 1) * 8;
        } else if (q < 160) {
          const int n = (q - 48) >> 2, quarter = (q - 48) & 3, Ft = 4 * n + hw;
          ku0 = 8 * quarter; ld = 1792;
          src = (pc < 2 ? p.wg : p.wu) + (size_t)(ku0 * 16 + row) * 1792 + 16 * Ft + 8 * (pc & 1);
          ubase = WS_WC + (Ft * 33 + ku0) * KB; ustep = KB;
          xoff = WS_WC + (Ft * 33 + 32) * KB + (quarter >> 1) * 512 + (quarter & 1) * 8;
        } else if (q < 176) {
          const int r = q - 160; ufb = r >> 1;
          ku0 = 8 * (2 * (r & 1) + (hw >> 1)); ld = 512;
          src = p.wo + (size_t)(ku0 * 16 + row) * 512 + 64 * ufb + 32 * ft1 + 8 * pc;
          ubase = WS_WB + (ufb * 64 + (ku0 & 15) * 4 + (ku0 >> 4) * 2 + ft1) * KB; ustep = 4 * KB;
        } else {
          const int r = q < 232 ? q - 176 : 0; ufb = r / 7;
          ku0 = 8 * (2 * (r % 7) + (hw >> 1)); ld = 512;
          src = p.wd + (size_t)(ku0 * 16 + row) * 512 + 64 * ufb + 32 * ft1 + 8 * pc;
          ubase = WS_WD + (ufb * 224 + ft1) * KB; ustep = 0;
        }
      }
      v4u uw[8];
#pragma unroll
      for (int u = 0; u < 8; ++u) uw[u] = *(const GAS v4u*)(src + (size_t)u * 16 * ld);
#pragma unroll 1
      for (int pass = 0; pass < 2; ++pass) {
        if (has_unit && first == (pass == 0)) {
          lptr sc = L + BUF_D + hw * 8 * KB;
#pragma unroll
          for (int u = 0; u < 8; ++u) ldsw(sc + u * KB + l16, uw[u]);
          const int gi = lane >> 4, q4 = (lane & 15) >> 2, pp = lane & 3, ps = ((pp & 1) << 1) | (pp >> 1);
          lptr tp = sc + (8 * (gi >> 1) + q4) * 64 + (gi & 1) * 32 + ps * 8;
          float cs = 0.f;
          int j = ku0 / 7, t = ku0 % 7;
#pragma unroll
          for (int u = 0; u < 8; ++u) {
            const v2u lo = __builtin_bit_cast(v2u, __builtin_amdgcn_ds_read_tr16_b64_v4i16((LAS v4s*)(tp + u * KB)));
            const v2u hi = __builtin_bit_cast(v2u, __builtin_amdgcn_ds_read_tr16_b64_v4i16((LAS v4s*)(tp + u * KB + 256)));
            const v4u f = {lo.x, lo.y, hi.x, hi.y};
            const int jj = (j - ufb) & 7;
            const int dpos = ((t * 8 + jj) * 4 + (j >> 3) * 2) * KB;
            gst(rs, f, l16, ubase + (ustep ? u * ustep : dpos));
            if (++t == 7) { t = 0; ++j; }
#pragma unroll
            for (int e = 0; e < 4; ++e) { const unsigned fe = f[e]; cs = __builtin_amdgcn_fdot2_f32_bf16(__builtin_bit_cast(v2bf, fe), __builtin_bit_cast(v2bf, 0x3f803f80u), cs, false); }
          }
          if (xoff >= 0) {
            cs = xhalf_sum(cs);
            const unsigned h2 = pk(cs, cs);
            const unsigned wd = pk(cs, cs - bflo(h2));
            if (lane < 32) gst2(rs, v2u{wd, wd}, l16, xoff);
          }
        }
        if (pass == 0) {
          if (first) WAITV(0);
          BARL();
          if (tid == 256) set_flag(ws, F_P1, me, epoch);
        }
      }
      if (hw == 0) {
        STAMP(1);
        const int qq = ((lane - 16) >> 1) * 16 + (m >> 1) * 2 + (lane & 1);
        const int idx = lane < 16 ? g * 16 + lane : wg_index(qq);
        poll(flag_ptr(ws, F_P1, lane < 22 ? idx : 0), lane < 22, epoch, dead);
      }
      BAR();
    }
    const int lb = hw * 8 * KB;
    const int wa = WS_WA + m * 99 * KB + lb, wb2 = WS_WB + fb * 64 * KB + 2 * lb, wc = WS_WC + m * 231 * KB + lb, wdn = WS_WD + fb * 224 * KB + lb;
    const int l16h = (tid & 63) * 16;
#define DMA(EXTRA, base, G, BUF) dma_chunk<EXTRA, G, BUF>(rs, base, lb, l16h)
#define STAGE(arr, base, G) do { const int o_ = ops(base) + (G); _Pragma("unroll") for (int i_ = 0; i_ < 8; ++i_) arr[i_] = gldf(rs, l16, o_, i_); } while (0)
#define ZERO(arr) _Pragma("unroll") for (int i_ = 0; i_ < 8; ++i_) arr[i_] = v4u{0u, 0u, 0u, 0u}
#define PIN(arr) asm volatile("" : "+v"(arr[0]), "+v"(arr[1]), "+v"(arr[2]), "+v"(arr[3]), "+v"(arr[4]), "+v"(arr[5]), "+v"(arr[6]), "+v"(arr[7]))
#define PUT(arr, BUF) do { lptr d_ = L + opv((BUF) + lb + l16); _Pragma("unroll") for (int i_ = 0; i_ < 8; ++i_) ldsw(d_ + i_ * KB, arr[i_]); } while (0)
    const int ln = tid & 63;
    const int lineAC = MAPV == 0 ? (g & 1) * 132 + hw * 33 + ln : (g & 3) * 66 + hw * 17 + ln;
    const int vAC = (MAPV == 0 ? ln < 33 : (ln < 17 && lineAC < 264)) ? lineAC * 128 : WS_BYTES;
    const int vBD = ln < 16 ? ((MAPV == 0 ? (g & 1) * 2 + th : (g & 3)) * 64 + hw * 16 + ln) * 128 : WS_BYTES;
    const int wa0 = wa - lb, wb0 = wb2 - 2 * lb, wc0 = wc - lb, wd0 = wdn - lb;
#define PFA(c) touch<(c) * CH>(rs, wa0, vAC)
#define PFB(c) touch<(c) * 32 * KB>(rs, wb0, vBD)
#define PFC(c) touch<(c) * CH>(rs, wc0, vAC)
#define PFD(c) touch<(c) * 32 * KB>(rs, wd0, vBD)
    DMA(true, wa, 0, BUF_A); DMA(true, wa, CH, BUF_B); DMA(true, wa, 2 * CH, BUF_C);
    v4u a0[8], a1[8];
#define GET(arr, BUF) do { lptr d_ = L + opv((BUF) + lb + l16); _Pragma("unroll") for (int i_ = 0; i_ < 8; ++i_) arr[i_] = ldsr(d_ + i_ * KB); } while (0)
#define HPUB(nt) do { if (tid == 320) set_flag(ws, fl + F_H, me, (epoch << 3) | (nt)); if (hw >= 2) touch<((nt) - 1) * KB>(rs, hoo, vh); } while (0)
#define HGATE(nt) do { if (hw == 0) poll_h(flag_ptr(ws, fl + F_H, g * 16 + (lane & 15)), lane < 16, epoch, nt, have, dead); } while (0)
#pragma unroll 1
    for (int layer = 0; layer < 2; ++layer) {
      LANE_VALS();
      const int act = WS_ACT + layer * A_LAYER;
      const int fl = layer * F_PER_LAYER;
      const int sb = 2 + layer * 28;
      (void)sb;
      v4u d2[8], d3[8], d4[8], d5[8], d6[8];
      WAITV(16); BARL();
      if (layer == 0 && tid == 256) set_flag(ws, F_P2, me, epoch);
      STAMP(sb + 0);
      WAITV(8); BAR();
      if (layer == 0) {
        GET(a0, BUF_A); GET(a1, BUF_B);
        if (hw == 0) {
          int qq;
          if (lane < 2) qq = 160 + 2 * fb + lane;
          else if (lane < 9) qq = 176 + 7 * fb + (lane - 2);
          else { int n = (7 * m) / 4 + ((lane - 9) >> 2); n = n > 27 ? 27 : n; qq = 48 + 4 * n + ((lane - 9) & 3); }
          const int idx = wg_index(qq);
          poll(flag_ptr(ws, F_P2, lane < 21 ? idx : 0), lane < 21, epoch, dead);
        }
      }
      PIN(a0); PIN(a1);
      WAITV(0); BARL();
      STAMP(sb + 1);
      __builtin_amdgcn_s_sleep(4);
      {
        const int wol = (hw < 2 ? BUF_D : BUF_A - 32 * KB) + 2 * lb;
        dma4<0, 0>(rs, wb2, wol, l16); dma4<4 * KB, 4 * KB>(rs, wb2, wol, l16); dma4<8 * KB, 8 * KB>(rs, wb2, wol, l16); dma4<12 * KB, 12 * KB>(rs, wb2, wol, l16);
      }
      if (layer == 0) PFC(0);
      PFD(6); PFC(1);
      STAMP(sb + 2);
      BAR();
      {
        const int tq = 3 - hw;
        const v4u Qb = ldsr(L + L_QB + tq * KB + l16);
        DMA(true, wc, 0, BUF_C);
        PFD(5);
        const v4u of = attn_item<1>(L, Qb, lane, tq, g, [&]() AI {
          if (hw == 3 && g >= 2) {
            poll(flag_ptr(ws, fl + F_KV, me - 32), lane == 0, epoch, dead);
            const int s2 = act + A_KV + (me - 32) * 16 * KB;
            dma4<0, BUF_B>(rs, s2, 0, l16); dma4<4 * KB, BUF_B + 4 * KB>(rs, s2, 0, l16);
            dma4<8 * KB, BUF_B + 8 * KB>(rs, s2, 0, l16); dma4<12 * KB, BUF_B + 12 * KB>(rs, s2, 0, l16);
            WAITV(0);
          }
          STAMP(sb + 4);
          BAR();
          STAGE(d6, wdn, 192 * KB);
          PFD(4); PFC(2); PFC(3); PFD(3);
        }, []() {});
        gst(rs, of, l16, act + A_OT + ((4 * g + tq) * 32 + 2 * m + 1) * KB);
        STAMP(sb + 6);
        WAITV(0);
        PIN(d6);
        BAR();
      }
      if (hw == 1) touch<A_OT>(rs, act + (4 * g * 32 + 2 * m) * KB, (lane >> 4) * 32 * KB + (lane & 15) * 128);
      DMA(true, wc, CH, BUF_B);
      STAGE(d5, wdn, 160 * KB);
      WAITV(8);
      lds_add(L, SY_HWL, lane);
      STAMP(sb + 7);
      lds_wait(L, SY_GO, 3 * layer + 1);
      __builtin_amdgcn_s_sleep(14);
      STAGE(d4, wdn, 128 * KB);
      PFC(4); PFC(5);
      lds_wait(L, SY_DR, 8 * layer + 4);
      const int vx = (lane >> 5) * 32 * KB + (lane & 31) * 128, vs = lane < 8 ? (lane >> 2) * 4096 + (lane & 3) * 128 : WS_BYTES;
      const int xoo = ((4 * g + 2 * th) * 32 + 4 * fb) * KB, soo = (4 * g + 2 * th) * 4096 + fb * 512;
      if (hw == 1) touch<A_X1T>(rs, act + xoo, vx);
      if (hw == 2) touch<A_ST1>(rs, act + soo, vs);
      DMA(true, wc, 2 * CH, BUF_D); DMA(true, wc, 3 * CH, BUF_A);
      STAMP(sb + 11);
      WAITV(8); BAR();
      PIN(d5); PIN(d4);
      STAMP(sb + 13);
      DMA(true, wc, 4 * CH, BUF_C); DMA(true, wc, 5 * CH, BUF_B);
      PFC(6); PFD(2); PFD(0);
      WAITV(19); BAR();
      STAGE(d3, wdn, 96 * KB);
      PFD(1);
      const int hoo = act + A_HT + ((4 * g + 2 * (hw - 2)) * 112 + 7 * m) * KB;
      const int vh = (lane & 24) ? WS_BYTES : (lane >> 5) * 112 * KB + (lane & 7) * 128;
      unsigned have = 0;
      WAITV(20); BAR();
      if (hw >= 2) touch<0>(rs, hoo, vh);
      HPUB(2);
      DMA(true, wc, 6 * CH, BUF_D);
      STAGE(d2, wdn, 64 * KB);
      if (layer == 0) { PFA(2); PFB(0); }
      WAITV(28); BAR();
      HPUB(3);
      DMA(false, wdn, 0, BUF_A);
      if (layer == 0) { PFB(1); PFC(0); }
      WAITV(16); BAR();
      HPUB(4);
      DMA(false, wdn, 32 * KB, BUF_C);
      HGATE(2);
      STAMP(sb + 14);
      BAR();
      if (tid == 320) set_flag(ws, fl + F_H, me, (epoch << 3) | 5u);
      WAITV(0); PIN(d2); PIN(d3);
      if (hw >= 2) touch<4 * KB>(rs, hoo, vh);
      HGATE(3);
      BAR();
      PUT(d2, BUF_B); PUT(d3, BUF_D);
      BAR();
      HPUB(6);
      HGATE(4);
      STAMP(sb + 15);
      BARL();
      PUT(d4, BUF_A);
      HGATE(5);
      BAR();
      HPUB(7);
      PUT(d5, BUF_C);
      HGATE(6);
      BARL();
      PUT(d6, BUF_B);
      HGATE(7);
      STAMP(sb + 16);
      BAR();
      BARL();
      if (layer == 0) {
        PUT(a0, BUF_A);
        lds_wait4(L, 2);
        PUT(a1, BUF_B);
        if (hw == 0) { dma1<32 * KB, BUF_A + 32 * KB>(rs, wa, 0, l16); dma1<CH + 32 * KB, BUF_B + 32 * KB>(rs, wa, 0, l16); WAITV(0); }
        lds_wait(L, SY_DR, 8);
        if (hw == 1) touch<A_LAYER + A_XT>(rs, act + xoo, vx);
        if (hw == 2) touch<A_LAYER + A_ST0>(rs, act + soo, vs);
        DMA(true, wa, 2 * CH, BUF_C);
      }
    }
  }
}
}

extern "C" void kernel_launch(void* const* d_in, const int* in_sizes, int n_in, void* d_out, int out_size, void* d_ws, size_t ws_size,
                              hipStream_t stream) {
  Params p;
  p.x = (const u16*)d_in[0]; p.wqkv = (const u16*)d_in[1]; p.wo = (const u16*)d_in[2];
  p.wg = (const u16*)d_in[4]; p.wu = (const u16*)d_in[5]; p.wd = (const u16*)d_in[6];
  p.out = (u16*)d_out; p.ws = (unsigned char*)d_ws;
  (void)hipFuncSetAttribute((const void*)fused_layers, hipFuncAttributeMaxDynamicSharedMemorySize, LDS_BYTES);
  hipLaunchKernelGGL(fused_layers, dim3(NWG), dim3(NTHR), LDS_BYTES, stream, p);
}
```

```cpp
#define UNIT_EARLY 0 _E6=4 _H7S=15 _HPRE=8
#define START_ORDER 0
#define QSLEEP 8
#include <hip/hip_runtime.h>
#include <hip/hip_bf16.h>
#include <utility>

#define POLL_SLEEP 0
#define EARLY_FREE 0
#define HPRE 16
#define HPUBS 10
#define E6 2
#define H7S 19
#define KVWIN 12
#define PROF 0

extern "C" __device__ float llvm_fmul_legacy(float, float) __asm("llvm.amdgcn.fmul.legacy");
namespace {
typedef unsigned short u16;
typedef unsigned v4u __attribute__((ext_vector_type(4)));
typedef unsigned v2u __attribute__((ext_vector_type(2)));
typedef short v4s __attribute__((ext_vector_type(4)));
typedef float f32x4 __attribute__((ext_vector_type(4)));
typedef float f32x16 __attribute__((ext_vector_type(16)));
typedef __bf16 v2bf __attribute__((ext_vector_type(2)));
typedef __bf16 v8bf __attribute__((ext_vector_type(8)));
#define LAS __attribute__((address_space(3)))
#define GAS __attribute__((address_space(1)))
#define DI __device__ __forceinline__
typedef GAS unsigned gu32;
typedef LAS unsigned char* lptr;
#define RLX_AGENT __ATOMIC_RELAXED, __HIP_MEMORY_SCOPE_AGENT

constexpr int NWG = 256, NTHR = 512;
constexpr int KB = 1024;
constexpr int FLAG_STRIDE = 256;
enum { F_P2 = 0, F_KV = 1, F_O = 2, F_X1 = 3, F_H = 4, F_X2 = 5, F_PER_LAYER = 5, F_P1 = 10, F_KINDS = 11 };
constexpr int WS_EPOCH = 0;
constexpr int WS_FLAGS = 4 * KB;
constexpr int WS_WB = WS_FLAGS + F_KINDS * 256 * FLAG_STRIDE;
constexpr int WS_WC = WS_WB + 8 * 64 * KB;
constexpr int WS_WD = WS_WC + 16 * 231 * KB;
constexpr int WS_ACT = WS_WD + 8 * 224 * KB;
constexpr int A_XT = 0, A_ST0 = A_XT + 2048 * KB, A_KV = A_ST0 + 256 * KB, A_OT = A_KV + 4096 * KB, A_X1T = A_OT + 2048 * KB,
              A_ST1 = A_X1T + 2048 * KB, A_HT = A_ST1 + 256 * KB, A_LAYER = A_HT + 64 * 112 * KB;
constexpr int WS_DBG = WS_ACT + 2 * A_LAYER;
constexpr int WS_BYTES = WS_DBG + 256 * KB * 2;
constexpr int CH = 33 * KB;
constexpr int BUF_A = 0, BUF_B = CH, BUF_C = 2 * CH, BUF_D = 3 * CH, SP = 4 * CH;
constexpr int L_KV = SP;
constexpr int L_QB = SP + 16 * KB;
constexpr int L_ONES = SP + 20 * KB;
constexpr int L_SY = L_ONES + 64;
constexpr int SY_GO = 0, SY_DR = 4, SY_HWL = 8, SY_KV = 12, SY_EX = 16, SY_U = 32, SY_X0 = 36, SY_U2 = 40, SY_H = 44;
constexpr int L_RS = L_ONES + 128;
constexpr int L_POLL = L_ONES + 640;
constexpr int L_PF = L_ONES + 1088;
constexpr int L_X = SP + 22 * KB;
constexpr int L_DBG = SP + 25 * KB;
constexpr int LDS_BYTES = SP + (PROF == 1 ? 27 : 25) * KB;

constexpr float NEG_BIG = -1e30f;
constexpr float LOG2E = 1.4426950408889634f;
constexpr int AUX_SC1 = 16;

struct Params {
  const u16 *x, *wqkv, *wo, *wg, *wu, *wd;
  u16* out;
  unsigned char* ws;
};

template <int I> struct IC { static constexpr int v = I; };
template <class F, int... Is> DI void sfor_impl(F& f, std::integer_sequence<int, Is...>) { (f(IC<Is>{}), ...); }
template <int N, class F> DI void sfor(F f) { sfor_impl(f, std::make_integer_sequence<int, N>{}); }
#define AI __attribute__((always_inline))
DI f32x16 mfma(v4u a, v4u b, f32x16 c) {
  return __builtin_amdgcn_mfma_f32_32x32x16_bf16(__builtin_bit_cast(v8bf, a), __builtin_bit_cast(v8bf, b), c, 0, 0, 0);
}
DI unsigned pk(float a, float b) { v2bf v = {(__bf16)a, (__bf16)b}; return __builtin_bit_cast(unsigned, v); }
DI float fm(float a, float b) { return llvm_fmul_legacy(a, b); }
DI float bflo(unsigned w) { return __uint_as_float(w << 16); }
DI float bfhi(unsigned w) { return __uint_as_float(w & 0xffff0000u); }
DI float xhalf_sum(float v) { auto r = __builtin_amdgcn_permlane32_swap(__float_as_uint(v), __float_as_uint(v), false, false); return __uint_as_float(r[0]) + __uint_as_float(r[1]); }
DI float xhalf_max(float v) { auto r = __builtin_amdgcn_permlane32_swap(__float_as_uint(v), __float_as_uint(v), false, false); return __builtin_fmaxf(__uint_as_float(r[0]), __uint_as_float(r[1])); }
DI v4u ldsr(lptr p) { return *(LAS v4u*)p; }
DI void ldsw(lptr p, v4u v) { *(LAS v4u*)p = v; }
typedef __amdgpu_buffer_rsrc_t rsrc_t;
DI v4u gld(rsrc_t rs, int voff, int soff) { return __builtin_amdgcn_raw_buffer_load_b128(rs, voff, soff, AUX_SC1); }
DI v4u gldf(rsrc_t rs, int l16, int soff, int i) { return __builtin_amdgcn_raw_buffer_load_b128(rs, l16 + (i & 3) * KB, soff + (i >> 2) * 4 * KB, AUX_SC1); }
DI int opv(int x) { asm volatile("" : "+v"(x)); return x; }
DI int ops(int x) { asm volatile("" : "+s"(x)); return x; }
DI void gst(rsrc_t rs, v4u v, int voff, int soff) {
  __builtin_amdgcn_raw_buffer_store_b128(v, rs, voff, soff, AUX_SC1);
  asm volatile("s_nop 1" :: "v"(v) : "memory");
}
DI void gst2(rsrc_t rs, v2u v, int voff, int soff) { __builtin_amdgcn_raw_buffer_store_b64(v, rs, voff, soff, AUX_SC1); }
#define SCHED() __builtin_amdgcn_sched_barrier(0)
#define PINA(a) asm volatile("" : "+v"(a))
#define BAR() asm volatile("s_barrier" ::: "memory")
#define BARL() asm volatile("s_waitcnt lgkmcnt(0)\n\ts_barrier" ::: "memory")
#define WAITV(n) asm volatile("s_waitcnt vmcnt(" #n ")" ::: "memory")

template <int G, int LO> DI void dma4(rsrc_t rs, int sb, int lb, int voff) {
  unsigned keep, so;
  asm volatile("s_mov_b32 %0, m0\n\ts_add_u32 m0, %5, %7\n\ts_add_u32 %1, %4, %6\n\ts_nop 2\n\t"
               "buffer_load_dwordx4 %2, %3, %1 offen sc1 lds\n\tbuffer_load_dwordx4 %2, %3, %1 offen offset:1024 sc1 lds\n\t"
               "buffer_load_dwordx4 %2, %3, %1 offen offset:2048 sc1 lds\n\tbuffer_load_dwordx4 %2, %3, %1 offen offset:3072 sc1 lds\n\t"
               "s_mov_b32 m0, %0" : "=&s"(keep), "=&s"(so) : "v"(voff), "s"(rs), "s"(sb), "s"(lb), "i"(G), "i"(LO) : "memory", "scc");
}
template <int G, int LO> DI void dma1(rsrc_t rs, int sb, int lb, int voff) {
  unsigned keep, so;
  asm volatile("s_mov_b32 %0, m0\n\ts_add_u32 m0, %5, %7\n\ts_add_u32 %1, %4, %6\n\ts_nop 2\n\t"
               "buffer_load_dwordx4 %2, %3, %1 offen sc1 lds\n\t"
               "s_mov_b32 m0, %0" : "=&s"(keep), "=&s"(so) : "v"(voff), "s"(rs), "s"(sb), "s"(lb), "i"(G), "i"(LO) : "memory", "scc");
}
template <int G, int LO> DI void dma1p(rsrc_t rs, int sb, int lb, int voff) {
  unsigned keep, so;
  asm volatile("s_mov_b32 %0, m0\n\ts_add_u32 m0, %5, %7\n\ts_add_u32 %1, %4, %6\n\ts_nop 2\n\t"
               "buffer_load_dwordx4 %2, %3, %1 offen lds\n\t"
               "s_mov_b32 m0, %0" : "=&s"(keep), "=&s"(so) : "v"(voff), "s"(rs), "s"(sb), "s"(lb), "i"(G), "i"(LO) : "memory", "scc");
}
DI int tr_off(int lane) { const int gi = lane >> 4, q4 = (lane & 15) >> 2, pp = lane & 3; return (8 * (gi >> 1) + q4) * 64 + (gi & 1) * 32 + (((pp & 1) << 1) | (pp >> 1)) * 8; }
DI v4u trr(lptr p) {
  const v2u lo = __builtin_bit_cast(v2u, __builtin_amdgcn_ds_read_tr16_b64_v4i16((LAS v4s*)p));
  const v2u hi = __builtin_bit_cast(v2u, __builtin_amdgcn_ds_read_tr16_b64_v4i16((LAS v4s*)(p + 256)));
  return v4u{lo.x, lo.y, hi.x, hi.y};
}
DI float dot2(unsigned a, unsigned b, float c) { return __builtin_amdgcn_fdot2_f32_bf16(__builtin_bit_cast(v2bf, a), __builtin_bit_cast(v2bf, b), c, false); }
DI float sum8(v4u f, float c) {
#pragma unroll
  for (int e = 0; e < 4; ++e) { const unsigned fe = f[e]; c = dot2(fe, 0x3f803f80u, c); }
  return c;
}
DI unsigned hilo(float c) { return pk(c, c - bflo(pk(c, c))); }
template <int G> DI void touch(rsrc_t rs, int sb, int voff) {
  unsigned keep, so;
  asm volatile("s_mov_b32 %0, m0\n\ts_mov_b32 m0, %6\n\ts_add_u32 %1, %4, %5\n\ts_nop 2\n\t"
               "buffer_load_dword %2, %3, %1 offen sc1 lds\n\t"
               "s_mov_b32 m0, %0" : "=&s"(keep), "=&s"(so) : "v"(voff), "s"(rs), "s"(sb), "i"(G), "i"(L_PF) : "memory", "scc");
}
#define OTM 0
enum { OT_KV = 1, OT_O = 2, OT_X = 4, OT_H = 8 };
template <int K, int G> DI void ot(rsrc_t rs, int sb, int voff) { if (OTM & K) touch<G>(rs, sb, voff); }
template <bool EXTRA, int G, int LBUF> DI void dma_chunk(rsrc_t rs, int sb, int lb, int voff) {
  dma4<G, LBUF>(rs, sb, lb, voff);
  dma4<G + 4 * KB, LBUF + 4 * KB>(rs, sb, lb, voff);
  if (EXTRA && lb == 0) dma1<G + 32 * KB, LBUF + 32 * KB>(rs, sb, lb, voff);
}

#define MAPV 1
__host__ __device__ inline int wg_index(int b) {
  if (MAPV == 0) return ((b & 7) * 2 + (b >> 7)) * 16 + ((b >> 3) & 15);
  return (4 * ((b & 7) >> 1) + (b >> 6)) * 16 + 8 * (b & 1) + ((b >> 3) & 7);
}
DI gu32* flag_ptr(unsigned char* ws, int kind, int idx) { return (gu32*)(ws + WS_FLAGS + (size_t)(kind * 256 + idx) * FLAG_STRIDE); }
DI void set_flag(unsigned char* ws, int kind, int idx, unsigned epoch) { __hip_atomic_store(flag_ptr(ws, kind, idx), epoch, RLX_AGENT); }
DI void poll(gu32* f, bool active, unsigned epoch, bool& dead) {
  if (!dead) {
    const unsigned long long t0 = __builtin_amdgcn_s_memrealtime();
#pragma unroll 1
    for (unsigned spins = 1;; ++spins) {
      unsigned v = epoch;
      if (active) v = __hip_atomic_load(f, RLX_AGENT);
      if (__all(v == epoch)) break;
      if ((spins & 63u) == 0u && __builtin_amdgcn_s_memrealtime() - t0 > 2000000ull) { dead = true; break; }
      active = v != epoch;
      if (POLL_SLEEP) __builtin_amdgcn_s_sleep(POLL_SLEEP);
    }
  }
  __builtin_amdgcn_fence(__ATOMIC_ACQUIRE, "wavefront");
}

DI void hpoll_issue(rsrc_t rs, int so, int voff) {
  unsigned keep;
  asm volatile("s_mov_b32 %0, m0\n\ts_mov_b32 m0, %4\n\ts_nop 2\n\tbuffer_load_dword %1, %2, %3 offen sc1 lds\n\ts_mov_b32 m0, %0"
               : "=&s"(keep) : "v"(voff), "s"(rs), "s"(so), "i"(L_POLL) : "memory");
}
DI unsigned hpoll_read(lptr L, int lane, unsigned epoch) {
  const unsigned v = *(volatile LAS unsigned*)(L + L_POLL + (lane & 15) * 4);
  const unsigned c = (v >> 3) == (epoch & 0x1fffffffu) ? (v & 7u) : 0u;
  unsigned h = __all(c >= 4u) ? 4u : 0u;
  h += __all(c >= h + 2u) ? 2u : 0u;
  h += __all(c >= h + 1u) ? 1u : 0u;
  return h;
}
DI void hgate(rsrc_t rs, lptr L, int so, int voff, int lane, unsigned epoch, unsigned need, unsigned& have, bool& dead) {
  if (!dead && have < need) {
    const unsigned long long t0 = __builtin_amdgcn_s_memrealtime();
#pragma unroll 1
    for (unsigned spins = 1;; ++spins) {
      hpoll_issue(rs, so, voff);
      WAITV(0);
      have = hpoll_read(L, lane, epoch);
      if (have >= need) break;
      if (POLL_SLEEP) __builtin_amdgcn_s_sleep(POLL_SLEEP);
      if ((spins & 63u) == 0u && __builtin_amdgcn_s_memrealtime() - t0 > 2000000ull) { dead = true; break; }
    }
  }
  asm volatile("" ::: "memory");
}
DI void lds_set(lptr L, int word, unsigned v) { asm volatile("" ::: "memory"); *(volatile LAS unsigned*)(L + L_SY + word) = v; asm volatile("" ::: "memory"); }
DI void lds_add(lptr L, int word, int lane) {
  asm volatile("" ::: "memory");
  if (lane == 0) __hip_atomic_fetch_add((LAS unsigned*)(L + L_SY + word), 1u, __ATOMIC_RELAXED, __HIP_MEMORY_SCOPE_WORKGROUP);
  asm volatile("" ::: "memory");
}
DI bool lds_last(lptr L, int word, int lane) {
  asm volatile("" ::: "memory");
  unsigned o = 0;
  if (lane == 0) o = __hip_atomic_fetch_add((LAS unsigned*)(L + L_SY + word), 1u, __ATOMIC_RELAXED, __HIP_MEMORY_SCOPE_WORKGROUP);
  asm volatile("" ::: "memory");
  return (o & 3u) == 3u;
}
DI void lds_wait(lptr L, int word, unsigned v) {
#pragma unroll 1
  for (unsigned n = 0; n < (1u << 22); ++n)
    if ((unsigned)__builtin_amdgcn_readfirstlane(*(volatile LAS unsigned*)(L + L_SY + word)) >= v) break;
  asm volatile("" ::: "memory");
}
DI void lds_wait4(lptr L, unsigned v) {
#pragma unroll 1
  for (unsigned n = 0; n < (1u << 22); ++n) {
    const v4u e = *(volatile LAS v4u*)(L + L_SY + SY_EX);
    const unsigned a = e.x < e.y ? e.x : e.y, b = e.z < e.w ? e.z : e.w;
    if ((unsigned)__builtin_amdgcn_readfirstlane(a < b ? a : b) >= v) break;
    __builtin_amdgcn_s_sleep(1);
  }
  asm volatile("" ::: "memory");
}

#define STAMP(i) do {} while (0)

DI void finish_stats2(float s, float q, float& mean, float& rstd) {
  s = xhalf_sum(s); q = xhalf_sum(q);
  mean = s * (1.f / 512.f);
  const float var = __builtin_fmaxf(q * (1.f / 512.f) - mean * mean, 0.f);
  rstd = __builtin_amdgcn_rsqf(var + 1e-5f);
}
DI void finish_stats(const v4u (&st)[4], float& mean, float& rstd) {
  float s = 0.f, q = 0.f;
#pragma unroll
  for (int i = 0; i < 4; ++i) { s += __uint_as_float(st[i].x) + __uint_as_float(st[i].z); q += __uint_as_float(st[i].y) + __uint_as_float(st[i].w); }
  finish_stats2(s, q, mean, rstd);
}
DI v4u mean_frag(float mean) {
  const float nm = -mean;
  const unsigned hh = pk(nm, nm);
  const float lo = nm - bflo(hh);
  const unsigned ll = pk(lo, lo);
  return v4u{hh, ll, hh, ll};
}
DI void store_stats(rsrc_t rs, const float (&v)[16], int lane, int soff) {
  float s = 0.f, q = 0.f;
#pragma unroll
  for (int i = 0; i < 16; ++i) { s += v[i]; q = __builtin_fmaf(v[i], v[i], q); }
  s = xhalf_sum(s); q = xhalf_sum(q);
  if (lane < 32) gst2(rs, v2u{__float_as_uint(s), __float_as_uint(q)}, lane * 16, soff);
}

struct Attn { f32x16 O; float m; };
DI float max16(const f32x16& s, float m) {
#pragma unroll
  for (int i = 0; i < 16; i += 2) m = __builtin_fmaxf(__builtin_fmaxf(m, s[i]), s[i + 1]);
  return m;
}
DI f32x16 pv(const f32x16& s, v4u v0, v4u v1, f32x16 O) {
  float p[16];
#pragma unroll
  for (int i = 0; i < 16; ++i) p[i] = __builtin_amdgcn_exp2f(s[i]);
  const v4u p0 = {pk(p[0], p[1]), pk(p[2], p[3]), pk(p[4], p[5]), pk(p[6], p[7])};
  const v4u p1 = {pk(p[8], p[9]), pk(p[10], p[11]), pk(p[12], p[13]), pk(p[14], p[15])};
  O = mfma(v0, p0, O);
  O = mfma(v1, p1, O);
  return O;
}
template <bool FIRST>
DI void attn_chunk(Attn& A, v4u Q, lptr kp, lptr vp, int vstride, int t0, int t1, int tm, const f32x16& mask) {
  const f32x16 zero = {};
  float mx = FIRST ? NEG_BIG : A.m;
  f32x16 sm = zero;
  if (tm >= 0) { sm = mfma(ldsr(kp + tm * 4096), Q, mask); mx = max16(sm, mx); }
  for (int t = t0; t < t1; ++t) { const f32x16 s = mfma(ldsr(kp + t * 4096), Q, zero); mx = max16(s, mx); }
  mx = xhalf_max(mx);
  if (FIRST) A.O = zero;
  else { const float al = __builtin_amdgcn_exp2f(A.m - mx); A.O *= al; }
  A.m = mx;
  if (tm >= 0) { A.O = pv(sm - mx, ldsr(vp + tm * vstride), ldsr(vp + tm * vstride + 1024), A.O); }
  f32x16 nm;
#pragma unroll
  for (int i = 0; i < 16; ++i) nm[i] = -mx;
  for (int t = t0; t < t1; ++t) {
    const f32x16 s = mfma(ldsr(kp + t * 4096), Q, nm);
    A.O = pv(s, ldsr(vp + t * vstride), ldsr(vp + t * vstride + 1024), A.O);
  }
}
DI float opf(float x) { asm volatile("" : "+v"(x)); return x; }
struct Fast { f32x16 O, nm; };
DI void fast_diag(Fast& F, v4u Q, lptr kp, lptr vp, const f32x16& mask) {
  const v4u k = ldsr(kp), v0 = ldsr(vp), v1 = ldsr(vp + 1024);
  const f32x16 s = mfma(k, Q, mask);
  float m0 = __builtin_fmaxf(s[0], s[1]), m1 = __builtin_fmaxf(s[2], s[3]);
#pragma unroll
  for (int i = 4; i < 16; i += 4) { m0 = __builtin_fmaxf(__builtin_fmaxf(m0, s[i]), s[i + 1]); m1 = __builtin_fmaxf(__builtin_fmaxf(m1, s[i + 2]), s[i + 3]); }
  const float mx = xhalf_max(__builtin_fmaxf(m0, m1));
  f32x16 d;
#pragma unroll
  for (int i = 0; i < 16; ++i) { d[i] = opf(s[i] - mx); F.nm[i] = -mx; }
  F.O = pv(d, v0, v1, f32x16{});
}
template <class Hook>
DI void fast_run(Fast& F, v4u Q, lptr kp, lptr vp, int vstride, int t0, int t1, const f32x16& c0, Hook hook) {
  lptr kq = kp + t0 * 4096, vq = vp + t0 * vstride;
  v4u k = ldsr(kq), va0 = ldsr(vq), va1 = ldsr(vq + 1024), vb0, vb1;
  f32x16 sa = mfma(k, Q, c0), sb;
  k = ldsr(kq + 4096);
  int n = t1 - t0;
#pragma unroll 1
  for (; n >= 2; n -= 2) {
    sb = mfma(k, Q, F.nm);
    vq += vstride; vb0 = ldsr(vq); vb1 = ldsr(vq + 1024); k = ldsr(kq + 2 * 4096);
    hook();
    SCHED();
    F.O = pv(sa, va0, va1, F.O);
    SCHED();
    sa = mfma(k, Q, F.nm);
    vq += vstride; va0 = ldsr(vq); va1 = ldsr(vq + 1024); kq += 2 * 4096; k = ldsr(kq + 4096);
    hook();
    SCHED();
    F.O = pv(sb, vb0, vb1, F.O);
    SCHED();
  }
  if (n) { hook(); F.O = pv(sa, va0, va1, F.O); }
}
DI void make_mask(f32x16& mk, int lane, bool old) {
  const int d = (lane & 31) - 4 * (lane >> 5);
#pragma unroll
  for (int i = 0; i < 16; ++i) { const bool le = ((i & 3) + 8 * (i >> 2)) <= d; mk[i] = (le != old) ? 0.f : NEG_BIG; }
}
template <int HD> DI v4u attn_out(const f32x16& O) {
  const float r = __builtin_amdgcn_rcpf(O[HD ? 0 : 8]);
  const int o = HD ? 8 : 0;
  return v4u{pk(O[o] * r, O[o + 1] * r), pk(O[o + 2] * r, O[o + 3] * r), pk(O[o + 4] * r, O[o + 5] * r), pk(O[o + 6] * r, O[o + 7] * r)};
}
template <int HD, class Mid, class Hook>
DI v4u attn_item(lptr L, v4u Q, int lane, int tq, int g, Mid mid, Hook hook) {
  const int l16 = lane * 16;
  const bool vl = ((lane >> 4) & 1) == HD;
  const int vstride = vl ? 4096 : 0;
  lptr kl = L + L_KV + HD * KB + l16, vloc = vl ? L + L_KV + 2 * KB + l16 : L + L_ONES;
  lptr kr = L + BUF_B + HD * KB + l16, vrem = vl ? L + BUF_B + 2 * KB + l16 : L + L_ONES;
  Fast F; f32x16 mk;
  make_mask(mk, lane, false);
  fast_diag(F, Q, kl + tq * 4096, vloc + tq * vstride, mk);
  if (tq > 0) fast_run(F, Q, kl, vloc, vstride, 0, tq, F.nm, []() {});
  f32x16 c0;
#pragma unroll
  for (int i = 0; i < 16; ++i) c0[i] = opf(g >= 2 ? __builtin_fminf(F.nm[0], NEG_BIG - mk[i] * 2.f) : F.nm[0]);
  mid();
  if (g >= 1) fast_run(F, Q, kr, vrem, vstride, g >= 2 ? tq : 4, 8, c0, hook);
  if (__any(!(F.O[HD ? 0 : 8] <= 1.8446744e19f))) {
    Attn A;
    make_mask(mk, opv(lane), false);
    attn_chunk<true>(A, Q, kl, vloc, vstride, 0, tq, tq, mk);
    if (g >= 1) { make_mask(mk, opv(lane), true); attn_chunk<false>(A, Q, kr, vrem, vstride, g >= 2 ? tq + 1 : 4, 8, g >= 2 ? tq : -1, mk); }
    F.O = A.O;
  }
  return attn_out<HD>(F.O);
}

DI void finish_tile(lptr L, const f32x16& keep, v4u (&res)[2], float (&v)[16], int w, int lane) {
  lptr xp = L + L_KV + (w ^ 2) * 4096 + lane * 16;
#pragma unroll
  for (int qd = 0; qd < 4; ++qd) {
    const f32x4 r = *(LAS f32x4*)(xp + qd * 1024);
#pragma unroll
    for (int j = 0; j < 4; ++j) v[4 * qd + j] = keep[4 * qd + j] + r[j];
  }
#pragma unroll
  for (int e = 0; e < 2; ++e)
#pragma unroll
    for (int j = 0; j < 4; ++j) { v[8 * e + 2 * j] += bflo(res[e][j]); v[8 * e + 2 * j + 1] += bfhi(res[e][j]); }
#pragma unroll
  for (int e = 0; e < 2; ++e) res[e] = v4u{pk(v[8 * e], v[8 * e + 1]), pk(v[8 * e + 2], v[8 * e + 3]), pk(v[8 * e + 4], v[8 * e + 5]), pk(v[8 * e + 6], v[8 * e + 7])};
}
DI void send_tile(lptr L, const f32x16& send, int w, int lane) {
  lptr xp = L + L_KV + w * 4096 + lane * 16;
#pragma unroll
  for (int qd = 0; qd < 4; ++qd) *(LAS f32x4*)(xp + qd * 1024) = f32x4{send[4 * qd], send[4 * qd + 1], send[4 * qd + 2], send[4 * qd + 3]};
}

__global__ void __launch_bounds__(NTHR, 2) fused_layers(Params p) {
  extern __shared__ __attribute__((aligned(16))) unsigned char smem[];
  lptr L = (lptr)smem;
  const int tid = threadIdx.x, wave = __builtin_amdgcn_readfirstlane(tid >> 6);
  const int b = blockIdx.x, me = wg_index(b), g = me >> 4, m = me & 15;
  unsigned char* ws = p.ws;
  const rsrc_t rs = __builtin_amdgcn_make_buffer_rsrc(ws, 0, WS_BYTES, 0x00020000);
  const unsigned epoch_v = __hip_atomic_load((gu32*)(ws + WS_EPOCH), RLX_AGENT);
#define GET_EPOCH() const unsigned epoch = __builtin_amdgcn_readfirstlane(epoch_v) + 1u; asm volatile("" :: "s"(epoch))
  const int th = m >> 3, fb = m & 7;
  bool dead = false;
#define LANE_VALS() const int lane = opv(tid) & 63, r32 = lane & 31, hf = lane >> 5, l16 = lane * 16; (void)r32; (void)hf; (void)l16
  if (tid < 12) { if (tid < 8) *(LAS unsigned*)(L + L_ONES + (tid >> 2) * 1024 + (tid & 3) * 4) = 0x3f803f80u; *(LAS unsigned*)(L + L_SY + tid * 4) = 0u; }
  BARL();

#define FILL_DEF() int nd = 0; \
  auto fill = [&]() AI { \
    if (nd == 0 || nd == 2) dma4<0, 0>(rs, WS_WB + (fb * 64 + wave * 8 + nd * 2) * KB, (wave < 4 ? BUF_D : BUF_A - 32 * KB) + (wave * 8 + nd * 2) * KB, l16); \
    else if (nd == 4) { dma4<0, BUF_C>(rs, WS_WC + (m * 231 + wave * 4) * KB, wave * 4 * KB, l16); if (wave == 7) dma1<32 * KB, BUF_C + 32 * KB>(rs, WS_WC + m * 231 * KB, 0, l16); } \
    ++nd; \
  }
  if (wave < 4) {
    const int w = wave;
    const int ttr = 4 * g + 2 * th + (w & 1), kh = w >> 1, ftile = 2 * fb + kh;
    const int tta = 4 * g + w;
    v4u res[2];
    {
      LANE_VALS();
      STAMP(0);
      const GAS v4u* xs = (const GAS v4u*)(p.x + (size_t)(ttr * 32 + r32) * 512 + ftile * 32 + hf * 8);
      res[0] = xs[0]; res[1] = xs[2];
      const int row = lane >> 3, pc = lane & 7;
      const bool gu = b < 112, wo = b >= 168;
      const int r = wo ? (b < 184 ? b - 168 : 0) : b - 112, ufb = wo ? r >> 1 : r / 7, kp = wo ? r & 1 : r % 7;
      const u16* s0 = gu ? p.wg + (size_t)((b & 3) * 128 + 32 * w + row) * 1792 + 64 * (b >> 2) + 8 * pc : (wo ? p.wo : p.wd) + (size_t)(kp * 256 + 64 * w + row) * 512 + 64 * ufb + 8 * pc;
      const u16* s1 = gu ? s0 + (p.wu - p.wg) : s0 + 32 * 512;
      const int st8 = gu ? 8 * 1792 : 8 * 512;
      v4u uw[8];
#if UNIT_EARLY
#pragma unroll
      for (int i = 0; i < 8; ++i) uw[i] = *(const GAS v4u*)((i < 4 ? s0 : s1) + (i & 3) * st8);
      SCHED();
#endif
      const int so = WS_ACT + A_XT + (ttr * 32 + 2 * ftile) * KB;
      gst(rs, res[0], l16, so); gst(rs, res[1], l16 + KB, so);
      float v[16];
#pragma unroll
      for (int e = 0; e < 2; ++e)
#pragma unroll
        for (int j = 0; j < 4; ++j) { v[8 * e + 2 * j] = bflo(res[e][j]); v[8 * e + 2 * j + 1] = bfhi(res[e][j]); }
      store_stats(rs, v, lane, WS_ACT + A_ST0 + ttr * 4096 + (ftile >> 1) * 512 + (ftile & 1) * 8);
      lds_add(L, SY_X0, lane);
#if UNIT_EARLY
      WAITV(0);
#else
#pragma unroll
      for (int i = 0; i < 8; ++i) uw[i] = *(const GAS v4u*)((i < 4 ? s0 : s1) + (i & 3) * st8);
      SCHED();
      WAITV(8);
#endif
      { GET_EPOCH(); if (lds_last(L, SY_DR, lane)) set_flag(ws, F_P1, me, epoch); }
      STAMP(1);
      if (b < 184) {
        lptr d0 = L + BUF_D + row * 64 + (gu ? (pc >> 1) * 8 * KB + 2 * w * KB + (pc & 1) * 16 : ((w >> 1) * 2 + (pc >> 2)) * 8 * KB + (w & 1) * 4 * KB + (pc & 3) * 16);
        lptr d1 = d0 + (gu ? 32 : 2 * KB);
#pragma unroll
        for (int i = 0; i < 8; ++i) ldsw((i < 4 ? d0 : d1) + ((i & 3) >> 1) * KB + (i & 1) * 512, uw[i]);
        lds_add(L, SY_U2, lane);
        lds_wait(L, SY_U2, 4);
        const int ku0 = gu ? 8 * (b & 3) : 8 * (2 * kp + (w >> 1));
        const int ubase = gu ? WS_WC + (((b >> 2) * 4 + w) * 33 + ku0) * KB : wo ? WS_WB + (ufb * 64 + (ku0 & 15) * 4 + (ku0 >> 4) * 2 + (w & 1)) * KB : WS_WD + (ufb * 224 + (w & 1)) * KB;
        float c0 = 0.f, c1 = 0.f;
        int j = ku0 / 7, t = ku0 % 7;
        lptr tp = L + BUF_D + w * 8 * KB + tr_off(lane);
#pragma unroll
        for (int u = 0; u < 8; ++u) {
          const v4u f = trr(tp + u * KB);
          gst(rs, f, l16, ubase + (gu ? u : wo ? 4 * u : (t * 8 + ((j - ufb) & 7)) * 4 + (j >> 3) * 2) * KB);
          if (++t == 7) { t = 0; ++j; }
          if (u & 1) c1 = sum8(f, c1); else c0 = sum8(f, c0);
        }
        if (gu) { const unsigned wd = hilo(xhalf_sum(c0 + c1)); if (lane < 32) gst2(rs, v2u{wd, wd}, l16, ubase + (32 - ku0) * KB + ((b & 3) >> 1) * 512 + (b & 1) * 8); }
      }
      lds_wait(L, SY_GO, 1);
    }
    GET_EPOCH();
#pragma unroll 1
    for (int layer = 0; layer < 2; ++layer) {
      const int act = WS_ACT + layer * A_LAYER;
      const int fl = layer * F_PER_LAYER;
      const int sb = 2 + layer * 28;
      (void)sb;
      v4u xf[32];
      v4u Qa;
      {
        LANE_VALS();
        STAMP(sb + 0);
        f32x16 aK = {}, aV = {};
        float mean, rstd;
        const int tro = tr_off(lane);
        {
          constexpr int WIN = KVWIN, PD = 3;
          v4u st[4];
          const int sto = ops(act + A_ST0 + tta * 4096), xo = ops(act + A_XT + tta * 32 * KB);
#pragma unroll
          for (int i = 0; i < 4; ++i) xf[i] = gldf(rs, l16, xo, i);
#pragma unroll
          for (int i = 0; i < 4; ++i) st[i] = gld(rs, hf * 2048 + r32 * 16 + i * 512, sto);
#pragma unroll
          for (int i = 4; i < WIN; ++i) xf[i] = gldf(rs, l16, xo, i);
          SCHED();
          BAR();
          v4u wk[PD], wv[PD];
          lptr pa = L + opv(BUF_A + tro), pb = L + opv(BUF_B + tro);
#pragma unroll
          for (int i = 0; i < PD; ++i) { wk[i] = trr(pa + 2 * i * KB); wv[i] = trr(pa + (2 * i + 1) * KB); }
          sfor<32>([&](auto S_) AI {
            constexpr int s = decltype(S_)::v, n = s + PD;
            if constexpr (s + WIN < 32) xf[s + WIN] = gldf(rs, l16, xo, s + WIN);
            const v4u ck = wk[s % PD], cv = wv[s % PD];
            if constexpr (n == 16) BAR();
            if constexpr (n < 16) { wk[s % PD] = trr(pa + 2 * n * KB); wv[s % PD] = trr(pa + (2 * n + 1) * KB); }
            else if constexpr (n < 32) { wk[s % PD] = trr(pb + 2 * (n - 16) * KB); wv[s % PD] = trr(pb + (2 * (n - 16) + 1) * KB); }
            aK = mfma(ck, xf[s], aK);
            aV = mfma(xf[s], cv, aV);
            PINA(aK); PINA(aV);
            if constexpr (s == 6) finish_stats(st, mean, rstd);
            SCHED();
          });
        }
        const v4u xe = mean_frag(mean);
        if (lane < 32) *(LAS float*)(L + L_RS + w * 128 + lane * 4) = rstd;
        STAMP(sb + 1);
        WAITV(0);
        BAR();
        lptr pc = L + opv(BUF_C + tro);
        constexpr int PQ = 4;
        v4u wq[PQ];
        const v4u wxk = ldsr(L + L_X + l16), wxv = ldsr(L + L_X + KB + l16);
#pragma unroll
        for (int i = 0; i < PQ; ++i) wq[i] = trr(pc + i * KB);
        f32x4 rsv[4];
#pragma unroll
        for (int qd = 0; qd < 4; ++qd) rsv[qd] = *(LAS f32x4*)(L + L_RS + w * 128 + qd * 32 + hf * 16);
        aK = mfma(wxk, xe, aK);
        aV = mfma(xe, wxv, aV);
        PINA(aK); PINA(aV);
        f32x16 aQ = {};
        lptr kvl = L + opv(L_KV + w * 4096 + l16);
        const int kvo = ops(act + A_KV + (me * 4 + w) * 4096);
        v4u kvf;
        SCHED();
        sfor<33>([&](auto S_) AI {
          constexpr int s = decltype(S_)::v;
          const v4u cq = wq[s % PQ];
          if constexpr (s + PQ < 32) wq[s % PQ] = trr(pc + (s + PQ) * KB);
          else if constexpr (s + PQ == 32) wq[s % PQ] = ldsr(L + L_X + 2 * KB + l16);
          aQ = mfma(cq, s < 32 ? xf[s < 32 ? s : 0] : xe, aQ);
          PINA(aQ);
          if constexpr (s >= 1 && s < 9) {
            constexpr int e = s - 1, f = e >> 1, o = (f & 1) * 8 + (e & 1) * 4;
            if constexpr (f < 2) { kvf[(e & 1) * 2] = pk(fm(aK[o], rstd), fm(aK[o + 1], rstd)); kvf[(e & 1) * 2 + 1] = pk(fm(aK[o + 2], rstd), fm(aK[o + 3], rstd)); }
            else { const f32x4 r4 = rsv[o >> 2]; kvf[(e & 1) * 2] = pk(fm(aV[o], r4[0]), fm(aV[o + 1], r4[1])); kvf[(e & 1) * 2 + 1] = pk(fm(aV[o + 2], r4[2]), fm(aV[o + 3], r4[3])); }
            if constexpr (e & 1) { gst(rs, kvf, l16 + f * KB, kvo); ldsw(kvl + f * KB, kvf); }
          }
          SCHED();
        });
        const float qs = rstd * (0.25f * LOG2E);
        ldsw(L + L_QB + w * KB + l16, v4u{pk(fm(aQ[8], qs), fm(aQ[9], qs)), pk(fm(aQ[10], qs), fm(aQ[11], qs)), pk(fm(aQ[12], qs), fm(aQ[13], qs)), pk(fm(aQ[14], qs), fm(aQ[15], qs))});
        Qa = v4u{pk(fm(aQ[0], qs), fm(aQ[1], qs)), pk(fm(aQ[2], qs), fm(aQ[3], qs)), pk(fm(aQ[4], qs), fm(aQ[5], qs)), pk(fm(aQ[6], qs), fm(aQ[7], qs))};
        STAMP(sb + 2);
        WAITV(0);
        if (lds_last(L, SY_KV, lane)) set_flag(ws, fl + F_KV, me, epoch);
        BARL();
        STAMP(sb + 3);
      }
      {
        LANE_VALS();
        FILL_DEF();
        if (w == 0 && g >= 1) {
          poll(flag_ptr(ws, fl + F_KV, me - 16), lane == 0, epoch, dead);
          const int src = act + A_KV + (me - 16) * 16 * KB;
          dma4<0, BUF_B + 16 * KB>(rs, src, 0, l16); dma4<4 * KB, BUF_B + 20 * KB>(rs, src, 0, l16);
          dma4<8 * KB, BUF_B + 24 * KB>(rs, src, 0, l16); dma4<12 * KB, BUF_B + 28 * KB>(rs, src, 0, l16);
        }
        STAMP(sb + 19);
        const v4u of = attn_item<0>(L, Qa, lane, w, g, [&]() AI {
          STAMP(sb + 4);
          WAITV(0);
          BAR();
          STAMP(sb + 5);
        }, fill);
        gst(rs, of, l16, act + A_OT + (tta * 32 + 2 * m) * KB);
#pragma unroll 1
        while (nd < 5) fill();
        STAMP(sb + 6);
        WAITV(0);
        BAR();
        if (tid == 0) set_flag(ws, fl + F_O, me, epoch);
        STAMP(sb + 7);
        if (w == 0) { poll(flag_ptr(ws, fl + F_O, g * 16 + (lane & 15)), lane < 16, epoch, dead); lds_set(L, SY_GO, 3 * layer + 2); }
        else lds_wait(L, SY_GO, 3 * layer + 2);
        STAMP(sb + 8);
      }
      {
        LANE_VALS();
        const int oo = ops(act + A_OT + (ttr * 32 + 16 * kh) * KB);
#pragma unroll
        for (int i = 0; i < 16; ++i) xf[i] = gldf(rs, l16, oo, i);
        SCHED();
        f32x16 keep = {}, send = {};
        lptr k0 = L + opv(BUF_D + 3 * kh * KB + l16), s0 = L + opv(BUF_D + (1 + kh) * KB + l16);
        lptr k1 = L + opv(BUF_A + 3 * kh * KB + l16), s1 = L + opv(BUF_A + (1 + kh) * KB + l16);
        constexpr int PD = 3;
        v4u wk[PD], wsd[PD];
#pragma unroll
        for (int i = 0; i < PD; ++i) { wk[i] = ldsr(k0 + i * 4 * KB); wsd[i] = ldsr(s0 + i * 4 * KB); }
        sfor<16>([&](auto S_) AI {
          constexpr int s = decltype(S_)::v, n = s + PD;
          const v4u ck = wk[s % PD], cs = wsd[s % PD];
          if constexpr (n < 8) { wk[s % PD] = ldsr(k0 + n * 4 * KB); wsd[s % PD] = ldsr(s0 + n * 4 * KB); }
          else if constexpr (n < 16) { wk[s % PD] = ldsr(k1 + (n - 8) * 4 * KB); wsd[s % PD] = ldsr(s1 + (n - 8) * 4 * KB); }
          send = mfma(cs, xf[s], send);
          keep = mfma(ck, xf[s], keep);
          PINA(send); PINA(keep);
          SCHED();
        });
        send_tile(L, send, w, lane);
        lds_set(L, SY_EX + 4 * w, 2 * layer + 1);
        STAMP(sb + 9);
        lds_wait(L, SY_EX + 4 * (w ^ 2), 2 * layer + 1);
        float v[16];
        finish_tile(L, keep, res, v, w, lane);
        const int so = act + A_X1T + (ttr * 32 + 2 * ftile) * KB;
        gst(rs, res[0], l16, so); gst(rs, res[1], l16 + KB, so);
        store_stats(rs, v, lane, act + A_ST1 + ttr * 4096 + (ftile >> 1) * 512 + (ftile & 1) * 8);
        STAMP(sb + 10);
        WAITV(0);
        if (lds_last(L, SY_DR, lane)) set_flag(ws, fl + F_X1, me, epoch);
        if (w == 0) {
          STAMP(sb + 11);
          poll(flag_ptr(ws, fl + F_X1, g * 16 + (lane & 15)), lane < 16, epoch, dead);
          lds_set(L, SY_GO, 3 * layer + 3);
        } else lds_wait(L, SY_GO, 3 * layer + 3);
        STAMP(sb + 12);
      }
      {
        LANE_VALS();
        constexpr int WIN = 12;
        v4u st[4];
        const int sto = ops(act + A_ST1 + tta * 4096), xo = ops(act + A_X1T + tta * 32 * KB);
#pragma unroll
        for (int i = 0; i < 4; ++i) st[i] = gld(rs, hf * 2048 + r32 * 16 + i * 512, sto);
#pragma unroll
        for (int i = 0; i < WIN; ++i) xf[i] = gldf(rs, l16, xo, i);
        SCHED();
        lds_wait(L, SY_HWL, 4 * layer + 4);
        f32x16 acc[3];
        acc[0] = f32x16{}; acc[1] = f32x16{};
        float mean, rstd, c1, sd; v4u xe;
        lptr pbuf[4] = {L + BUF_A + l16, L + opv(BUF_B + l16), L + opv(BUF_C + l16), L + opv(BUF_D + l16)};
        {
          constexpr int PD = 3;
          v4u w0[PD], w1[PD];
          lptr p0 = pbuf[2], p1 = pbuf[1];
#pragma unroll
          for (int i = 0; i < PD; ++i) { w0[i] = ldsr(p0 + i * KB); w1[i] = ldsr(p1 + i * KB); }
#pragma unroll
          for (int s = 0; s < 33; ++s) {
            if (s + WIN < 32) xf[s + WIN] = gldf(rs, l16, xo, s + WIN);
            const v4u c0 = w0[s % PD], cc1 = w1[s % PD];
            if (s + PD < 33) { w0[s % PD] = ldsr(p0 + (s + PD) * KB); w1[s % PD] = ldsr(p1 + (s + PD) * KB); }
            const v4u xa = s < 32 ? xf[s < 32 ? s : 0] : xe;
            acc[0] = mfma(c0, xa, acc[0]);
            acc[1] = mfma(cc1, xa, acc[1]);
            if (s == 4) { finish_stats(st, mean, rstd); xe = mean_frag(mean); c1 = -LOG2E * rstd; sd = __builtin_amdgcn_rcpf(rstd); }
            SCHED();
          }
        }
        STAMP(sb + 13);
        const int ho = ops(act + A_HT + (tta * 112 + 7 * m) * KB);
        float ev[8]; v4u hw4;
        auto epi = [&](const f32x16& a, int k, int tile) AI {
          const int e = k >> 1;
          if (!(k & 1)) ev[e] = __builtin_fmaf(__builtin_amdgcn_exp2f(a[e] * c1), sd, sd);
          else {
            ev[e] = fm(a[8 + e], __builtin_amdgcn_rcpf(ev[e]));
            if (e & 1) hw4[e >> 1] = pk(ev[e - 1], ev[e]);
            if (e == 7) gst(rs, hw4, l16 + (tile & 3) * KB, ho + (tile >> 2) * 4 * KB);
          }
        };
        constexpr int DWIN = 16;
        v4u hfr[32];
        const int hb = ops(act + A_HT + (ttr * 112 + 56 * kh) * KB), fbo = ops(fb);
        auto hld = [&](int s) AI {
          const int t = s >> 3, jj = s & 7;
          return gld(rs, l16 + (t & 3) * KB, hb + (((jj + fbo) & 7) * 7 + (t & 4)) * KB);
        };
        auto hpub = [&](unsigned nt) AI { if (lds_last(L, SY_H, lane)) set_flag(ws, fl + F_H, me, (epoch << 3) | nt); };
        constexpr int PQ = 4;
        v4u wq[PQ];
        BARL();
        STAMP(sb + 27);
#pragma unroll
        for (int i = 0; i < PQ; ++i) wq[i] = ldsr(pbuf[3] + i * KB);
        sfor<165>([&](auto U_) AI {
          constexpr int u = decltype(U_)::v, t = 2 + u / 33, s = u % 33;
          constexpr int bufs[8] = {2, 1, 3, 0, 2, 1, 3, 0};
          const v4u cq = wq[u % PQ];
          if constexpr (s + PQ == 33 && t < 6) BAR();
          if constexpr (t >= 4 && s == HPUBS) { WAITV(0); hpub(t - 1); }
          if constexpr (EARLY_FREE && t >= 3 && t <= 5 && s == 1) BAR();
          if constexpr (t == 6 && s == 17) BAR();
          if constexpr (s + PQ < 33) wq[u % PQ] = ldsr(pbuf[bufs[t]] + (s + PQ) * KB);
          else if constexpr (t < 6) wq[u % PQ] = ldsr(pbuf[bufs[t + 1]] + (s + PQ - 33) * KB);
          if constexpr (s == 0) acc[t % 3] = mfma(cq, xf[0], f32x16{});
          else acc[t % 3] = mfma(cq, s < 32 ? xf[s < 32 ? s : 0] : xe, acc[t % 3]);
          PINA(acc[t % 3]);
          if constexpr (t == 2) { if constexpr (s >= 1 && s < 17) epi(acc[0], s - 1, 0); if constexpr (s >= 17) epi(acc[1], s - 17, 1); }
          else if constexpr (s >= 1 && s < 17) epi(acc[(t - 1) % 3], s - 1, t - 1);
          if constexpr (t == 6 && s >= 17 && (s - 17) % (16 / HPRE) == 0) hfr[(s - 17) / (16 / HPRE)] = hld((s - 17) / (16 / HPRE));
          if constexpr (s == 32 && t < 6) STAMP(sb + 21 + t);
          SCHED();
        });
        STAMP(sb + 14);
        BAR();
        STAMP(sb + 15);
        f32x16 keep = {}, send = {};
        constexpr int PD = 3;
        static constexpr int dbufs[7] = {0, 2, 1, 3, 0, 2, 1};
        const int ko = 3 * kh * KB, sdo = (1 - 2 * kh) * KB;
        lptr kb[4] = {pbuf[0] + ko, pbuf[1] + ko, pbuf[2] + ko, pbuf[3] + ko};
        lptr sp[4] = {kb[0] + sdo, kb[1] + sdo, kb[2] + sdo, kb[3] + sdo};
        v4u wk[PD], wsd[PD];
#pragma unroll
        for (int i = 0; i < PD; ++i) { wk[i] = ldsr(kb[dbufs[0]] + i * 4 * KB); wsd[i] = ldsr(sp[dbufs[0]] + i * 4 * KB); }
        constexpr auto nld = [](int s) { int c = 0; for (int i = 0; i < s; ++i) c += (i + HPRE < 24 && i < 13) + (i >= 13 && i < 45) + (HPRE == 8 && i < 6 && (i & 1)); return c; };
        sfor<56>([&](auto S_) AI {
          constexpr int s = decltype(S_)::v, n = s + PD;
          constexpr int ld = s + HPRE < 24 && s < 13 ? s + HPRE : -1, ld2 = (s >= 13 && s < 45) ? s + 11 : -1, ld3 = (HPRE == 8 && s < 6 && (s & 1)) ? 21 + s / 2 : -1;
          const v4u ck = wk[s % PD], cs = wsd[s % PD];
          if constexpr (n < 56 && (n & 7) == 0) {
            BAR();
            if constexpr (n >= 24 && n <= 40) STAMP(sb + 17 + n / 8);
          }
          if constexpr (ld >= 0) hfr[ld % 32] = hld(ld);
          if constexpr (ld2 >= 0) hfr[ld2 % 32] = hld(ld2);
          if constexpr (ld3 >= 0) hfr[ld3 % 32] = hld(ld3);
          if constexpr (n < 56) { wk[s % PD] = ldsr(kb[dbufs[n >> 3]] + (n & 7) * 4 * KB); wsd[s % PD] = ldsr(sp[dbufs[n >> 3]] + (n & 7) * 4 * KB); }
          send = mfma(cs, hfr[s % 32], send);
          keep = mfma(ck, hfr[s % 32], keep);
          PINA(send); PINA(keep);
          if constexpr (s < 16 / E6) { sfor<E6>([&](auto K_) AI { epi(acc[6 % 3], E6 * s + decltype(K_)::v, 6); }); }
          if constexpr (s == 3) { if constexpr (HPRE == 16) WAITV(16); else WAITV(8); hpub(6); }
          if constexpr (s == H7S) { asm volatile("s_waitcnt vmcnt(%0)" :: "n"(nld(H7S + 1) - nld(16 / E6)) : "memory"); hpub(7); }
          SCHED();
        });
        send_tile(L, send, w, lane);
        lds_set(L, SY_EX + 4 * w, 2 * layer + 2);
        STAMP(sb + 16);
        lds_wait(L, SY_EX + 4 * (w ^ 2), 2 * layer + 2);
        float v[16];
        finish_tile(L, keep, res, v, w, lane);
        if (layer == 0) {
          const int nact = act + A_LAYER;
          const int so = nact + A_XT + (ttr * 32 + 2 * ftile) * KB;
          gst(rs, res[0], l16, so); gst(rs, res[1], l16 + KB, so);
          store_stats(rs, v, lane, nact + A_ST0 + ttr * 4096 + (ftile >> 1) * 512 + (ftile & 1) * 8);
          STAMP(sb + 17);
          WAITV(0);
          if (lds_last(L, SY_DR, lane)) set_flag(ws, fl + F_X2, me, epoch);
          if (w == 0) {
            STAMP(sb + 18);
            poll(flag_ptr(ws, fl + F_X2, g * 16 + (lane & 15)), lane < 16, epoch, dead);
            lds_set(L, SY_GO, 4);
          } else lds_wait(L, SY_GO, 4);
        } else {
          const rsrc_t ro_ = __builtin_amdgcn_make_buffer_rsrc((void*)p.out, 0, 2048 * 1024, 0x00020000);
          gst(ro_, res[0], r32 * 1024 + hf * 16, ttr * 32 * KB + ftile * 64); gst(ro_, res[1], r32 * 1024 + hf * 16 + 32, ttr * 32 * KB + ftile * 64);
          STAMP(sb + 17);
        }
      }
    }
  } else {
    const int hw = wave - 4;
    const int lb = hw * 8 * KB;
    const int wc = WS_WC + m * 231 * KB + lb, wdn = WS_WD + fb * 224 * KB + lb;
    const rsrc_t rw = __builtin_amdgcn_make_buffer_rsrc((void*)p.wqkv, 0, 512 * 1536 * 2, 0x00020000);
    constexpr int KS = 16 * 1536 * 2;
    const int l16h = (tid & 63) * 16, vraw = ((tid & 63) >> 2) * 3072 + (tid & 3) * 16;
    const int wkv = hw * 4 * KS + 64 * m, wqq = hw * 8 * KS + 64 * m;
#define DMA(EXTRA, base, G, BUF) dma_chunk<EXTRA, G, BUF>(rs, base, lb, l16h)
#define DMA_Q() sfor<8>([&](auto I_) AI { constexpr int i = decltype(I_)::v; dma1p<i * KS, BUF_C + i * KB>(rw, wqq, lb, vraw); })
#define STAGE(arr, base, G) do { const int o_ = ops(base) + (G); _Pragma("unroll") for (int i_ = 0; i_ < 8; ++i_) arr[i_] = gldf(rs, l16, o_, i_); } while (0)
#define ZERO(arr) do { _Pragma("unroll") for (int i_ = 0; i_ < 8; ++i_) arr[i_] = v4u{0u, 0u, 0u, 0u}; } while (0)
#define PIN(arr) asm volatile("" : "+v"(arr[0]), "+v"(arr[1]), "+v"(arr[2]), "+v"(arr[3]), "+v"(arr[4]), "+v"(arr[5]), "+v"(arr[6]), "+v"(arr[7]))
#define PUT(arr, BUF) do { if (DBG_NOPUT) { PIN(arr); break; } lptr d_ = L + opv((BUF) + lb + l16); _Pragma("unroll") for (int i_ = 0; i_ < 8; ++i_) ldsw(d_ + i_ * KB, arr[i_]); } while (0)
#define GET(arr, BUF) do { lptr d_ = L + opv((BUF) + lb + l16); _Pragma("unroll") for (int i_ = 0; i_ < 8; ++i_) arr[i_] = ldsr(d_ + i_ * KB); } while (0)
    const int vpd = ((MAPV == 0 ? (g & 1) * 2 + th : g & 3) * 64 + (tid & 63)) * 128, vpc = ((MAPV == 0 ? (g & 1) * 2 : g & 3) * 64 + (tid & 63)) * 128;
#define PFB(c) do { if (hw == 3) touch<(c) * 32 * KB>(rs, WS_WB + fb * 64 * KB, vpd); } while (0)
#define PFC(c) do { if (hw == 3) touch<(c) * CH>(rs, wc - lb, vpc); if (MAPV == 0 && hw == 2) touch<(c) * CH + 8 * KB>(rs, wc - lb, vpc); } while (0)
#define PFD(c) do { if (hw == 3) touch<(c) * 32 * KB>(rs, wdn - lb, vpd); } while (0)
#define HFLAG(nt) do { if (tid == 320) set_flag(ws, fl + F_H, me, (epoch << 3) | (nt)); } while (0)
#define HTOUCH(t) do { if (hw >= 2) ot<OT_H, (t) * KB>(rs, hoo, vh); } while (0)
#define BARL8() asm volatile("s_waitcnt lgkmcnt(8)\n\ts_barrier" ::: "memory")
#define HISSUE() do { if (hw == 0 && have < 7u && !DBG_NOGATE) hpoll_issue(rs, hco, vc); } while (0)
#define DBG_NOGATE 0
#define DBG_NOPUT 0
#define HGATE(nt, younger) do { if (hw == 0 && !DBG_NOGATE) { have = hpoll_read(L, lane, epoch); if (have < (nt)) { WAITV(younger); have = hpoll_read(L, lane, epoch); hgate(rs, L, hco, vc, lane, epoch, nt, have, dead); } } } while (0)
    v4u a0[8], a1[8];
    {
      LANE_VALS();
      sfor<16>([&](auto I_) AI { constexpr int i = decltype(I_)::v; dma1p<((i >> 3) * 16 + ((i & 7) >> 1)) * KS + ((i & 1) ? 2048 : 1024), (i >> 3) * CH + (i & 7) * KB>(rw, wkv, lb, vraw); });
      DMA_Q();
      STAMP(0);
      WAITV(0);
    }
    GET_EPOCH();
    {
      LANE_VALS();
      STAMP(1);
      const int tro = tr_off(lane);
      lptr ta = L + opv(BUF_A + lb + tro), tb = L + opv(BUF_B + lb + tro), tc = L + opv(BUF_C + lb + tro);
      float c[6] = {0.f, 0.f, 0.f, 0.f, 0.f, 0.f};
#pragma unroll
      for (int i = 0; i < 4; ++i) {
        c[0] = sum8(trr(ta + 2 * i * KB), c[0]); c[1] = sum8(trr(ta + (2 * i + 1) * KB), c[1]); c[2] = sum8(trr(tc + 2 * i * KB), c[2]);
        c[3] = sum8(trr(tb + 2 * i * KB), c[3]); c[4] = sum8(trr(tb + (2 * i + 1) * KB), c[4]); c[5] = sum8(trr(tc + (2 * i + 1) * KB), c[5]);
      }
      const unsigned hk = hilo(xhalf_sum(c[0] + c[3])), hv = hilo(xhalf_sum(c[1] + c[4])), hq = hilo(xhalf_sum(c[2] + c[5]));
      if (lane < 32) {
        lptr xp = L + L_X + (hw >> 1) * 512 + (hw & 1) * 8 + l16;
        *(LAS v2u*)xp = v2u{hk, hk}; *(LAS v2u*)(xp + KB) = v2u{hv, hv}; *(LAS v2u*)(xp + 2 * KB) = v2u{hq, hq};
      }
      GET(a0, BUF_A); GET(a1, BUF_B);
      PIN(a0); PIN(a1);
      if (hw == 0) { poll(flag_ptr(ws, F_P1, g * 16 + (lane & 15)), lane < 16, epoch, dead); lds_set(L, SY_GO, 1); }
      STAMP(2);
    }
#pragma unroll 1
    for (int layer = 0; layer < 2; ++layer) {
      LANE_VALS();
      const int act = WS_ACT + layer * A_LAYER;
      const int fl = layer * F_PER_LAYER;
      const int sb = 2 + layer * 28;
      (void)sb;
      v4u d2[8], d3[8], d4[8], d5[8], d6[8];
      BARL();
      if (layer == 1 && b == 0 && hw == 2) {
        for (unsigned n = 0; n < (1u << 16); ++n) {
          gu32* f = flag_ptr(ws, F_P2, lane);
          const unsigned v0 = __hip_atomic_load(f, RLX_AGENT), v1 = __hip_atomic_load(f + 16 * FLAG_STRIDE, RLX_AGENT);
          const unsigned v2 = __hip_atomic_load(f + 32 * FLAG_STRIDE, RLX_AGENT), v3 = __hip_atomic_load(f + 48 * FLAG_STRIDE, RLX_AGENT);
          if (__all(v0 == epoch && v1 == epoch && v2 == epoch && v3 == epoch)) break;
        }
        if (lane == 0) __hip_atomic_store((gu32*)(ws + WS_EPOCH), epoch, RLX_AGENT);
      } else if (layer == 1) STAMP(sb + 0);

      BAR();
      WAITV(0);

      if (hw == 0) *(volatile LAS unsigned*)(L + L_POLL + l16 / 4) = (epoch + 1u) << 3;
      BARL();
      STAMP(sb + 1);
      if (layer == 0 && tid == 256) set_flag(ws, F_P2, me, epoch);

      STAMP(sb + 2);
      BAR();

      {
        const int tq = 3 - hw;
        const v4u Qb = ldsr(L + L_QB + tq * KB + l16);
        FILL_DEF();
        if (hw == 3 && g >= 2) {
          poll(flag_ptr(ws, fl + F_KV, me - 32), lane == 0, epoch, dead);
          const int s2 = act + A_KV + (me - 32) * 16 * KB;
          dma4<0, BUF_B>(rs, s2, 0, l16); dma4<4 * KB, BUF_B + 4 * KB>(rs, s2, 0, l16);
          dma4<8 * KB, BUF_B + 8 * KB>(rs, s2, 0, l16); dma4<12 * KB, BUF_B + 12 * KB>(rs, s2, 0, l16);
        }
        const v4u of = attn_item<1>(L, Qb, lane, tq, g, [&]() AI {
          if (hw == 3) WAITV(0);
          if (hw == 2 && layer == 0) {
            int qq;
            if (lane < 7) qq = 112 + 7 * fb + lane;
            else if (lane < 9) qq = 168 + 2 * fb + (lane - 7);
            else { int n = (7 * m) / 4 + ((lane - 9) >> 2); n = n > 27 ? 27 : n; qq = 4 * n + ((lane - 9) & 3); }
            poll(flag_ptr(ws, F_P2, lane < 21 ? wg_index(qq) : 0), lane < 21, epoch, dead);
          }
          STAMP(sb + 4);
          BAR();
          STAGE(d6, wdn, 6 * 32 * KB);
        }, fill);
        gst(rs, of, l16, act + A_OT + ((4 * g + tq) * 32 + 2 * m + 1) * KB);
#pragma unroll 1
        while (nd < 5) fill();
        STAMP(sb + 6);
        WAITV(0);
        PIN(d6);
        BAR();
      }
      if (hw == 1) ot<OT_O, A_OT>(rs, act + (4 * g * 32 + 2 * m) * KB, (lane >> 4) * 32 * KB + (lane & 15) * 128);
      DMA(true, wc, 1 * CH, BUF_B);
      STAMP(sb + 7);
      lds_wait(L, SY_GO, 3 * layer + 2);
      __builtin_amdgcn_s_sleep(14);
      WAITV(0);
      lds_add(L, SY_HWL, lane);
      STAGE(d5, wdn, 5 * 32 * KB); STAGE(d4, wdn, 4 * 32 * KB);
      STAMP(sb + 9);
      lds_wait(L, SY_DR, 8 * layer + 8);
      const int vx = (lane >> 5) * 32 * KB + (lane & 31) * 128, vs = lane < 8 ? (lane >> 2) * 4096 + (lane & 3) * 128 : WS_BYTES;
      const int xoo = ((4 * g + 2 * th) * 32 + 4 * fb) * KB, soo = (4 * g + 2 * th) * 4096 + fb * 512;
      if (hw == 1) ot<OT_X, A_X1T>(rs, act + xoo, vx);
      if (hw == 2) ot<OT_X, A_ST1>(rs, act + soo, vs);
      DMA(true, wc, 2 * CH, BUF_D);
      STAMP(sb + 11);
      lds_wait(L, SY_GO, 3 * layer + 3);
      __builtin_amdgcn_s_sleep(36);
      DMA(true, wc, 3 * CH, BUF_A);
      WAITV(8); BAR();
      STAMP(sb + 13);
      DMA(true, wc, 4 * CH, BUF_C); DMA(true, wc, 5 * CH, BUF_B);
      WAITV(16); BAR();
      STAGE(d3, wdn, 3 * 32 * KB);
      const int hoo = act + A_HT + ((4 * g + 2 * (hw - 2)) * 112 + 7 * m) * KB;
      const int vh = (lane & 24) ? WS_BYTES : (lane >> 5) * 112 * KB + (lane & 7) * 128;
      const int hco = WS_FLAGS + ((fl + F_H) * 256 + g * 16) * FLAG_STRIDE, vc = lane < 16 ? lane * FLAG_STRIDE : WS_BYTES;
      unsigned have = 0;
      WAITV(16); BAR();

      DMA(true, wc, 6 * CH, BUF_D); STAGE(d2, wdn, 2 * 32 * KB);
      WAITV(24); BAR();

      HISSUE();
      DMA(false, wdn, 0 * 32 * KB, BUF_A);
      WAITV(16); BAR();

      if (hw == 0) have = hpoll_read(L, lane, epoch);
      HISSUE();
      DMA(false, wdn, 1 * 32 * KB, BUF_C);
      HGATE(2, 8);
      STAMP(sb + 14);
      BAR();
      WAITV(0); PIN(d5); PIN(d4); PIN(d3); PIN(d2);

      HISSUE();

      HGATE(3, 0);
      BAR();
      HISSUE();
      PUT(d2, BUF_B);

      BAR();
      PUT(d3, BUF_D);

      HGATE(4, 0);
      HISSUE();
      STAMP(sb + 15);
      BARL8();
      PUT(d4, BUF_A);

      HGATE(5, 0);
      HISSUE();
      BARL8();

      PUT(d5, BUF_C);
      HGATE(6, 0);
      HISSUE();
      BARL8();
      PUT(d6, BUF_B);

      HGATE(7, 0);
      STAMP(sb + 16);
      BARL8();
      BARL();
      if (layer == 0) {
        PUT(a0, BUF_A);
        lds_wait4(L, 2);
        PUT(a1, BUF_B);
        DMA_Q();
        lds_wait(L, SY_DR, 12);
        if (hw == 1) ot<OT_X, A_LAYER + A_XT>(rs, act + xoo, vx);
        if (hw == 2) ot<OT_X, A_LAYER + A_ST0>(rs, act + soo, vs);
      }
    }
  }
}
}

extern "C" void kernel_launch(void* const* d_in, const int* in_sizes, int n_in, void* d_out, int out_size, void* d_ws, size_t ws_size,
                              hipStream_t stream) {
  Params p;
  p.x = (const u16*)d_in[0]; p.wqkv = (const u16*)d_in[1]; p.wo = (const u16*)d_in[2];
  p.wg = (const u16*)d_in[4]; p.wu = (const u16*)d_in[5]; p.wd = (const u16*)d_in[6];
  p.out = (u16*)d_out; p.ws = (unsigned char*)d_ws;
  (void)hipFuncSetAttribute((const void*)fused_layers, hipFuncAttributeMaxDynamicSharedMemorySize, LDS_BYTES);
  hipLaunchKernelGGL(fused_layers, dim3(NWG), dim3(NTHR), LDS_BYTES, stream, p);
}
```

```cpp
#define HPRE 8 _E6=4 _PUBMODE=0 _H7S=15 _H6S=5 b3= b5=d6@1 b6=C1 go1=d6@0,d5,d4 dr1=C2 b10=C3 b11=C4,C5 b12=d3 b13=C6,d2 b14=D0 b15=D1
#define HI_EARLY 0
#define L2 (PUBMODE == 0 ? 3 : 2)
#define L3 (PUBMODE == 0 ? 3 : 4)
#define UNIT_W 1
#define HPRIO 3
#define UNIT_AT 0
#define POLL_FIRST 0
#define START_ORDER 0
#define QSLEEP 8
#include <hip/hip_runtime.h>
#include <hip/hip_bf16.h>
#include <utility>

#define POLL_SLEEP 0
#define UNIT_EARLY 1
#define EARLY_FREE 0
#define HPUBS 10
#define E6 2
#define H7S 19
#define XW0 8
#define XW1 16
#define H6S 5
#define PUBMODE 1
#define PDD 3
#define PD_O 18
#define PD_X1 18
#define PD_X2 18
#define PD_KV 10
#define HSLEEP 2
#define POLLK 0
#define HPOLLBITS "sc1"
#define HD5 0
#define HD6 0
#define HD7 0
#define KVWIN 12
#define PROF 0

extern "C" __device__ float llvm_fmul_legacy(float, float) __asm("llvm.amdgcn.fmul.legacy");
namespace {
typedef unsigned short u16;
typedef unsigned v4u __attribute__((ext_vector_type(4)));
typedef unsigned v2u __attribute__((ext_vector_type(2)));
typedef short v4s __attribute__((ext_vector_type(4)));
typedef float f32x4 __attribute__((ext_vector_type(4)));
typedef float f32x16 __attribute__((ext_vector_type(16)));
typedef __bf16 v2bf __attribute__((ext_vector_type(2)));
typedef __bf16 v8bf __attribute__((ext_vector_type(8)));
#define LAS __attribute__((address_space(3)))
#define GAS __attribute__((address_space(1)))
#define DI __device__ __forceinline__
typedef GAS unsigned gu32;
typedef LAS unsigned char* lptr;
#define RLX_AGENT __ATOMIC_RELAXED, __HIP_MEMORY_SCOPE_AGENT

constexpr int NWG = 256, NTHR = 512;
constexpr int KB = 1024;
constexpr int FLAG_STRIDE = 256;
enum { F_P2 = 0, F_KV = 1, F_O = 2, F_X1 = 3, F_X2 = 4, F_H = 5, F_PER_LAYER = 10, F_KINDS = 21 };
constexpr int WS_EPOCH = 0;
constexpr int WS_FLAGS = 4 * KB;
constexpr int WS_WC = WS_FLAGS + F_KINDS * 256 * FLAG_STRIDE;
constexpr int WS_WD = WS_WC + 16 * 231 * KB;
constexpr int WS_ACT = WS_WD + 8 * 224 * KB;
constexpr int A_XT = 0, A_ST0 = A_XT + 2048 * KB, A_KV = A_ST0 + 256 * KB, A_OT = A_KV + 4096 * KB, A_X1T = A_OT + 2048 * KB,
              A_ST1 = A_X1T + 2048 * KB, A_HT = A_ST1 + 256 * KB, A_LAYER = A_HT + 64 * 112 * KB;
constexpr int WS_DBG = WS_ACT + 2 * A_LAYER;
constexpr int WS_BYTES = WS_DBG + 256 * KB * 2;
constexpr int CH = 33 * KB;
constexpr int BUF_A = 0, BUF_B = CH, BUF_C = 2 * CH, BUF_D = 3 * CH, SP = 4 * CH;
constexpr int L_KV = SP;
constexpr int L_QB = SP + 16 * KB;
constexpr int L_ONES = SP + 20 * KB;
constexpr int L_SY = L_ONES + 64;
constexpr int SY_GO = 0, SY_DR = 4, SY_HWL = 8, SY_KV = 12, SY_EX = 16, SY_U = 32, SY_X0 = 36, SY_U2 = 40, SY_H = 44, SY_EP = 48;
constexpr int L_RS = L_ONES + 128;
constexpr int L_POLL = L_ONES + 640;
constexpr int L_PF = L_ONES + 1088;
constexpr int L_X = SP + 22 * KB;
constexpr int L_DBG = SP + 25 * KB;
constexpr int LDS_BYTES = SP + (PROF == 1 ? 27 : 25) * KB;

constexpr float NEG_BIG = -1e30f;
constexpr float LOG2E = 1.4426950408889634f;
constexpr int AUX_SC1 = 16;

struct Params {
  const u16 *x, *wqkv, *wo, *wg, *wu, *wd;
  u16* out;
  unsigned char* ws;
};

template <int I> struct IC { static constexpr int v = I; };
template <class F, int... Is> DI void sfor_impl(F& f, std::integer_sequence<int, Is...>) { (f(IC<Is>{}), ...); }
template <int N, class F> DI void sfor(F f) { sfor_impl(f, std::make_integer_sequence<int, N>{}); }
#define AI __attribute__((always_inline))
DI f32x16 mfma(v4u a, v4u b, f32x16 c) {
  return __builtin_amdgcn_mfma_f32_32x32x16_bf16(__builtin_bit_cast(v8bf, a), __builtin_bit_cast(v8bf, b), c, 0, 0, 0);
}
DI unsigned pk(float a, float b) { v2bf v = {(__bf16)a, (__bf16)b}; return __builtin_bit_cast(unsigned, v); }
DI float fm(float a, float b) { return llvm_fmul_legacy(a, b); }
DI float bflo(unsigned w) { return __uint_as_float(w << 16); }
DI float bfhi(unsigned w) { return __uint_as_float(w & 0xffff0000u); }
DI float xhalf_sum(float v) { auto r = __builtin_amdgcn_permlane32_swap(__float_as_uint(v), __float_as_uint(v), false, false); return __uint_as_float(r[0]) + __uint_as_float(r[1]); }
DI float xhalf_max(float v) { auto r = __builtin_amdgcn_permlane32_swap(__float_as_uint(v), __float_as_uint(v), false, false); return __builtin_fmaxf(__uint_as_float(r[0]), __uint_as_float(r[1])); }
DI v4u ldsr(lptr p) { return *(LAS v4u*)p; }
DI void ldsw(lptr p, v4u v) { *(LAS v4u*)p = v; }
typedef __amdgpu_buffer_rsrc_t rsrc_t;
DI v4u gld(rsrc_t rs, int voff, int soff) { return __builtin_amdgcn_raw_buffer_load_b128(rs, voff, soff, AUX_SC1); }
DI v4u gldf(rsrc_t rs, int l16, int soff, int i) { return __builtin_amdgcn_raw_buffer_load_b128(rs, l16 + (i & 3) * KB, soff + (i >> 2) * 4 * KB, AUX_SC1); }
DI int opv(int x) { asm volatile("" : "+v"(x)); return x; }
DI int ops(int x) { asm volatile("" : "+s"(x)); return x; }
DI void gst(rsrc_t rs, v4u v, int voff, int soff) {
  __builtin_amdgcn_raw_buffer_store_b128(v, rs, voff, soff, AUX_SC1);
  asm volatile("s_nop 1" :: "v"(v) : "memory");
}
DI void gst2(rsrc_t rs, v2u v, int voff, int soff) { __builtin_amdgcn_raw_buffer_store_b64(v, rs, voff, soff, AUX_SC1); }
#define SCHED() __builtin_amdgcn_sched_barrier(0)
#define PINA(a) asm volatile("" : "+v"(a))
#define BAR() asm volatile("s_barrier" ::: "memory")
#define BARL() asm volatile("s_waitcnt lgkmcnt(0)\n\ts_barrier" ::: "memory")
#define WAITV(n) asm volatile("s_waitcnt vmcnt(" #n ")" ::: "memory")

template <int G, int LO> DI void dma4(rsrc_t rs, int sb, int lb, int voff) {
  unsigned keep, so;
  asm volatile("s_mov_b32 %0, m0\n\ts_add_u32 m0, %5, %7\n\ts_add_u32 %1, %4, %6\n\ts_nop 2\n\t"
               "buffer_load_dwordx4 %2, %3, %1 offen sc1 lds\n\tbuffer_load_dwordx4 %2, %3, %1 offen offset:1024 sc1 lds\n\t"
               "buffer_load_dwordx4 %2, %3, %1 offen offset:2048 sc1 lds\n\tbuffer_load_dwordx4 %2, %3, %1 offen offset:3072 sc1 lds\n\t"
               "s_mov_b32 m0, %0" : "=&s"(keep), "=&s"(so) : "v"(voff), "s"(rs), "s"(sb), "s"(lb), "i"(G), "i"(LO) : "memory", "scc");
}
template <int G, int LO> DI void dma1(rsrc_t rs, int sb, int lb, int voff) {
  unsigned keep, so;
  asm volatile("s_mov_b32 %0, m0\n\ts_add_u32 m0, %5, %7\n\ts_add_u32 %1, %4, %6\n\ts_nop 2\n\t"
               "buffer_load_dwordx4 %2, %3, %1 offen sc1 lds\n\t"
               "s_mov_b32 m0, %0" : "=&s"(keep), "=&s"(so) : "v"(voff), "s"(rs), "s"(sb), "s"(lb), "i"(G), "i"(LO) : "memory", "scc");
}
template <int G, int LO> DI void dma1p(rsrc_t rs, int sb, int lb, int voff) {
  unsigned keep, so;
  asm volatile("s_mov_b32 %0, m0\n\ts_add_u32 m0, %5, %7\n\ts_add_u32 %1, %4, %6\n\ts_nop 2\n\t"
               "buffer_load_dwordx4 %2, %3, %1 offen lds\n\t"
               "s_mov_b32 m0, %0" : "=&s"(keep), "=&s"(so) : "v"(voff), "s"(rs), "s"(sb), "s"(lb), "i"(G), "i"(LO) : "memory", "scc");
}
DI int tr_off(int lane) { const int gi = lane >> 4, q4 = (lane & 15) >> 2, pp = lane & 3; return (8 * (gi >> 1) + q4) * 64 + (gi & 1) * 32 + (((pp & 1) << 1) | (pp >> 1)) * 8; }
DI v4u trr(lptr p) {
  const v2u lo = __builtin_bit_cast(v2u, __builtin_amdgcn_ds_read_tr16_b64_v4i16((LAS v4s*)p));
  const v2u hi = __builtin_bit_cast(v2u, __builtin_amdgcn_ds_read_tr16_b64_v4i16((LAS v4s*)(p + 256)));
  return v4u{lo.x, lo.y, hi.x, hi.y};
}
DI float dot2(unsigned a, unsigned b, float c) { return __builtin_amdgcn_fdot2_f32_bf16(__builtin_bit_cast(v2bf, a), __builtin_bit_cast(v2bf, b), c, false); }
DI float sum8(v4u f, float c) {
#pragma unroll
  for (int e = 0; e < 4; ++e) { const unsigned fe = f[e]; c = dot2(fe, 0x3f803f80u, c); }
  return c;
}
DI unsigned hilo(float c) { return pk(c, c - bflo(pk(c, c))); }
template <int G> DI void touch(rsrc_t rs, int sb, int voff) {
  unsigned keep, so;
  asm volatile("s_mov_b32 %0, m0\n\ts_mov_b32 m0, %6\n\ts_add_u32 %1, %4, %5\n\ts_nop 2\n\t"
               "buffer_load_dword %2, %3, %1 offen sc1 lds\n\t"
               "s_mov_b32 m0, %0" : "=&s"(keep), "=&s"(so) : "v"(voff), "s"(rs), "s"(sb), "i"(G), "i"(L_PF) : "memory", "scc");
}
#define OTM 0
enum { OT_KV = 1, OT_O = 2, OT_X = 4, OT_H = 8 };
template <int K, int G> DI void ot(rsrc_t rs, int sb, int voff) { if (OTM & K) touch<G>(rs, sb, voff); }
template <bool EXTRA, int G, int LBUF> DI void dma_chunk(rsrc_t rs, int sb, int lb, int voff) {
  dma4<G, LBUF>(rs, sb, lb, voff);
  dma4<G + 4 * KB, LBUF + 4 * KB>(rs, sb, lb, voff);
  if (EXTRA && lb == 0) dma1<G + 32 * KB, LBUF + 32 * KB>(rs, sb, lb, voff);
}

#define MAPV 1
__host__ __device__ inline int wg_index(int b) {
  if (MAPV == 0) return ((b & 7) * 2 + (b >> 7)) * 16 + ((b >> 3) & 15);
  return (4 * ((b & 7) >> 1) + (b >> 6)) * 16 + 8 * (b & 1) + ((b >> 3) & 7);
}
DI gu32* flag_ptr(unsigned char* ws, int kind, int idx) { return (gu32*)(ws + WS_FLAGS + (size_t)(kind * 256 + idx) * FLAG_STRIDE); }
DI void set_flag(unsigned char* ws, int kind, int idx, unsigned epoch) { __hip_atomic_store(flag_ptr(ws, kind, idx), epoch, RLX_AGENT); }
DI void poll(gu32* f, bool active, unsigned epoch, bool& dead) {
  if (!dead) {
    const unsigned long long t0 = __builtin_amdgcn_s_memrealtime();
#pragma unroll 1
    for (unsigned spins = 1;; ++spins) {
      unsigned v = epoch;
      if (active) v = __hip_atomic_load(f, RLX_AGENT);
      if (__all(v == epoch)) break;
      if ((spins & 63u) == 0u && __builtin_amdgcn_s_memrealtime() - t0 > 2000000ull) { dead = true; break; }
      active = v != epoch;
      if (POLL_SLEEP) __builtin_amdgcn_s_sleep(POLL_SLEEP);
    }
  }
  __builtin_amdgcn_fence(__ATOMIC_ACQUIRE, "wavefront");
}

DI void hpoll_issue(rsrc_t rs, int so, int voff) {
  unsigned keep;
  asm volatile("s_mov_b32 %0, m0\n\ts_mov_b32 m0, %4\n\ts_nop 2\n\tbuffer_load_dword %1, %2, %3 offen " HPOLLBITS " lds\n\ts_mov_b32 m0, %0"
               : "=&s"(keep) : "v"(voff), "s"(rs), "s"(so), "i"(L_POLL) : "memory");
}
DI void hgate(rsrc_t rs, lptr L, int so, int voff, int lane, unsigned want, bool& dead) {
  if (!dead) {
    const unsigned long long t0 = __builtin_amdgcn_s_memrealtime();
#pragma unroll 1
    for (unsigned spins = 1;; ++spins) {
      if (__all(*(volatile LAS unsigned*)(L + L_POLL + (lane & 15) * 4) == want)) break;
      if ((spins & 63u) == 0u && __builtin_amdgcn_s_memrealtime() - t0 > 2000000ull) { dead = true; break; }
      if (HSLEEP) __builtin_amdgcn_s_sleep(HSLEEP);
      hpoll_issue(rs, so, voff);
      WAITV(0);
    }
  }
  asm volatile("" ::: "memory");
}
DI void lds_set(lptr L, int word, unsigned v) { asm volatile("" ::: "memory"); *(volatile LAS unsigned*)(L + L_SY + word) = v; asm volatile("" ::: "memory"); }
DI void lds_add(lptr L, int word, int lane) {
  asm volatile("" ::: "memory");
  if (lane == 0) __hip_atomic_fetch_add((LAS unsigned*)(L + L_SY + word), 1u, __ATOMIC_RELAXED, __HIP_MEMORY_SCOPE_WORKGROUP);
  asm volatile("" ::: "memory");
}
DI bool lds_last(lptr L, int word, int lane) {
  asm volatile("" ::: "memory");
  unsigned o = 0;
  if (lane == 0) o = __hip_atomic_fetch_add((LAS unsigned*)(L + L_SY + word), 1u, __ATOMIC_RELAXED, __HIP_MEMORY_SCOPE_WORKGROUP);
  asm volatile("" ::: "memory");
  return (o & 3u) == 3u;
}
DI void lds_wait(lptr L, int word, unsigned v) {
#pragma unroll 1
  for (unsigned n = 0; n < (1u << 22); ++n)
    if ((unsigned)__builtin_amdgcn_readfirstlane(*(volatile LAS unsigned*)(L + L_SY + word)) >= v) break;
  asm volatile("" ::: "memory");
}
DI void lds_wait4(lptr L, unsigned v) {
#pragma unroll 1
  for (unsigned n = 0; n < (1u << 22); ++n) {
    const v4u e = *(volatile LAS v4u*)(L + L_SY + SY_EX);
    const unsigned a = e.x < e.y ? e.x : e.y, b = e.z < e.w ? e.z : e.w;
    if ((unsigned)__builtin_amdgcn_readfirstlane(a < b ? a : b) >= v) break;
    __builtin_amdgcn_s_sleep(1);
  }
  asm volatile("" ::: "memory");
}

#define STAMP(i) do {} while (0)

DI void finish_stats2(float s, float q, float& mean, float& rstd) {
  s = xhalf_sum(s); q = xhalf_sum(q);
  mean = s * (1.f / 512.f);
  const float var = __builtin_fmaxf(q * (1.f / 512.f) - mean * mean, 0.f);
  rstd = __builtin_amdgcn_rsqf(var + 1e-5f);
}
DI void finish_stats(const v4u (&st)[4], float& mean, float& rstd) {
  float s = 0.f, q = 0.f;
#pragma unroll
  for (int i = 0; i < 4; ++i) { s += __uint_as_float(st[i].x) + __uint_as_float(st[i].z); q += __uint_as_float(st[i].y) + __uint_as_float(st[i].w); }
  finish_stats2(s, q, mean, rstd);
}
DI v4u mean_frag(float mean) {
  const float nm = -mean;
  const unsigned hh = pk(nm, nm);
  const float lo = nm - bflo(hh);
  const unsigned ll = pk(lo, lo);
  return v4u{hh, ll, hh, ll};
}
DI void store_stats(rsrc_t rs, const float (&v)[16], int lane, int soff) {
  float s = 0.f, q = 0.f;
#pragma unroll
  for (int i = 0; i < 16; ++i) { s += v[i]; q = __builtin_fmaf(v[i], v[i], q); }
  s = xhalf_sum(s); q = xhalf_sum(q);
  if (lane < 32) gst2(rs, v2u{__float_as_uint(s), __float_as_uint(q)}, lane * 16, soff);
}

struct Attn { f32x16 O; float m; };
DI float max16(const f32x16& s, float m) {
#pragma unroll
  for (int i = 0; i < 16; i += 2) m = __builtin_fmaxf(__builtin_fmaxf(m, s[i]), s[i + 1]);
  return m;
}
DI f32x16 pv(const f32x16& s, v4u v0, v4u v1, f32x16 O) {
  float p[16];
#pragma unroll
  for (int i = 0; i < 16; ++i) p[i] = __builtin_amdgcn_exp2f(s[i]);
  const v4u p0 = {pk(p[0], p[1]), pk(p[2], p[3]), pk(p[4], p[5]), pk(p[6], p[7])};
  const v4u p1 = {pk(p[8], p[9]), pk(p[10], p[11]), pk(p[12], p[13]), pk(p[14], p[15])};
  O = mfma(v0, p0, O);
  O = mfma(v1, p1, O);
  return O;
}
template <bool FIRST>
DI void attn_chunk(Attn& A, v4u Q, lptr kp, lptr vp, int vstride, int t0, int t1, int tm, const f32x16& mask) {
  const f32x16 zero = {};
  float mx = FIRST ? NEG_BIG : A.m;
  f32x16 sm = zero;
  if (tm >= 0) { sm = mfma(ldsr(kp + tm * 4096), Q, mask); mx = max16(sm, mx); }
  for (int t = t0; t < t1; ++t) { const f32x16 s = mfma(ldsr(kp + t * 4096), Q, zero); mx = max16(s, mx); }
  mx = xhalf_max(mx);
  if (FIRST) A.O = zero;
  else { const float al = __builtin_amdgcn_exp2f(A.m - mx); A.O *= al; }
  A.m = mx;
  if (tm >= 0) { A.O = pv(sm - mx, ldsr(vp + tm * vstride), ldsr(vp + tm * vstride + 1024), A.O); }
  f32x16 nm;
#pragma unroll
  for (int i = 0; i < 16; ++i) nm[i] = -mx;
  for (int t = t0; t < t1; ++t) {
    const f32x16 s = mfma(ldsr(kp + t * 4096), Q, nm);
    A.O = pv(s, ldsr(vp + t * vstride), ldsr(vp + t * vstride + 1024), A.O);
  }
}
DI float opf(float x) { asm volatile("" : "+v"(x)); return x; }
struct Fast { f32x16 O, nm; };
DI void fast_diag(Fast& F, v4u Q, lptr kp, lptr vp, const f32x16& mask) {
  const v4u k = ldsr(kp), v0 = ldsr(vp), v1 = ldsr(vp + 1024);
  const f32x16 s = mfma(k, Q, mask);
  float m0 = __builtin_fmaxf(s[0], s[1]), m1 = __builtin_fmaxf(s[2], s[3]);
#pragma unroll
  for (int i = 4; i < 16; i += 4) { m0 = __builtin_fmaxf(__builtin_fmaxf(m0, s[i]), s[i + 1]); m1 = __builtin_fmaxf(__builtin_fmaxf(m1, s[i + 2]), s[i + 3]); }
  const float mx = xhalf_max(__builtin_fmaxf(m0, m1));
  f32x16 d;
#pragma unroll
  for (int i = 0; i < 16; ++i) { d[i] = opf(s[i] - mx); F.nm[i] = -mx; }
  F.O = pv(d, v0, v1, f32x16{});
}
template <class Hook>
DI void fast_run(Fast& F, v4u Q, lptr kp, lptr vp, int vstride, int t0, int t1, const f32x16& c0, Hook hook) {
  lptr kq = kp + t0 * 4096, vq = vp + t0 * vstride;
  v4u k = ldsr(kq), va0 = ldsr(vq), va1 = ldsr(vq + 1024), vb0, vb1;
  f32x16 sa = mfma(k, Q, c0), sb;
  k = ldsr(kq + 4096);
  int n = t1 - t0;
#pragma unroll 1
  for (; n >= 2; n -= 2) {
    sb = mfma(k, Q, F.nm);
    vq += vstride; vb0 = ldsr(vq); vb1 = ldsr(vq + 1024); k = ldsr(kq + 2 * 4096);
    hook();
    SCHED();
    F.O = pv(sa, va0, va1, F.O);
    SCHED();
    sa = mfma(k, Q, F.nm);
    vq += vstride; va0 = ldsr(vq); va1 = ldsr(vq + 1024); kq += 2 * 4096; k = ldsr(kq + 4096);
    hook();
    SCHED();
    F.O = pv(sb, vb0, vb1, F.O);
    SCHED();
  }
  if (n) { hook(); F.O = pv(sa, va0, va1, F.O); }
}
DI void make_mask(f32x16& mk, int lane, bool old) {
  const int d = (lane & 31) - 4 * (lane >> 5);
#pragma unroll
  for (int i = 0; i < 16; ++i) { const bool le = ((i & 3) + 8 * (i >> 2)) <= d; mk[i] = (le != old) ? 0.f : NEG_BIG; }
}
template <int HD> DI v4u attn_out(const f32x16& O) {
  const float r = __builtin_amdgcn_rcpf(O[HD ? 0 : 8]);
  const int o = HD ? 8 : 0;
  return v4u{pk(O[o] * r, O[o + 1] * r), pk(O[o + 2] * r, O[o + 3] * r), pk(O[o + 4] * r, O[o + 5] * r), pk(O[o + 6] * r, O[o + 7] * r)};
}
template <int HD, class Diag, class Mid, class Hook>
DI v4u attn_item(lptr L, v4u Q, int lane, int tq, int g, Diag diag, Mid mid, Hook hook) {
  const int l16 = lane * 16;
  const bool vl = ((lane >> 4) & 1) == HD;
  const int vstride = vl ? 4096 : 0;
  lptr kl = L + L_KV + HD * KB + l16, vloc = vl ? L + L_KV + 2 * KB + l16 : L + L_ONES;
  lptr kr = L + BUF_B + HD * KB + l16, vrem = vl ? L + BUF_B + 2 * KB + l16 : L + L_ONES;
  Fast F; f32x16 mk;
  make_mask(mk, lane, false);
  fast_diag(F, Q, kl + tq * 4096, vloc + tq * vstride, mk);
  diag();
  if (tq > 0) fast_run(F, Q, kl, vloc, vstride, 0, tq, F.nm, []() {});
  f32x16 c0;
#pragma unroll
  for (int i = 0; i < 16; ++i) c0[i] = opf(g >= 2 ? __builtin_fminf(F.nm[0], NEG_BIG - mk[i] * 2.f) : F.nm[0]);
  mid();
  if (g >= 1) fast_run(F, Q, kr, vrem, vstride, g >= 2 ? tq : 4, 8, c0, hook);
  if (__any(!(F.O[HD ? 0 : 8] <= 1.8446744e19f))) {
    Attn A;
    make_mask(mk, opv(lane), false);
    attn_chunk<true>(A, Q, kl, vloc, vstride, 0, tq, tq, mk);
    if (g >= 1) { make_mask(mk, opv(lane), true); attn_chunk<false>(A, Q, kr, vrem, vstride, g >= 2 ? tq + 1 : 4, 8, g >= 2 ? tq : -1, mk); }
    F.O = A.O;
  }
  return attn_out<HD>(F.O);
}

DI void finish_tile(lptr L, const f32x16& keep, v4u (&res)[2], float (&v)[16], int w, int lane) {
  lptr xp = L + L_KV + (w ^ 2) * 4096 + lane * 16;
#pragma unroll
  for (int qd = 0; qd < 4; ++qd) {
    const f32x4 r = *(LAS f32x4*)(xp + qd * 1024);
#pragma unroll
    for (int j = 0; j < 4; ++j) v[4 * qd + j] = keep[4 * qd + j] + r[j];
  }
#pragma unroll
  for (int e = 0; e < 2; ++e)
#pragma unroll
    for (int j = 0; j < 4; ++j) { v[8 * e + 2 * j] += bflo(res[e][j]); v[8 * e + 2 * j + 1] += bfhi(res[e][j]); }
#pragma unroll
  for (int e = 0; e < 2; ++e) res[e] = v4u{pk(v[8 * e], v[8 * e + 1]), pk(v[8 * e + 2], v[8 * e + 3]), pk(v[8 * e + 4], v[8 * e + 5]), pk(v[8 * e + 6], v[8 * e + 7])};
}
DI void send_tile(lptr L, const f32x16& send, int w, int lane) {
  lptr xp = L + L_KV + w * 4096 + lane * 16;
#pragma unroll
  for (int qd = 0; qd < 4; ++qd) *(LAS f32x4*)(xp + qd * 1024) = f32x4{send[4 * qd], send[4 * qd + 1], send[4 * qd + 2], send[4 * qd + 3]};
}

__global__ void __launch_bounds__(NTHR, 2) fused_layers(Params p) {
  extern __shared__ __attribute__((aligned(16))) unsigned char smem[];
  lptr L = (lptr)smem;
  const int tid = threadIdx.x, wave = __builtin_amdgcn_readfirstlane(tid >> 6);
  const int b = blockIdx.x, me = wg_index(b), g = me >> 4, m = me & 15;
  unsigned char* ws = p.ws;
  const rsrc_t rs = __builtin_amdgcn_make_buffer_rsrc(ws, 0, WS_BYTES, 0x00020000);
  const int th = m >> 3, fb = m & 7;
  bool dead = false;
#define LANE_VALS() const int lane = opv(tid) & 63, r32 = lane & 31, hf = lane >> 5, l16 = lane * 16; (void)r32; (void)hf; (void)l16
  if (tid < 12) { if (tid < 8) *(LAS unsigned*)(L + L_ONES + (tid >> 2) * 1024 + (tid & 3) * 4) = 0x3f803f80u; *(LAS unsigned*)(L + L_SY + tid * 4) = 0u; }
  BARL();

#define FILL_DEF() int nd = 0; \
  const rsrc_t ro = __builtin_amdgcn_make_buffer_rsrc((void*)p.wo, 0, 512 * 512 * 2, 0x00020000); \
  auto fill = [&]() AI { \
    if (nd == 0 || nd == 2) { \
      const int so_ = (wave * 2 + (nd >> 1)) * 16 * KB + fb * 128, lo_ = (wave < 4 ? BUF_D : BUF_A - 32 * KB) + (wave * 8 + nd * 2) * KB, vo_ = (lane >> 2) * KB + (lane & 3) * 16; \
      dma1p<0, 0>(ro, so_, lo_, vo_); dma1p<64, KB>(ro, so_, lo_, vo_); dma1p<256 * KB, 2 * KB>(ro, so_, lo_, vo_); dma1p<256 * KB + 64, 3 * KB>(ro, so_, lo_, vo_); \
    } else if (nd == 4 && layer == 1) { dma4<0, BUF_C>(rs, WS_WC + (m * 231 + wave * 4) * KB, wave * 4 * KB, l16); if (wave == 7) dma1<32 * KB, BUF_C + 32 * KB>(rs, WS_WC + m * 231 * KB, 0, l16); } \
    ++nd; \
  }
  if (wave < 4) {
    const int w = wave;
    const int ttr = 4 * g + 2 * th + (w & 1), kh = w >> 1, ftile = 2 * fb + kh;
    const int tta = 4 * g + w;
    v4u res[2];
    unsigned epoch;
    STAMP(0);
#pragma unroll 1
    for (int layer = 0; layer < 2; ++layer) {
      const int act = WS_ACT + layer * A_LAYER;
      const int fl = layer * F_PER_LAYER;
      const int sb = 2 + layer * 28;
      (void)sb;
      v4u xf[32];
      v4u Qa;
      {
        LANE_VALS();
        STAMP(sb + 0);
        f32x16 aK = {}, aV = {}, aQ = {};
        float mean, rstd;
        const int tro = tr_off(lane);
        constexpr int PQ = 4;
        v4u wq[PQ];
        lptr pc = L + opv(BUF_C + tro);
        auto kv_pass = [&](auto FIRST_) AI {
          constexpr bool FIRST = decltype(FIRST_)::v;
          constexpr int WIN = KVWIN, PD = 3;
          v4u st[4];
          const int sto = ops(act + A_ST0 + tta * 4096), xo = ops(FIRST ? tta * 32 * KB : act + A_XT + tta * 32 * KB);
          lptr tw = L + opv(L_KV + w * 4096 + (lane >> 3) * 128 + (((lane & 7) ^ (lane >> 3)) * 16)), tr = L + opv(L_KV + w * 4096 + r32 * 128);
          const int sw = (r32 & 7) ^ hf;
          auto xpose = [&](int j) AI {
#pragma unroll
            for (int i = 0; i < 4; ++i) ldsw(tw + i * KB, xf[4 * j + i]);
#pragma unroll
            for (int e = 0; e < 4; ++e) xf[4 * j + e] = ldsr(tr + ((2 * e) ^ sw) * 16);
          };
          const rsrc_t rx = __builtin_amdgcn_make_buffer_rsrc((void*)p.x, 0, 2048 * 1024, 0x00020000);
          const int vx = (lane >> 3) * KB + (lane & 7) * 16;
          auto xraw = [&](int i) AI { return __builtin_amdgcn_raw_buffer_load_b128(rx, vx + (i >> 2) * 128, xo + (i & 3) * 8 * KB, 0); };
          if constexpr (FIRST) {
            const unsigned epoch_v = __hip_atomic_load((gu32*)(ws + WS_EPOCH), RLX_AGENT);
#pragma unroll
            for (int i = 0; i < XW0; ++i) xf[i] = xraw(i);
            SCHED();
            epoch = __builtin_amdgcn_readfirstlane(epoch_v) + 1u;
            if (w == 0) lds_set(L, SY_EP, epoch);
            xpose(0);
#pragma unroll
            for (int i = XW0; i < XW1; ++i) xf[i] = xraw(i);
            asm volatile("s_waitcnt lgkmcnt(0)" ::: "memory");
          } else {
#pragma unroll
            for (int i = 0; i < 4; ++i) xf[i] = gldf(rs, l16, xo, i);
#pragma unroll
            for (int i = 0; i < 4; ++i) st[i] = gld(rs, hf * 2048 + r32 * 16 + i * 512, sto);
#pragma unroll
            for (int i = 4; i < WIN; ++i) xf[i] = gldf(rs, l16, xo, i);
          }
          SCHED();
          BAR();
          v4u wk[PD], wv[PD];
          lptr pa = L + opv(BUF_A + tro), pb = L + opv(BUF_B + tro);
#pragma unroll
          for (int i = 0; i < PD; ++i) { wk[i] = trr(pa + 2 * i * KB); wv[i] = trr(pa + (2 * i + 1) * KB); }
          float s0 = 0.f, s1 = 0.f, q0 = 0.f, q1 = 0.f;
          sfor<32>([&](auto S_) AI {
            constexpr int s = decltype(S_)::v, n = s + PD;
            if constexpr (FIRST) {
              if constexpr (s + XW1 < 32) xf[s + XW1] = xraw(s + XW1);
              if constexpr (s + XW1 == 32) { const GAS v4u* xs = (const GAS v4u*)(p.x + (size_t)(ttr * 32 + r32) * 512 + ftile * 32 + hf * 8); res[0] = xs[0]; res[1] = xs[2]; }
              if constexpr ((s & 3) == 0 && s < 28) xpose(s / 4 + 1);
            }
            else if constexpr (s + WIN < 32) xf[s + WIN] = gldf(rs, l16, xo, s + WIN);
            const v4u ck = wk[s % PD], cv = wv[s % PD];
            if constexpr (n == 16) BAR();
            if constexpr (n < 16) { wk[s % PD] = trr(pa + 2 * n * KB); wv[s % PD] = trr(pa + (2 * n + 1) * KB); }
            else if constexpr (n < 32) { wk[s % PD] = trr(pb + 2 * (n - 16) * KB); wv[s % PD] = trr(pb + (2 * (n - 16) + 1) * KB); }
            aK = mfma(ck, xf[s], aK);
            aV = mfma(xf[s], cv, aV);
            PINA(aK); PINA(aV);
            if constexpr (s >= 16) { aQ = mfma(wq[(s - 16) % PQ], xf[s - 16], aQ); PINA(aQ); }
            if constexpr (n >= 16) wq[(n - 16) % PQ] = trr(pc + (n - 16) * KB);
            if constexpr (FIRST) {
              const unsigned e0 = xf[s].x, e1 = xf[s].y, e2 = xf[s].z, e3 = xf[s].w;
              s0 = dot2(e0, 0x3f803f80u, s0); q0 = dot2(e0, e0, q0); s1 = dot2(e1, 0x3f803f80u, s1); q1 = dot2(e1, e1, q1);
              s0 = dot2(e2, 0x3f803f80u, s0); q0 = dot2(e2, e2, q0); s1 = dot2(e3, 0x3f803f80u, s1); q1 = dot2(e3, e3, q1);
            } else if constexpr (s == 6) finish_stats(st, mean, rstd);
            SCHED();
          });
          if constexpr (FIRST) finish_stats2(s0 + s1, q0 + q1, mean, rstd);
        };
        if (layer == 0) kv_pass(IC<1>{}); else kv_pass(IC<0>{});
        if (lane < 32) *(LAS float*)(L + L_RS + w * 128 + lane * 4) = rstd;
        const v4u xe = mean_frag(mean);
        STAMP(sb + 1);
        BAR();
        const v4u wxk = ldsr(L + L_X + l16), wxv = ldsr(L + L_X + KB + l16);
        f32x4 rsv[4];
#pragma unroll
        for (int qd = 0; qd < 4; ++qd) rsv[qd] = *(LAS f32x4*)(L + L_RS + w * 128 + qd * 32 + hf * 16);
        lptr kvl = L + opv(L_KV + w * 4096 + l16);
        const int kvo = ops(act + A_KV + (me * 4 + w) * 4096);
        v4u kvf;
        SCHED();
        sfor<17>([&](auto S_) AI {
          constexpr int s = 16 + decltype(S_)::v, n = s + PQ - 1;
          const v4u cq = wq[s % PQ];
          if constexpr (n < 32) wq[n % PQ] = trr(pc + n * KB);
          else if constexpr (n == 32) wq[n % PQ] = ldsr(L + L_X + 2 * KB + l16);
          aQ = mfma(cq, s < 32 ? xf[s < 32 ? s : 0] : xe, aQ);
          PINA(aQ);
          if constexpr (s == 16) { aK = mfma(wxk, xe, aK); aV = mfma(xe, wxv, aV); PINA(aK); PINA(aV); }
          if constexpr (s >= 18 && s < 26) {
            constexpr int e = s - 18, f = e >> 1, o = (f & 1) * 8 + (e & 1) * 4;
            if constexpr (f < 2) { kvf[(e & 1) * 2] = pk(fm(aK[o], rstd), fm(aK[o + 1], rstd)); kvf[(e & 1) * 2 + 1] = pk(fm(aK[o + 2], rstd), fm(aK[o + 3], rstd)); }
            else { const f32x4 r4 = rsv[o >> 2]; kvf[(e & 1) * 2] = pk(fm(aV[o], r4[0]), fm(aV[o + 1], r4[1])); kvf[(e & 1) * 2 + 1] = pk(fm(aV[o + 2], r4[2]), fm(aV[o + 3], r4[3])); }
            if constexpr (e & 1) { gst(rs, kvf, l16 + f * KB, kvo); ldsw(kvl + f * KB, kvf); }
          }
          SCHED();
        });
        const float qs = rstd * (0.25f * LOG2E);
        ldsw(L + L_QB + w * KB + l16, v4u{pk(fm(aQ[8], qs), fm(aQ[9], qs)), pk(fm(aQ[10], qs), fm(aQ[11], qs)), pk(fm(aQ[12], qs), fm(aQ[13], qs)), pk(fm(aQ[14], qs), fm(aQ[15], qs))});
        Qa = v4u{pk(fm(aQ[0], qs), fm(aQ[1], qs)), pk(fm(aQ[2], qs), fm(aQ[3], qs)), pk(fm(aQ[4], qs), fm(aQ[5], qs)), pk(fm(aQ[6], qs), fm(aQ[7], qs))};
        STAMP(sb + 2);
        BARL();
        STAMP(sb + 3);
      }
      {
        LANE_VALS();
        FILL_DEF();
        auto kvpub = [&]() AI { WAITV(0); if (lds_last(L, SY_KV, lane)) set_flag(ws, fl + F_KV, me, epoch); };
        kvpub();
        if (w == 0 && g >= 1) {
          __builtin_amdgcn_s_sleep(PD_KV); poll(flag_ptr(ws, fl + F_KV, me - 16), lane == 0, epoch, dead);
          const int src = act + A_KV + (me - 16) * 16 * KB;
          dma4<0, BUF_B + 16 * KB>(rs, src, 0, l16); dma4<4 * KB, BUF_B + 20 * KB>(rs, src, 0, l16);
          dma4<8 * KB, BUF_B + 24 * KB>(rs, src, 0, l16); dma4<12 * KB, BUF_B + 28 * KB>(rs, src, 0, l16);
        }
        STAMP(sb + 19);
        const v4u of = attn_item<0>(L, Qa, lane, w, g, []() {}, [&]() AI {
          STAMP(sb + 4);
          WAITV(0);
          BAR();
          STAMP(sb + 5);
        }, fill);
        gst(rs, of, l16, act + A_OT + (tta * 32 + 2 * m) * KB);
#pragma unroll 1
        while (nd < 5) fill();
        STAMP(sb + 6);
        WAITV(0);
        BAR();
        if (tid == 0) set_flag(ws, fl + F_O, me, epoch);
        STAMP(sb + 7);
        if (w == 0) { __builtin_amdgcn_s_sleep(PD_O); poll(flag_ptr(ws, fl + F_O, g * 16 + (lane & 15)), lane < 16, epoch, dead); lds_set(L, SY_GO, 3 * layer + 1); }
        else lds_wait(L, SY_GO, 3 * layer + 1);
        STAMP(sb + 8);
      }
      {
        LANE_VALS();
        const int oo = ops(act + A_OT + (ttr * 32 + 16 * kh) * KB);
#pragma unroll
        for (int i = 0; i < 16; ++i) xf[i] = gldf(rs, l16, oo, i);
        SCHED();
        f32x16 keep = {}, send = {};
        const int tro = tr_off(lane);
        lptr k0 = L + opv(BUF_D + 3 * kh * KB + tro), s0 = L + opv(BUF_D + (1 + kh) * KB + tro);
        lptr k1 = L + opv(BUF_A + 3 * kh * KB + tro), s1 = L + opv(BUF_A + (1 + kh) * KB + tro);
        constexpr int PD = 3;
        v4u wk[PD], wsd[PD];
#pragma unroll
        for (int i = 0; i < PD; ++i) { wk[i] = trr(k0 + i * 4 * KB); wsd[i] = trr(s0 + i * 4 * KB); }
        sfor<16>([&](auto S_) AI {
          constexpr int s = decltype(S_)::v, n = s + PD;
          const v4u ck = wk[s % PD], cs = wsd[s % PD];
          if constexpr (n < 8) { wk[s % PD] = trr(k0 + n * 4 * KB); wsd[s % PD] = trr(s0 + n * 4 * KB); }
          else if constexpr (n < 16) { wk[s % PD] = trr(k1 + (n - 8) * 4 * KB); wsd[s % PD] = trr(s1 + (n - 8) * 4 * KB); }
          send = mfma(cs, xf[s], send);
          keep = mfma(ck, xf[s], keep);
          PINA(send); PINA(keep);
          SCHED();
        });
        send_tile(L, send, w, lane);
        lds_set(L, SY_EX + 4 * w, 2 * layer + 1);
        STAMP(sb + 9);
        lds_wait(L, SY_EX + 4 * (w ^ 2), 2 * layer + 1);
        float v[16];
        finish_tile(L, keep, res, v, w, lane);
        const int so = act + A_X1T + (ttr * 32 + 2 * ftile) * KB;
        gst(rs, res[0], l16, so); gst(rs, res[1], l16 + KB, so);
        store_stats(rs, v, lane, act + A_ST1 + ttr * 4096 + (ftile >> 1) * 512 + (ftile & 1) * 8);
        STAMP(sb + 10);
        WAITV(0);
        if (lds_last(L, SY_DR, lane)) set_flag(ws, fl + F_X1, me, epoch);
        if (w == 0) {
          STAMP(sb + 11);
          __builtin_amdgcn_s_sleep(PD_X1); poll(flag_ptr(ws, fl + F_X1, g * 16 + (lane & 15)), lane < 16, epoch, dead);
          lds_set(L, SY_GO, 3 * layer + 2);
        } else lds_wait(L, SY_GO, 3 * layer + 2);
        STAMP(sb + 12);
      }
      {
        LANE_VALS();
        constexpr int WIN = 12;
        v4u st[4];
        const int sto = ops(act + A_ST1 + tta * 4096), xo = ops(act + A_X1T + tta * 32 * KB);
#pragma unroll
        for (int i = 0; i < 4; ++i) xf[i] = gldf(rs, l16, xo, i);
#pragma unroll
        for (int i = 0; i < 4; ++i) st[i] = gld(rs, hf * 2048 + r32 * 16 + i * 512, sto);
#pragma unroll
        for (int i = 4; i < WIN; ++i) xf[i] = gldf(rs, l16, xo, i);
        SCHED();
        lds_wait(L, SY_HWL, 4 * layer + 4);
        f32x16 acc[3];
        acc[0] = f32x16{}; acc[1] = f32x16{}; acc[2] = f32x16{};
        float mean, rstd, c1, sd; v4u xe;
        lptr pbuf[4] = {L + BUF_A + l16, L + opv(BUF_B + l16), L + opv(BUF_C + l16), L + opv(BUF_D + l16)};
        constexpr int PQ = 4;
        v4u wq[PQ];
        {
          constexpr int PD = 3;
          v4u w0[PD], w1[PD];
          lptr p0 = pbuf[2], p1 = pbuf[1];
#pragma unroll
          for (int i = 0; i < PD; ++i) { w0[i] = ldsr(p0 + i * KB); w1[i] = ldsr(p1 + i * KB); }
          sfor<33>([&](auto S_) AI {
            constexpr int s = decltype(S_)::v, n = s + PD;
            if constexpr (s + WIN < 32) xf[s + WIN] = gldf(rs, l16, xo, s + WIN);
            const v4u c0 = w0[s % PD], cc1 = w1[s % PD];
            if constexpr (n == 17) BAR();
            if constexpr (n < 33) { w0[s % PD] = ldsr(p0 + n * KB); w1[s % PD] = ldsr(p1 + n * KB); }
            const v4u xa = s < 32 ? xf[s < 32 ? s : 0] : xe;
            acc[0] = mfma(c0, xa, acc[0]);
            acc[1] = mfma(cc1, xa, acc[1]);
            PINA(acc[0]); PINA(acc[1]);
            if constexpr (s >= 17) { acc[2] = mfma(wq[(s - 17) % PQ], xf[s - 17], acc[2]); PINA(acc[2]); }
            if constexpr (n >= 17) wq[(n - 17) % PQ] = ldsr(pbuf[3] + (n - 17) * KB);
            if constexpr (s == 6) { finish_stats(st, mean, rstd); xe = mean_frag(mean); c1 = -LOG2E * rstd; sd = __builtin_amdgcn_rcpf(rstd); }
            SCHED();
          });
        }
        STAMP(sb + 13);
        const int ho = ops(act + A_HT + (tta * 112 + 7 * m) * KB);
        float ev[8]; v4u hw4;
        auto epi = [&](const f32x16& a, int k, int tile) AI {
          const int e = k >> 1;
          if (!(k & 1)) ev[e] = __builtin_fmaf(__builtin_amdgcn_exp2f(a[e] * c1), sd, sd);
          else {
            ev[e] = fm(a[8 + e], __builtin_amdgcn_rcpf(ev[e]));
            if (e & 1) hw4[e >> 1] = pk(ev[e - 1], ev[e]);
            if (e == 7) gst(rs, hw4, l16 + (tile & 3) * KB, ho + (tile >> 2) * 4 * KB);
          }
        };
        constexpr int DWIN = 16;
        v4u hfr[32];
        const int hb = ops(act + A_HT + (ttr * 112 + 56 * kh) * KB), fbo = ops(fb);
        auto hld = [&](int s) AI {
          const int t = s >> 3, jj = s & 7;
          return gld(rs, l16 + (t & 3) * KB, hb + (((jj + fbo) & 7) * 7 + (t & 4)) * KB);
        };
        auto hpub = [&](unsigned nt) AI { if (lds_last(L, SY_H, lane)) set_flag(ws, fl + F_H + nt - 2, me, epoch * 8u + nt); };
        sfor<149>([&](auto U_) AI {
          constexpr int u = decltype(U_)::v + 16, t = 2 + u / 33, s = u % 33, n = s + PQ - 1;
          constexpr int bufs[8] = {2, 1, 3, 0, 2, 1, 3, 0};
          const v4u cq = wq[u % PQ];
          if constexpr (u == 17) { BAR(); STAMP(sb + 27); }
          if constexpr (n == 33 && t < 6) { if constexpr (PUBMODE == 0 && t >= 4) WAITV(1); BAR(); }
          if constexpr (PUBMODE == 1 && t == 4 && s == HPUBS) { WAITV(1); hpub(2); }
          if constexpr (PUBMODE == 1 && t >= 5 && s == HPUBS) { WAITV(0); hpub(t - 1); }
          if constexpr (EARLY_FREE && t >= 3 && t <= 5 && s == 1) BAR();
          if constexpr (t == 6 && s == 17) { if constexpr (PUBMODE == 0) WAITV(1); BAR(); }
          if constexpr (n < 33) wq[(u + PQ - 1) % PQ] = ldsr(pbuf[bufs[t]] + n * KB);
          else if constexpr (t < 6) wq[(u + PQ - 1) % PQ] = ldsr(pbuf[bufs[t + 1]] + (n - 33) * KB);
          if constexpr (s == 0) acc[t % 3] = mfma(cq, xf[0], f32x16{});
          else acc[t % 3] = mfma(cq, s < 32 ? xf[s < 32 ? s : 0] : xe, acc[t % 3]);
          PINA(acc[t % 3]);
          if constexpr (t == 2) { if constexpr (s >= 17) epi(acc[0], s - 17, 0); }
          else if constexpr (t == 3) { if constexpr (s < 16) epi(acc[1], s, 1); if constexpr (s >= 17) epi(acc[2], s - 17, 2); }
          else if constexpr (s >= 1 && s < 17) epi(acc[(t - 1) % 3], s - 1, t - 1);
          if constexpr (t == 6 && s >= 17 && (s - 17) % (16 / HPRE) == 0) hfr[(s - 17) / (16 / HPRE)] = hld((s - 17) / (16 / HPRE));
          if constexpr (s == 32 && t < 6) STAMP(sb + 21 + t);
          SCHED();
        });
        STAMP(sb + 14);
        BAR();
        STAMP(sb + 15);
        f32x16 keep = {}, send = {};
        constexpr int PD = PDD;
        static constexpr int dbufs[7] = {0, 2, 1, 3, 0, 2, 1};
        const int ko = 3 * kh * KB, sdo = (1 - 2 * kh) * KB;
        lptr kb[4] = {pbuf[0] + ko, pbuf[1] + ko, pbuf[2] + ko, pbuf[3] + ko};
        lptr sp[4] = {kb[0] + sdo, kb[1] + sdo, kb[2] + sdo, kb[3] + sdo};
        v4u wk[PD], wsd[PD];
#pragma unroll
        for (int i = 0; i < PD; ++i) { wk[i] = ldsr(kb[dbufs[0]] + i * 4 * KB); wsd[i] = ldsr(sp[dbufs[0]] + i * 4 * KB); }
        constexpr auto nld = [](int s) { int c = 0; for (int i = 0; i < s; ++i) c += (i + HPRE < 24 && i < 13) + (i >= 13 && i < 45) + (HPRE == 8 && i < 6 && (i & 1)); return c; };
        sfor<56>([&](auto S_) AI {
          constexpr int s = decltype(S_)::v, n = s + PD;
          constexpr int ld = s + HPRE < 24 && s < 13 ? s + HPRE : -1, ld2 = (s >= 13 && s < 45) ? s + 11 : -1, ld3 = (HPRE == 8 && s < 6 && (s & 1)) ? 21 + s / 2 : -1;
          const v4u ck = wk[s % PD], cs = wsd[s % PD];
          if constexpr (n < 56 && (n & 7) == 0) {
            if constexpr (n == 8 && H6S >= 8 - PD) { if constexpr (HPRE == 16) WAITV(16); else WAITV(8); }
            if constexpr (n == 24 && H7S >= 24 - PD) asm volatile("s_waitcnt vmcnt(%0)" :: "n"(nld(24 - PD) - nld(16 / E6)) : "memory");
            BAR();
            if constexpr (n >= 24 && n <= 40) STAMP(sb + 17 + n / 8);
          }
          if constexpr (ld >= 0) hfr[ld % 32] = hld(ld);
          if constexpr (ld2 >= 0) hfr[ld2 % 32] = hld(ld2);
          if constexpr (ld3 >= 0) hfr[ld3 % 32] = hld(ld3);
          if constexpr (n < 56) { wk[s % PD] = ldsr(kb[dbufs[n >> 3]] + (n & 7) * 4 * KB); wsd[s % PD] = ldsr(sp[dbufs[n >> 3]] + (n & 7) * 4 * KB); }
          send = mfma(cs, hfr[s % 32], send);
          keep = mfma(ck, hfr[s % 32], keep);
          PINA(send); PINA(keep);
          if constexpr (s < 16 / E6) { sfor<E6>([&](auto K_) AI { epi(acc[6 % 3], E6 * s + decltype(K_)::v, 6); }); }
          if constexpr (H6S < 8 - PD && s == H6S) { if constexpr (HPRE == 16) WAITV(16); else WAITV(8); hpub(6); }
          if constexpr (H7S < 24 - PD && s == H7S) { asm volatile("s_waitcnt vmcnt(%0)" :: "n"(nld(H7S + 1) - nld(16 / E6)) : "memory"); hpub(7); }
          SCHED();
        });
        send_tile(L, send, w, lane);
        lds_set(L, SY_EX + 4 * w, 2 * layer + 2);
        STAMP(sb + 16);
        lds_wait(L, SY_EX + 4 * (w ^ 2), 2 * layer + 2);
        float v[16];
        finish_tile(L, keep, res, v, w, lane);
        if (layer == 0) {
          const int nact = act + A_LAYER;
          const int so = nact + A_XT + (ttr * 32 + 2 * ftile) * KB;
          gst(rs, res[0], l16, so); gst(rs, res[1], l16 + KB, so);
          store_stats(rs, v, lane, nact + A_ST0 + ttr * 4096 + (ftile >> 1) * 512 + (ftile & 1) * 8);
          STAMP(sb + 17);
          WAITV(0);
          if (lds_last(L, SY_DR, lane)) set_flag(ws, fl + F_X2, me, epoch);
          if (w == 0) {
            STAMP(sb + 18);
            __builtin_amdgcn_s_sleep(PD_X2); poll(flag_ptr(ws, fl + F_X2, g * 16 + (lane & 15)), lane < 16, epoch, dead);
            lds_set(L, SY_GO, 3);
          } else lds_wait(L, SY_GO, 3);
        } else {
          GAS v4u* od = (GAS v4u*)(p.out + (size_t)(ttr * 32 + r32) * 512 + ftile * 32 + hf * 8);
          od[0] = res[0]; od[2] = res[1];
          STAMP(sb + 17);
        }
      }
    }
  } else {
    const int hw = wave - 4;
    const int lb = hw * 8 * KB;
    const int wc = WS_WC + m * 231 * KB + lb, wdn = WS_WD + fb * 224 * KB + lb;
    const rsrc_t rw = __builtin_amdgcn_make_buffer_rsrc((void*)p.wqkv, 0, 512 * 1536 * 2, 0x00020000);
    constexpr int KS = 16 * 1536 * 2;
    const int l16h = (tid & 63) * 16, vraw = ((tid & 63) >> 2) * 3072 + (tid & 3) * 16;
    const int wkv = hw * 4 * KS + 64 * m, wqq = hw * 8 * KS + 64 * m;
#define DMA(EXTRA, base, G, BUF) dma_chunk<EXTRA, G, BUF>(rs, base, lb, l16h)
#define DMA_Q() sfor<8>([&](auto I_) AI { constexpr int i = decltype(I_)::v; dma1p<i * KS, BUF_C + i * KB>(rw, wqq, lb, vraw); })
#define STAGE(arr, base, G) do { const int o_ = ops(base) + (G); _Pragma("unroll") for (int i_ = 0; i_ < 8; ++i_) arr[i_] = gldf(rs, l16, o_, i_); } while (0)
#define ZERO(arr) do { _Pragma("unroll") for (int i_ = 0; i_ < 8; ++i_) arr[i_] = v4u{0u, 0u, 0u, 0u}; } while (0)
#define PIN(arr) asm volatile("" : "+v"(arr[0]), "+v"(arr[1]), "+v"(arr[2]), "+v"(arr[3]), "+v"(arr[4]), "+v"(arr[5]), "+v"(arr[6]), "+v"(arr[7]))
#define PUT(arr, BUF) do { if (DBG_NOPUT) { PIN(arr); break; } lptr d_ = L + opv((BUF) + lb + l16); _Pragma("unroll") for (int i_ = 0; i_ < 8; ++i_) ldsw(d_ + i_ * KB, arr[i_]); } while (0)
#define GET(arr, BUF) do { lptr d_ = L + opv((BUF) + lb + l16); _Pragma("unroll") for (int i_ = 0; i_ < 8; ++i_) arr[i_] = ldsr(d_ + i_ * KB); } while (0)
    const int vpd = ((MAPV == 0 ? (g & 1) * 2 + th : g & 3) * 64 + (tid & 63)) * 128, vpc = ((MAPV == 0 ? (g & 1) * 2 : g & 3) * 64 + (tid & 63)) * 128;
#define PFC(c) do { if (hw == 3) touch<(c) * CH>(rs, wc - lb, vpc); if (MAPV == 0 && hw == 2) touch<(c) * CH + 8 * KB>(rs, wc - lb, vpc); } while (0)
#define PFD(c) do { if (hw == 3) touch<(c) * 32 * KB>(rs, wdn - lb, vpd); } while (0)
#define HFLAG(nt) do { if (tid == 320) set_flag(ws, fl + F_H + (nt) - 2, me, epoch * 8u + (nt)); } while (0)
#define BARL8() asm volatile("s_waitcnt lgkmcnt(8)\n\ts_barrier" ::: "memory")
#define DBG_NOPUT 0
#define HISSUE(nt) do { if (hw == 0) hpoll_issue(rs, hco + ((nt) - 2) * 256 * FLAG_STRIDE, vc); } while (0)
#define HGATE(nt, younger) do { if (hw == 0) { WAITV(younger); hgate(rs, L, hco + ((nt) - 2) * 256 * FLAG_STRIDE, vc, lane, epoch * 8u + (nt), dead); } } while (0)
    v4u a0[8], a1[8];
    v4u uw[8];
    {
      LANE_VALS();
      sfor<16>([&](auto I_) AI { constexpr int i = decltype(I_)::v; dma1p<((i >> 3) * 16 + ((i & 7) >> 1)) * KS + ((i & 1) ? 2048 : 1024), (i >> 3) * CH + (i & 7) * KB>(rw, wkv, lb, vraw); });
      DMA_Q();
      STAMP(0);
      WAITV(16); BAR();
      STAMP(1);
      __builtin_amdgcn_s_setprio(HPRIO);
      const int tro = tr_off(lane);
      float c[6] = {0.f, 0.f, 0.f, 0.f, 0.f, 0.f};
      {
        lptr ta = L + opv(BUF_A + lb + tro);
#pragma unroll
        for (int i = 0; i < 4; ++i) { c[0] = sum8(trr(ta + 2 * i * KB), c[0]); c[1] = sum8(trr(ta + (2 * i + 1) * KB), c[1]); }
      }
      GET(a0, BUF_A); PIN(a0);
      WAITV(0); BAR();
      STAMP(2);
      {
        lptr tb = L + opv(BUF_B + lb + tro), tc = L + opv(BUF_C + lb + tro);
#pragma unroll
        for (int i = 0; i < 4; ++i) {
          c[3] = sum8(trr(tb + 2 * i * KB), c[3]); c[4] = sum8(trr(tb + (2 * i + 1) * KB), c[4]);
          c[2] = sum8(trr(tc + 2 * i * KB), c[2]); c[5] = sum8(trr(tc + (2 * i + 1) * KB), c[5]);
        }
        const unsigned hk = hilo(xhalf_sum(c[0] + c[3])), hv = hilo(xhalf_sum(c[1] + c[4])), hq = hilo(xhalf_sum(c[2] + c[5]));
        if (lane < 32) {
          lptr xp = L + L_X + (hw >> 1) * 512 + (hw & 1) * 8 + l16;
          *(LAS v2u*)xp = v2u{hk, hk}; *(LAS v2u*)(xp + KB) = v2u{hv, hv}; *(LAS v2u*)(xp + 2 * KB) = v2u{hq, hq};
        }
      }
      STAMP(5);
      {
        const int row = lane >> 3, pc = lane & 7, r = b < 168 ? b - 112 : 0;
        const bool gu = b < 112;
        const u16* s0 = gu ? p.wg + (size_t)((b & 3) * 128 + 32 * hw + row) * 1792 + 64 * (b >> 2) + 8 * pc : p.wd + (size_t)((r % 7) * 256 + 64 * hw + row) * 512 + 64 * (r / 7) + 8 * pc;
        const u16* s1 = gu ? s0 + (p.wu - p.wg) : s0 + 32 * 512;
        const int st8 = gu ? 8 * 1792 : 8 * 512;
#pragma unroll
        for (int i = 0; i < 8; ++i) uw[i] = *(const GAS v4u*)((i < 4 ? s0 : s1) + (i & 3) * st8);
        SCHED();
      }
      GET(a1, BUF_B); PIN(a1);
      __builtin_amdgcn_s_setprio(0);
      BARL();
      STAMP(3);
    }
    const unsigned epoch = __builtin_amdgcn_readfirstlane(*(volatile LAS unsigned*)(L + L_SY + SY_EP));
#pragma unroll 1
    for (int layer = 0; layer < 2; ++layer) {
      LANE_VALS();
      const int act = WS_ACT + layer * A_LAYER;
      const int fl = layer * F_PER_LAYER;
      const int sb = 2 + layer * 28;
      (void)sb;
      v4u d2[8], d3[8], d4[8], d5[8], d6[8];
      if (layer == 1) {
        BARL();
      if (b == 0 && hw == 2) {
        for (unsigned n = 0; n < (1u << 16); ++n) {
          gu32* f = flag_ptr(ws, F_P2, lane);
          const unsigned v0 = __hip_atomic_load(f, RLX_AGENT), v1 = __hip_atomic_load(f + 16 * FLAG_STRIDE, RLX_AGENT);
          const unsigned v2 = __hip_atomic_load(f + 32 * FLAG_STRIDE, RLX_AGENT), v3 = __hip_atomic_load(f + 48 * FLAG_STRIDE, RLX_AGENT);
          if (__all(v0 == epoch && v1 == epoch && v2 == epoch && v3 == epoch)) break;
        }
        if (lane == 0) __hip_atomic_store((gu32*)(ws + WS_EPOCH), epoch, RLX_AGENT);
      }
        STAMP(sb + 0);
        WAITV(0);

        BAR();

        BAR();
        STAMP(sb + 1);
      }

      STAMP(sb + 2);
      BAR();

      {
        const int tq = 3 - hw;
        const v4u Qb = ldsr(L + L_QB + tq * KB + l16);
        FILL_DEF();
        auto unit = [&]() AI {
          if (layer == 0 && b < 168) {
            const int row = lane >> 3, pc = lane & 7, r = b - 112, ufb = r / 7;
            const bool gu = b < 112;
            lptr d0 = L + BUF_D + row * 64 + (gu ? (pc >> 1) * 8 * KB + 2 * hw * KB + (pc & 1) * 16 : ((hw >> 1) * 2 + (pc >> 2)) * 8 * KB + (hw & 1) * 4 * KB + (pc & 3) * 16);
            lptr d1 = d0 + (gu ? 32 : 2 * KB);
            if (UNIT_W == 0) {
#pragma unroll
              for (int i = 0; i < 8; ++i) ldsw((i < 4 ? d0 : d1) + ((i & 3) >> 1) * KB + (i & 1) * 512, uw[i]);
              lds_add(L, SY_U2, lane);
            }
            lds_wait(L, SY_U2, 4);
            const int ku0 = gu ? 8 * (b & 3) : 8 * (2 * (r % 7) + (hw >> 1));
            const int ubase = gu ? WS_WC + (((b >> 2) * 4 + hw) * 33 + ku0) * KB : WS_WD + (ufb * 224 + (hw & 1)) * KB;
            float c0 = 0.f, c1 = 0.f;
            int j = ku0 / 7, t = ku0 % 7;
            lptr tp = L + BUF_D + lb + tr_off(lane);
#pragma unroll
            for (int u = 0; u < 8; ++u) {
              const v4u f = trr(tp + u * KB);
              gst(rs, f, l16, ubase + (gu ? u : (t * 8 + ((j - ufb) & 7)) * 4 + (j >> 3) * 2) * KB);
              if (++t == 7) { t = 0; ++j; }
              if (u & 1) c1 = sum8(f, c1); else c0 = sum8(f, c0);
            }
            if (gu) { const unsigned wd = hilo(xhalf_sum(c0 + c1)); if (lane < 32) gst2(rs, v2u{wd, wd}, l16, ubase + (32 - ku0) * KB + ((b & 3) >> 1) * 512 + (b & 1) * 8); }
          }
        };
        auto unit_w = [&]() AI {
          if (UNIT_W == 1 && layer == 0 && b < 168) {
            const int row = lane >> 3, pc = lane & 7;
            const bool gu = b < 112;
            lptr d0 = L + BUF_D + row * 64 + (gu ? (pc >> 1) * 8 * KB + 2 * hw * KB + (pc & 1) * 16 : ((hw >> 1) * 2 + (pc >> 2)) * 8 * KB + (hw & 1) * 4 * KB + (pc & 3) * 16);
            lptr d1 = d0 + (gu ? 32 : 2 * KB);
#pragma unroll
            for (int i = 0; i < 8; ++i) ldsw((i < 4 ? d0 : d1) + ((i & 3) >> 1) * KB + (i & 1) * 512, uw[i]);
            lds_add(L, SY_U2, lane);
          }
        };
        if (hw == 3 && g >= 2) {
          __builtin_amdgcn_s_sleep(PD_KV + 4); poll(flag_ptr(ws, fl + F_KV, me - 32), lane == 0, epoch, dead);
          const int s2 = act + A_KV + (me - 32) * 16 * KB;
          dma4<0, BUF_B>(rs, s2, 0, l16); dma4<4 * KB, BUF_B + 4 * KB>(rs, s2, 0, l16);
          dma4<8 * KB, BUF_B + 8 * KB>(rs, s2, 0, l16); dma4<12 * KB, BUF_B + 12 * KB>(rs, s2, 0, l16);
        }
        const v4u of = attn_item<1>(L, Qb, lane, tq, g, unit_w, [&]() AI {
          if (hw == 3) WAITV(0);
          unit();
          STAMP(sb + 4);
          BARL();

        }, fill);
        gst(rs, of, l16, act + A_OT + ((4 * g + tq) * 32 + 2 * m + 1) * KB);
#pragma unroll 1
        while (nd < 5) fill();
        STAMP(sb + 6);
        WAITV(0);

        BAR();
      }
      if (layer == 0) {
        if (hw == 0) {
          if (lane == 0) set_flag(ws, F_P2, me, epoch);
          int qq;
          if (lane < 7) qq = 112 + 7 * fb + lane;
          else { int n = (7 * m) / 4 + ((lane - 7) >> 2); n = n > 27 ? 27 : n; qq = 4 * n + ((lane - 7) & 3); }
          poll(flag_ptr(ws, F_P2, lane < 19 ? wg_index(qq) : 0), lane < 19, epoch, dead);
          lds_set(L, SY_U, 1);
        } else lds_wait(L, SY_U, 1);
        DMA(true, wc, 0, BUF_C);
      }
      if (hw == 1) ot<OT_O, A_OT>(rs, act + (4 * g * 32 + 2 * m) * KB, (lane >> 4) * 32 * KB + (lane & 15) * 128);
      DMA(true, wc, 1 * CH, BUF_B);
      STAMP(sb + 7);
      lds_wait(L, SY_GO, 3 * layer + 1);
      __builtin_amdgcn_s_sleep(14);
      WAITV(0);
      lds_add(L, SY_HWL, lane);
      STAGE(d6, wdn, 6 * 32 * KB); STAGE(d5, wdn, 5 * 32 * KB);
      STAMP(sb + 9);
      lds_wait(L, SY_DR, 8 * layer + 4);
      const int vx = (lane >> 5) * 32 * KB + (lane & 31) * 128, vs = lane < 8 ? (lane >> 2) * 4096 + (lane & 3) * 128 : WS_BYTES;
      const int xoo = ((4 * g + 2 * th) * 32 + 4 * fb) * KB, soo = (4 * g + 2 * th) * 4096 + fb * 512;
      if (hw == 1) ot<OT_X, A_X1T>(rs, act + xoo, vx);
      if (hw == 2) ot<OT_X, A_ST1>(rs, act + soo, vs);
      DMA(true, wc, 2 * CH, BUF_D); DMA(true, wc, 3 * CH, BUF_A);
      STAMP(sb + 11);
      WAITV(8); BAR();

      BAR();
      STAMP(sb + 13);
      DMA(true, wc, 4 * CH, BUF_C); DMA(true, wc, 5 * CH, BUF_B);
      WAITV(16); BAR();
      STAGE(d4, wdn, 4 * 32 * KB); STAGE(d3, wdn, 3 * 32 * KB);
      const int hoo = act + A_HT + ((4 * g + 2 * (hw - 2)) * 112 + 7 * m) * KB;
      const int vh = (lane & 24) ? WS_BYTES : (lane >> 5) * 112 * KB + (lane & 7) * 128;
      const int hco = WS_FLAGS + ((fl + F_H) * 256 + g * 16) * FLAG_STRIDE, vc = lane < 16 ? lane * FLAG_STRIDE : WS_BYTES;

      WAITV(24); BAR();

      DMA(true, wc, 6 * CH, BUF_D); STAGE(d2, wdn, 2 * 32 * KB);
      WAITV(32); BAR();
      if (PUBMODE == 0) HFLAG(3);
      DMA(false, wdn, 0 * 32 * KB, BUF_A);
      WAITV(16); BAR();
      if (PUBMODE == 0) HFLAG(4);
      HISSUE(L2);
      DMA(false, wdn, 1 * 32 * KB, BUF_C);
      HGATE(L2, 8);
      STAMP(sb + 14);
      BAR();
      WAITV(0); PIN(d6); PIN(d5); PIN(d4); PIN(d3); PIN(d2);
      if (PUBMODE == 0) HFLAG(5);
      if (L3 != L2) { HISSUE(L3); HGATE(L3, 0); }
      BAR();
      PUT(d2, BUF_B);

      if (L3 != 4) HISSUE(4);
      BAR();
      if (H6S >= 8 - PDD) HFLAG(6);
      PUT(d3, BUF_D);
      if (L3 != 4) HGATE(4, 0);
      if (HI_EARLY == 1) HISSUE(5);
      STAMP(sb + 15);
      BARL8();
      if (HI_EARLY == 0) HISSUE(5);
      PUT(d4, BUF_A);

      if (HI_EARLY == 2 && hw == 0) { __builtin_amdgcn_s_sleep(HD5); HISSUE(5); }
      HGATE(5, 0);
      if (HI_EARLY == 1) HISSUE(6);
      BARL8();
      if (H7S >= 24 - PDD) HFLAG(7);
      if (HI_EARLY == 0) HISSUE(6);
      PUT(d5, BUF_C);
      if (HI_EARLY == 2 && hw == 0) { __builtin_amdgcn_s_sleep(HD6); HISSUE(6); }
      HGATE(6, 0);
      if (HI_EARLY == 1) HISSUE(7);
      BARL8();
      if (HI_EARLY == 0) HISSUE(7);
      PUT(d6, BUF_B);

      if (HI_EARLY == 2 && hw == 0) { __builtin_amdgcn_s_sleep(HD7); HISSUE(7); }
      HGATE(7, 0);
      STAMP(sb + 16);
      BARL8();
      BARL();
      if (layer == 0) {
        PUT(a0, BUF_A);
        lds_wait4(L, 2);
        PUT(a1, BUF_B);
        DMA_Q();
        lds_wait(L, SY_DR, 8);
        if (hw == 1) ot<OT_X, A_LAYER + A_XT>(rs, act + xoo, vx);
        if (hw == 2) ot<OT_X, A_LAYER + A_ST0>(rs, act + soo, vs);
      }
      ZERO(uw);
    }
  }
}
}

extern "C" void kernel_launch(void* const* d_in, const int* in_sizes, int n_in, void* d_out, int out_size, void* d_ws, size_t ws_size,
                              hipStream_t stream) {
  Params p;
  p.x = (const u16*)d_in[0]; p.wqkv = (const u16*)d_in[1]; p.wo = (const u16*)d_in[2];
  p.wg = (const u16*)d_in[4]; p.wu = (const u16*)d_in[5]; p.wd = (const u16*)d_in[6];
  p.out = (u16*)d_out; p.ws = (unsigned char*)d_ws;
  (void)hipFuncSetAttribute((const void*)fused_layers, hipFuncAttributeMaxDynamicSharedMemorySize, LDS_BYTES);
  hipLaunchKernelGGL(fused_layers, dim3(NWG), dim3(NTHR), LDS_BYTES, stream, p);
}
```

```cpp
#define HPRE 8 _PUBMODE=0 _WO_SLEEP=12 _E6=4 _H6S=5 _H7S=18 b3= b5=d6@1 b6=C1,d3@1 go1=d5,d4 dr1=C2 b10=C3,d2 b11=C4 b12=C5,d6@0 b13=C6,d3@0 b14=D0 b15=D1
#define P4AT 1
#define P5AT 1
#define P6AT 0
#define P7AT 1
#define HI_EARLY 1
#define L2 (PUBMODE == 0 ? 3 : 2)
#define L3 (PUBMODE == 0 ? 3 : 4)
#define UNIT_W 1
#define HPRIO 3
#define UNIT_AT 0
#define POLL_FIRST 0
#define START_ORDER 0
#define QSLEEP 8
#include <hip/hip_runtime.h>
#include <hip/hip_bf16.h>
#include <utility>

#define POLL_SLEEP 0
#define UNIT_EARLY 1
#define EARLY_FREE 0
#define HPUBS 10
#define E6 2
#define H7S 19
#define XW0 8
#define XW1 16
#define H6S 5
#define PUBMODE 1
#define PDD 3
#define PD_O 18
#define PD_X1 18
#define PD_X2 18
#define PD_KV 10
#define HSLEEP 2
#define POLLK 0
#define HPOLLBITS "sc1"
#define HD5 0
#define HD6 0
#define HD7 0
#define WO_EARLY 1
#define WO_SLEEP 6
#define KVWIN 12
#define PROF 0

extern "C" __device__ float llvm_fmul_legacy(float, float) __asm("llvm.amdgcn.fmul.legacy");
namespace {
typedef unsigned short u16;
typedef unsigned v4u __attribute__((ext_vector_type(4)));
typedef unsigned v2u __attribute__((ext_vector_type(2)));
typedef short v4s __attribute__((ext_vector_type(4)));
typedef float f32x4 __attribute__((ext_vector_type(4)));
typedef float f32x16 __attribute__((ext_vector_type(16)));
typedef __bf16 v2bf __attribute__((ext_vector_type(2)));
typedef __bf16 v8bf __attribute__((ext_vector_type(8)));
#define LAS __attribute__((address_space(3)))
#define GAS __attribute__((address_space(1)))
#define DI __device__ __forceinline__
typedef GAS unsigned gu32;
typedef LAS unsigned char* lptr;
#define RLX_AGENT __ATOMIC_RELAXED, __HIP_MEMORY_SCOPE_AGENT

constexpr int NWG = 256, NTHR = 512;
constexpr int KB = 1024;
constexpr int FLAG_STRIDE = 256;
enum { F_P2 = 0, F_KV = 1, F_O = 2, F_X1 = 3, F_X2 = 4, F_H = 5, F_PER_LAYER = 10, F_KINDS = 21 };
constexpr int WS_EPOCH = 0;
constexpr int WS_FLAGS = 4 * KB;
constexpr int WS_WC = WS_FLAGS + F_KINDS * 256 * FLAG_STRIDE;
constexpr int WS_WD = WS_WC + 16 * 231 * KB;
constexpr int WS_ACT = WS_WD + 8 * 224 * KB;
constexpr int A_XT = 0, A_ST0 = A_XT + 2048 * KB, A_KV = A_ST0 + 256 * KB, A_OT = A_KV + 4096 * KB, A_X1T = A_OT + 2048 * KB,
              A_ST1 = A_X1T + 2048 * KB, A_HT = A_ST1 + 256 * KB, A_LAYER = A_HT + 64 * 112 * KB;
constexpr int WS_DBG = WS_ACT + 2 * A_LAYER;
constexpr int WS_BYTES = WS_DBG + 256 * KB * 2;
constexpr int CH = 33 * KB;
constexpr int BUF_A = 0, BUF_B = CH, BUF_C = 2 * CH, BUF_D = 3 * CH, SP = 4 * CH;
constexpr int L_KV = SP;
constexpr int L_QB = SP + 16 * KB;
constexpr int L_ONES = SP + 20 * KB;
constexpr int L_SY = L_ONES + 64;
constexpr int SY_GO = 0, SY_DR = 4, SY_HWL = 8, SY_KV = 12, SY_EX = 16, SY_U = 32, SY_X0 = 36, SY_U2 = 40, SY_H = 44, SY_EP = 48;
constexpr int L_RS = L_ONES + 128;
constexpr int L_POLL = L_ONES + 640;
constexpr int L_PF = L_ONES + 1088;
constexpr int L_X = SP + 22 * KB;
constexpr int L_DBG = SP + 25 * KB;
constexpr int LDS_BYTES = SP + (PROF == 1 ? 27 : 25) * KB;

constexpr float NEG_BIG = -1e30f;
constexpr float LOG2E = 1.4426950408889634f;
constexpr int AUX_SC1 = 16;

struct Params {
  const u16 *x, *wqkv, *wo, *wg, *wu, *wd;
  u16* out;
  unsigned char* ws;
};

template <int I> struct IC { static constexpr int v = I; };
template <class F, int... Is> DI void sfor_impl(F& f, std::integer_sequence<int, Is...>) { (f(IC<Is>{}), ...); }
template <int N, class F> DI void sfor(F f) { sfor_impl(f, std::make_integer_sequence<int, N>{}); }
#define AI __attribute__((always_inline))
DI f32x16 mfma(v4u a, v4u b, f32x16 c) {
  return __builtin_amdgcn_mfma_f32_32x32x16_bf16(__builtin_bit_cast(v8bf, a), __builtin_bit_cast(v8bf, b), c, 0, 0, 0);
}
DI unsigned pk(float a, float b) { v2bf v = {(__bf16)a, (__bf16)b}; return __builtin_bit_cast(unsigned, v); }
DI float fm(float a, float b) { return llvm_fmul_legacy(a, b); }
DI float bflo(unsigned w) { return __uint_as_float(w << 16); }
DI float bfhi(unsigned w) { return __uint_as_float(w & 0xffff0000u); }
DI float xhalf_sum(float v) { auto r = __builtin_amdgcn_permlane32_swap(__float_as_uint(v), __float_as_uint(v), false, false); return __uint_as_float(r[0]) + __uint_as_float(r[1]); }
DI float xhalf_max(float v) { auto r = __builtin_amdgcn_permlane32_swap(__float_as_uint(v), __float_as_uint(v), false, false); return __builtin_fmaxf(__uint_as_float(r[0]), __uint_as_float(r[1])); }
DI v4u ldsr(lptr p) { return *(LAS v4u*)p; }
DI void ldsw(lptr p, v4u v) { *(LAS v4u*)p = v; }
typedef __amdgpu_buffer_rsrc_t rsrc_t;
DI v4u gld(rsrc_t rs, int voff, int soff) { return __builtin_amdgcn_raw_buffer_load_b128(rs, voff, soff, AUX_SC1); }
DI v4u gldf(rsrc_t rs, int l16, int soff, int i) { return __builtin_amdgcn_raw_buffer_load_b128(rs, l16 + (i & 3) * KB, soff + (i >> 2) * 4 * KB, AUX_SC1); }
DI int opv(int x) { asm volatile("" : "+v"(x)); return x; }
DI int ops(int x) { asm volatile("" : "+s"(x)); return x; }
DI void gst(rsrc_t rs, v4u v, int voff, int soff) {
  __builtin_amdgcn_raw_buffer_store_b128(v, rs, voff, soff, AUX_SC1);
  asm volatile("s_nop 1" :: "v"(v) : "memory");
}
DI void gst2(rsrc_t rs, v2u v, int voff, int soff) { __builtin_amdgcn_raw_buffer_store_b64(v, rs, voff, soff, AUX_SC1); }
#define SCHED() __builtin_amdgcn_sched_barrier(0)
#define PINA(a) asm volatile("" : "+v"(a))
#define HOLD(a) asm volatile("" :: "v"(a))
#define BAR() asm volatile("s_barrier" ::: "memory")
#define BARL() asm volatile("s_waitcnt lgkmcnt(0)\n\ts_barrier" ::: "memory")
#define WAITV(n) asm volatile("s_waitcnt vmcnt(" #n ")" ::: "memory")

template <int G, int LO> DI void dma4(rsrc_t rs, int sb, int lb, int voff) {
  unsigned keep, so;
  asm volatile("s_mov_b32 %0, m0\n\ts_add_u32 m0, %5, %7\n\ts_add_u32 %1, %4, %6\n\ts_nop 2\n\t"
               "buffer_load_dwordx4 %2, %3, %1 offen sc1 lds\n\tbuffer_load_dwordx4 %2, %3, %1 offen offset:1024 sc1 lds\n\t"
               "buffer_load_dwordx4 %2, %3, %1 offen offset:2048 sc1 lds\n\tbuffer_load_dwordx4 %2, %3, %1 offen offset:3072 sc1 lds\n\t"
               "s_mov_b32 m0, %0" : "=&s"(keep), "=&s"(so) : "v"(voff), "s"(rs), "s"(sb), "s"(lb), "i"(G), "i"(LO) : "memory", "scc");
}
template <int G, int LO> DI void dma1(rsrc_t rs, int sb, int lb, int voff) {
  unsigned keep, so;
  asm volatile("s_mov_b32 %0, m0\n\ts_add_u32 m0, %5, %7\n\ts_add_u32 %1, %4, %6\n\ts_nop 2\n\t"
               "buffer_load_dwordx4 %2, %3, %1 offen sc1 lds\n\t"
               "s_mov_b32 m0, %0" : "=&s"(keep), "=&s"(so) : "v"(voff), "s"(rs), "s"(sb), "s"(lb), "i"(G), "i"(LO) : "memory", "scc");
}
template <int G, int LO> DI void dma1p(rsrc_t rs, int sb, int lb, int voff) {
  unsigned keep, so;
  asm volatile("s_mov_b32 %0, m0\n\ts_add_u32 m0, %5, %7\n\ts_add_u32 %1, %4, %6\n\ts_nop 2\n\t"
               "buffer_load_dwordx4 %2, %3, %1 offen lds\n\t"
               "s_mov_b32 m0, %0" : "=&s"(keep), "=&s"(so) : "v"(voff), "s"(rs), "s"(sb), "s"(lb), "i"(G), "i"(LO) : "memory", "scc");
}
DI int tr_off(int lane) { const int gi = lane >> 4, q4 = (lane & 15) >> 2, pp = lane & 3; return (8 * (gi >> 1) + q4) * 64 + (gi & 1) * 32 + (((pp & 1) << 1) | (pp >> 1)) * 8; }
DI v4u trr(lptr p) {
  const v2u lo = __builtin_bit_cast(v2u, __builtin_amdgcn_ds_read_tr16_b64_v4i16((LAS v4s*)p));
  const v2u hi = __builtin_bit_cast(v2u, __builtin_amdgcn_ds_read_tr16_b64_v4i16((LAS v4s*)(p + 256)));
  return v4u{lo.x, lo.y, hi.x, hi.y};
}
DI float dot2(unsigned a, unsigned b, float c) { return __builtin_amdgcn_fdot2_f32_bf16(__builtin_bit_cast(v2bf, a), __builtin_bit_cast(v2bf, b), c, false); }
DI float sum8(v4u f, float c) {
#pragma unroll
  for (int e = 0; e < 4; ++e) { const unsigned fe = f[e]; c = dot2(fe, 0x3f803f80u, c); }
  return c;
}
DI unsigned hilo(float c) { return pk(c, c - bflo(pk(c, c))); }
template <int G> DI void touch(rsrc_t rs, int sb, int voff) {
  unsigned keep, so;
  asm volatile("s_mov_b32 %0, m0\n\ts_mov_b32 m0, %6\n\ts_add_u32 %1, %4, %5\n\ts_nop 2\n\t"
               "buffer_load_dword %2, %3, %1 offen sc1 lds\n\t"
               "s_mov_b32 m0, %0" : "=&s"(keep), "=&s"(so) : "v"(voff), "s"(rs), "s"(sb), "i"(G), "i"(L_PF) : "memory", "scc");
}
#define OTM 0
enum { OT_KV = 1, OT_O = 2, OT_X = 4, OT_H = 8 };
template <int K, int G> DI void ot(rsrc_t rs, int sb, int voff) { if (OTM & K) touch<G>(rs, sb, voff); }
template <bool EXTRA, int G, int LBUF> DI void dma_chunk(rsrc_t rs, int sb, int lb, int voff) {
  dma4<G, LBUF>(rs, sb, lb, voff);
  dma4<G + 4 * KB, LBUF + 4 * KB>(rs, sb, lb, voff);
  if (EXTRA && lb == 0) dma1<G + 32 * KB, LBUF + 32 * KB>(rs, sb, lb, voff);
}

#define MAPV 1
__host__ __device__ inline int wg_index(int b) {
  if (MAPV == 0) return ((b & 7) * 2 + (b >> 7)) * 16 + ((b >> 3) & 15);
  return (4 * ((b & 7) >> 1) + (b >> 6)) * 16 + 8 * (b & 1) + ((b >> 3) & 7);
}
DI gu32* flag_ptr(unsigned char* ws, int kind, int idx) { return (gu32*)(ws + WS_FLAGS + (size_t)(kind * 256 + idx) * FLAG_STRIDE); }
DI void set_flag(unsigned char* ws, int kind, int idx, unsigned epoch) { __hip_atomic_store(flag_ptr(ws, kind, idx), epoch, RLX_AGENT); }
DI void poll(gu32* f, bool active, unsigned epoch, bool& dead) {
  if (!dead) {
    const unsigned long long t0 = __builtin_amdgcn_s_memrealtime();
#pragma unroll 1
    for (unsigned spins = 1;; ++spins) {
      unsigned v = epoch;
      if (active) v = __hip_atomic_load(f, RLX_AGENT);
      if (__all(v == epoch)) break;
      if ((spins & 63u) == 0u && __builtin_amdgcn_s_memrealtime() - t0 > 2000000ull) { dead = true; break; }
      active = v != epoch;
      if (POLL_SLEEP) __builtin_amdgcn_s_sleep(POLL_SLEEP);
    }
  }
  __builtin_amdgcn_fence(__ATOMIC_ACQUIRE, "wavefront");
}

template <int NT> DI void hpoll_issue(rsrc_t rs, int so, int lane) {
  unsigned keep, o;
  if (lane < 16)
    asm volatile("s_mov_b32 %0, m0\n\ts_mov_b32 m0, %5\n\ts_add_u32 %1, %4, %6\n\ts_nop 2\n\tbuffer_load_dword %2, %3, %1 offen sc1 lds\n\ts_mov_b32 m0, %0"
                 : "=&s"(keep), "=&s"(o) : "v"(lane * FLAG_STRIDE), "s"(rs), "s"(so), "i"(L_POLL + (NT - 2) * 64), "i"((NT - 2) * 256 * FLAG_STRIDE) : "memory", "scc");
}
template <int NT> DI void hgate(rsrc_t rs, lptr L, int so, int lane, unsigned tag, bool& dead) {
  if (!dead) {
    const unsigned long long t0 = __builtin_amdgcn_s_memrealtime();
#pragma unroll 1
    for (unsigned spins = 1;; ++spins) {
      if (__all(*(volatile LAS unsigned*)(L + L_POLL + (NT - 2) * 64 + (lane & 15) * 4) == tag + NT)) break;
      if ((spins & 63u) == 0u && __builtin_amdgcn_s_memrealtime() - t0 > 2000000ull) { dead = true; break; }
      if (HSLEEP) __builtin_amdgcn_s_sleep(HSLEEP);
      hpoll_issue<NT>(rs, so, lane);
      WAITV(0);
    }
  }
  asm volatile("" ::: "memory");
}
DI void lds_set(lptr L, int word, unsigned v) { asm volatile("" ::: "memory"); *(volatile LAS unsigned*)(L + L_SY + word) = v; asm volatile("" ::: "memory"); }
DI void lds_add(lptr L, int word, int lane) {
  asm volatile("" ::: "memory");
  if (lane == 0) __hip_atomic_fetch_add((LAS unsigned*)(L + L_SY + word), 1u, __ATOMIC_RELAXED, __HIP_MEMORY_SCOPE_WORKGROUP);
  asm volatile("" ::: "memory");
}
DI bool lds_last(lptr L, int word, int lane) {
  asm volatile("" ::: "memory");
  unsigned o = 0;
  if (lane == 0) o = __hip_atomic_fetch_add((LAS unsigned*)(L + L_SY + word), 1u, __ATOMIC_RELAXED, __HIP_MEMORY_SCOPE_WORKGROUP);
  asm volatile("" ::: "memory");
  return (o & 3u) == 3u;
}
DI void lds_wait(lptr L, int word, unsigned v) {
#pragma unroll 1
  for (unsigned n = 0; n < (1u << 22); ++n)
    if ((unsigned)__builtin_amdgcn_readfirstlane(*(volatile LAS unsigned*)(L + L_SY + word)) >= v) break;
  asm volatile("" ::: "memory");
}
DI void lds_wait4(lptr L, unsigned v) {
#pragma unroll 1
  for (unsigned n = 0; n < (1u << 22); ++n) {
    const v4u e = *(volatile LAS v4u*)(L + L_SY + SY_EX);
    const unsigned a = e.x < e.y ? e.x : e.y, b = e.z < e.w ? e.z : e.w;
    if ((unsigned)__builtin_amdgcn_readfirstlane(a < b ? a : b) >= v) break;
    __builtin_amdgcn_s_sleep(1);
  }
  asm volatile("" ::: "memory");
}

#define STAMP(i) do {} while (0)

DI void finish_stats2(float s, float q, float& mean, float& rstd) {
  s = xhalf_sum(s); q = xhalf_sum(q);
  mean = s * (1.f / 512.f);
  const float var = __builtin_fmaxf(q * (1.f / 512.f) - mean * mean, 0.f);
  rstd = __builtin_amdgcn_rsqf(var + 1e-5f);
}
DI void finish_stats(const v4u (&st)[4], float& mean, float& rstd) {
  float s = 0.f, q = 0.f;
#pragma unroll
  for (int i = 0; i < 4; ++i) { s += __uint_as_float(st[i].x) + __uint_as_float(st[i].z); q += __uint_as_float(st[i].y) + __uint_as_float(st[i].w); }
  finish_stats2(s, q, mean, rstd);
}
DI v4u mean_frag(float mean) {
  const float nm = -mean;
  const unsigned hh = pk(nm, nm);
  const float lo = nm - bflo(hh);
  const unsigned ll = pk(lo, lo);
  return v4u{hh, ll, hh, ll};
}
DI void store_stats(rsrc_t rs, const float (&v)[16], int lane, int soff) {
  float s = 0.f, q = 0.f;
#pragma unroll
  for (int i = 0; i < 16; ++i) { s += v[i]; q = __builtin_fmaf(v[i], v[i], q); }
  s = xhalf_sum(s); q = xhalf_sum(q);
  if (lane < 32) gst2(rs, v2u{__float_as_uint(s), __float_as_uint(q)}, lane * 16, soff);
}

struct Attn { f32x16 O; float m; };
DI float max16(const f32x16& s, float m) {
#pragma unroll
  for (int i = 0; i < 16; i += 2) m = __builtin_fmaxf(__builtin_fmaxf(m, s[i]), s[i + 1]);
  return m;
}
DI f32x16 pv(const f32x16& s, v4u v0, v4u v1, f32x16 O) {
  float p[16];
#pragma unroll
  for (int i = 0; i < 16; ++i) p[i] = __builtin_amdgcn_exp2f(s[i]);
  const v4u p0 = {pk(p[0], p[1]), pk(p[2], p[3]), pk(p[4], p[5]), pk(p[6], p[7])};
  const v4u p1 = {pk(p[8], p[9]), pk(p[10], p[11]), pk(p[12], p[13]), pk(p[14], p[15])};
  O = mfma(v0, p0, O);
  O = mfma(v1, p1, O);
  return O;
}
template <bool FIRST>
DI void attn_chunk(Attn& A, v4u Q, lptr kp, lptr vp, int vstride, int t0, int t1, int tm, const f32x16& mask) {
  const f32x16 zero = {};
  float mx = FIRST ? NEG_BIG : A.m;
  f32x16 sm = zero;
  if (tm >= 0) { sm = mfma(ldsr(kp + tm * 4096), Q, mask); mx = max16(sm, mx); }
  for (int t = t0; t < t1; ++t) { const f32x16 s = mfma(ldsr(kp + t * 4096), Q, zero); mx = max16(s, mx); }
  mx = xhalf_max(mx);
  if (FIRST) A.O = zero;
  else { const float al = __builtin_amdgcn_exp2f(A.m - mx); A.O *= al; }
  A.m = mx;
  if (tm >= 0) { A.O = pv(sm - mx, ldsr(vp + tm * vstride), ldsr(vp + tm * vstride + 1024), A.O); }
  f32x16 nm;
#pragma unroll
  for (int i = 0; i < 16; ++i) nm[i] = -mx;
  for (int t = t0; t < t1; ++t) {
    const f32x16 s = mfma(ldsr(kp + t * 4096), Q, nm);
    A.O = pv(s, ldsr(vp + t * vstride), ldsr(vp + t * vstride + 1024), A.O);
  }
}
DI float opf(float x) { asm volatile("" : "+v"(x)); return x; }
struct Fast { f32x16 O, nm; };
DI void fast_diag(Fast& F, v4u Q, lptr kp, lptr vp, const f32x16& mask) {
  const v4u k = ldsr(kp), v0 = ldsr(vp), v1 = ldsr(vp + 1024);
  const f32x16 s = mfma(k, Q, mask);
  float m0 = __builtin_fmaxf(s[0], s[1]), m1 = __builtin_fmaxf(s[2], s[3]);
#pragma unroll
  for (int i = 4; i < 16; i += 4) { m0 = __builtin_fmaxf(__builtin_fmaxf(m0, s[i]), s[i + 1]); m1 = __builtin_fmaxf(__builtin_fmaxf(m1, s[i + 2]), s[i + 3]); }
  const float mx = xhalf_max(__builtin_fmaxf(m0, m1));
  f32x16 d;
#pragma unroll
  for (int i = 0; i < 16; ++i) { d[i] = opf(s[i] - mx); F.nm[i] = -mx; }
  F.O = pv(d, v0, v1, f32x16{});
}
DI v4u exp8(const f32x16& s, int o) {
  float p[8];
#pragma unroll
  for (int i = 0; i < 8; ++i) p[i] = __builtin_amdgcn_exp2f(s[o + i]);
  return v4u{pk(p[0], p[1]), pk(p[2], p[3]), pk(p[4], p[5]), pk(p[6], p[7])};
}
template <class Hook>
DI void fast_run(Fast& F, v4u Q, lptr kp, lptr vp, int vstride, int t0, int t1, const f32x16& c0, bool hk, Hook hook) {
  lptr kq = kp + t0 * 4096, vq = vp + t0 * vstride;
  v4u k = ldsr(kq), va0 = ldsr(vq), va1 = ldsr(vq + 1024), vb0, vb1;
  f32x16 sa = mfma(k, Q, c0), sb;
  k = ldsr(kq + 4096);
  auto half = [&](const f32x16& sc, f32x16& sn, v4u vc0, v4u vc1, v4u& vn0, v4u& vn1, lptr kn) AI {
    v4u p0 = exp8(sc, 0);
    PINA(p0);
    SCHED();
    sn = mfma(k, Q, F.nm); PINA(sn); SCHED();
    const v4u ko = k;
    vq += vstride; vn0 = ldsr(vq); vn1 = ldsr(vq + 1024); k = ldsr(kn);
    HOLD(ko); SCHED();
    float e[4];
#pragma unroll
    for (int i = 0; i < 4; ++i) e[i] = __builtin_amdgcn_exp2f(sc[8 + i]);
    asm volatile("" : "+v"(e[0]), "+v"(e[1]), "+v"(e[2]), "+v"(e[3]));
    SCHED();
    F.O = mfma(vc0, p0, F.O); PINA(F.O); SCHED();
    const v4u p1 = {pk(e[0], e[1]), pk(e[2], e[3]), pk(__builtin_amdgcn_exp2f(sc[12]), __builtin_amdgcn_exp2f(sc[13])), pk(__builtin_amdgcn_exp2f(sc[14]), __builtin_amdgcn_exp2f(sc[15]))};
    SCHED();
    F.O = mfma(vc1, p1, F.O); PINA(F.O); SCHED();
  };
  int n = t1 - t0;
#pragma unroll 1
  for (; n >= 2; n -= 2) {
    half(sa, sb, va0, va1, vb0, vb1, kq + 2 * 4096);
    kq += 2 * 4096;
    half(sb, sa, vb0, vb1, va0, va1, kq + 4096);
    if (hk) { hook(); hk = false; }
  }
  if (n) F.O = pv(sa, va0, va1, F.O);
  if (hk) hook();
}
DI void make_mask(f32x16& mk, int lane, bool old) {
  const int d = (lane & 31) - 4 * (lane >> 5);
#pragma unroll
  for (int i = 0; i < 16; ++i) { const bool le = ((i & 3) + 8 * (i >> 2)) <= d; mk[i] = (le != old) ? 0.f : NEG_BIG; }
}
template <int HD> DI v4u attn_out(const f32x16& O) {
  const float r = __builtin_amdgcn_rcpf(O[HD ? 0 : 8]);
  const int o = HD ? 8 : 0;
  return v4u{pk(O[o] * r, O[o + 1] * r), pk(O[o + 2] * r, O[o + 3] * r), pk(O[o + 4] * r, O[o + 5] * r), pk(O[o + 6] * r, O[o + 7] * r)};
}
template <int HD, class Diag, class Mid, class Hook>
DI v4u attn_item(lptr L, v4u Q, int lane, int tq, int g, Diag diag, Mid mid, bool hk, Hook hook) {
  const int l16 = lane * 16;
  const bool vl = ((lane >> 4) & 1) == HD;
  const int vstride = vl ? 4096 : 0;
  lptr kl = L + L_KV + HD * KB + l16, vloc = vl ? L + L_KV + 2 * KB + l16 : L + L_ONES;
  lptr kr = L + BUF_B + HD * KB + l16, vrem = vl ? L + BUF_B + 2 * KB + l16 : L + L_ONES;
  Fast F; f32x16 mk;
  make_mask(mk, lane, false);
  fast_diag(F, Q, kl + tq * 4096, vloc + tq * vstride, mk);
  diag();
  if (tq > 0) fast_run(F, Q, kl, vloc, vstride, 0, tq, F.nm, false, []() {});
  f32x16 c0;
#pragma unroll
  for (int i = 0; i < 16; ++i) c0[i] = opf(g >= 2 ? __builtin_fminf(F.nm[0], NEG_BIG - mk[i] * 2.f) : F.nm[0]);
  mid();
  if (g >= 1) fast_run(F, Q, kr, vrem, vstride, g >= 2 ? tq : 4, 8, c0, hk, hook);
  else if (hk) hook();
  if (__any(!(F.O[HD ? 0 : 8] <= 1.8446744e19f))) {
    Attn A;
    make_mask(mk, opv(lane), false);
    attn_chunk<true>(A, Q, kl, vloc, vstride, 0, tq, tq, mk);
    if (g >= 1) { make_mask(mk, opv(lane), true); attn_chunk<false>(A, Q, kr, vrem, vstride, g >= 2 ? tq + 1 : 4, 8, g >= 2 ? tq : -1, mk); }
    F.O = A.O;
  }
  return attn_out<HD>(F.O);
}

DI void finish_tile(lptr L, const f32x16& keep, v4u (&res)[2], float (&v)[16], int w, int lane) {
  lptr xp = L + L_KV + (w ^ 2) * 4096 + lane * 16;
#pragma unroll
  for (int qd = 0; qd < 4; ++qd) {
    const f32x4 r = *(LAS f32x4*)(xp + qd * 1024);
#pragma unroll
    for (int j = 0; j < 4; ++j) v[4 * qd + j] = keep[4 * qd + j] + r[j];
  }
#pragma unroll
  for (int e = 0; e < 2; ++e)
#pragma unroll
    for (int j = 0; j < 4; ++j) { v[8 * e + 2 * j] += bflo(res[e][j]); v[8 * e + 2 * j + 1] += bfhi(res[e][j]); }
#pragma unroll
  for (int e = 0; e < 2; ++e) res[e] = v4u{pk(v[8 * e], v[8 * e + 1]), pk(v[8 * e + 2], v[8 * e + 3]), pk(v[8 * e + 4], v[8 * e + 5]), pk(v[8 * e + 6], v[8 * e + 7])};
}
DI void send_tile(lptr L, const f32x16& send, int w, int lane) {
  lptr xp = L + L_KV + w * 4096 + lane * 16;
#pragma unroll
  for (int qd = 0; qd < 4; ++qd) *(LAS f32x4*)(xp + qd * 1024) = f32x4{send[4 * qd], send[4 * qd + 1], send[4 * qd + 2], send[4 * qd + 3]};
}

__global__ void __launch_bounds__(NTHR, 2) fused_layers(Params p) {
  extern __shared__ __attribute__((aligned(16))) unsigned char smem[];
  lptr L = (lptr)smem;
  const int tid = threadIdx.x, wave = __builtin_amdgcn_readfirstlane(tid >> 6);
  const int b = blockIdx.x, me = wg_index(b), g = me >> 4, m = me & 15;
  unsigned char* ws = p.ws;
  const rsrc_t rs = __builtin_amdgcn_make_buffer_rsrc(ws, 0, WS_BYTES, 0x00020000);
  const int th = m >> 3, fb = m & 7;
  bool dead = false;
#define LANE_VALS() const int lane = opv(tid) & 63, r32 = lane & 31, hf = lane >> 5, l16 = lane * 16; (void)r32; (void)hf; (void)l16
  if (tid < 12) { if (tid < 8) *(LAS unsigned*)(L + L_ONES + (tid >> 2) * 1024 + (tid & 3) * 4) = 0x3f803f80u; *(LAS unsigned*)(L + L_SY + tid * 4) = 0u; }
  BARL();

#define FILL_DEF() \
  auto fill_wo = [&](int nd) AI { \
    const rsrc_t ro = __builtin_amdgcn_make_buffer_rsrc((void*)p.wo, 0, 512 * 512 * 2, 0x00020000); \
    const int so_ = (wave * 2 + nd) * 16 * KB + fb * 128, lo_ = (wave < 4 ? BUF_D : BUF_A - 32 * KB) + (wave * 8 + nd * 4) * KB, vo_ = (lane >> 2) * KB + (lane & 3) * 16; \
    dma1p<0, 0>(ro, so_, lo_, vo_); dma1p<64, KB>(ro, so_, lo_, vo_); dma1p<256 * KB, 2 * KB>(ro, so_, lo_, vo_); dma1p<256 * KB + 64, 3 * KB>(ro, so_, lo_, vo_); \
  }; \
  auto fill0 = [&]() AI { \
    if (layer == 0) fill_wo(0); \
    else { dma4<0, BUF_C>(rs, WS_WC + (m * 231 + wave * 4) * KB, wave * 4 * KB, l16); if (wave == 7) dma1<32 * KB, BUF_C + 32 * KB>(rs, WS_WC + m * 231 * KB, 0, l16); } \
  }; \
  auto fill1 = [&]() AI { fill_wo(1); }
  if (wave < 4) {
    const int w = wave;
    const int ttr = 4 * g + 2 * th + (w & 1), kh = w >> 1, ftile = 2 * fb + kh;
    const int tta = 4 * g + w;
    v4u res[2];
    unsigned epoch;
    STAMP(0);
#pragma unroll 1
    for (int layer = 0; layer < 2; ++layer) {
      const int act = WS_ACT + layer * A_LAYER;
      const int fl = layer * F_PER_LAYER;
      const int sb = 2 + layer * 28;
      (void)sb;
      v4u xf[32];
      v4u Qa;
      {
        LANE_VALS();
        STAMP(sb + 0);
        f32x16 aK = {}, aV = {}, aQ = {};
        float mean, rstd;
        const int tro = tr_off(lane);
        constexpr int PQ = 4;
        v4u wq[PQ];
        lptr pc = L + opv(BUF_C + tro);
        auto kv_pass = [&](auto FIRST_) AI {
          constexpr bool FIRST = decltype(FIRST_)::v;
          constexpr int WIN = KVWIN, PD = 3;
          v4u st[4];
          const int sto = ops(act + A_ST0 + tta * 4096), xo = ops(FIRST ? tta * 32 * KB : act + A_XT + tta * 32 * KB);
          lptr tw = L + opv(L_KV + w * 4096 + (lane >> 3) * 128 + (((lane & 7) ^ (lane >> 3)) * 16)), tr = L + opv(L_KV + w * 4096 + r32 * 128);
          const int sw = (r32 & 7) ^ hf;
          auto xpose = [&](int j) AI {
#pragma unroll
            for (int i = 0; i < 4; ++i) ldsw(tw + i * KB, xf[4 * j + i]);
#pragma unroll
            for (int e = 0; e < 4; ++e) xf[4 * j + e] = ldsr(tr + ((2 * e) ^ sw) * 16);
          };
          const rsrc_t rx = __builtin_amdgcn_make_buffer_rsrc((void*)p.x, 0, 2048 * 1024, 0x00020000);
          const int vx = (lane >> 3) * KB + (lane & 7) * 16;
          auto xraw = [&](int i) AI { return __builtin_amdgcn_raw_buffer_load_b128(rx, vx + (i >> 2) * 128, xo + (i & 3) * 8 * KB, 0); };
          if constexpr (FIRST) {
            const unsigned epoch_v = __hip_atomic_load((gu32*)(ws + WS_EPOCH), RLX_AGENT);
#pragma unroll
            for (int i = 0; i < XW0; ++i) xf[i] = xraw(i);
            SCHED();
            epoch = __builtin_amdgcn_readfirstlane(epoch_v) + 1u;
            if (w == 0) lds_set(L, SY_EP, epoch);
            xpose(0);
#pragma unroll
            for (int i = XW0; i < XW1; ++i) xf[i] = xraw(i);
            asm volatile("s_waitcnt lgkmcnt(0)" ::: "memory");
          } else {
#pragma unroll
            for (int i = 0; i < 4; ++i) xf[i] = gldf(rs, l16, xo, i);
#pragma unroll
            for (int i = 0; i < 4; ++i) st[i] = gld(rs, hf * 2048 + r32 * 16 + i * 512, sto);
#pragma unroll
            for (int i = 4; i < WIN; ++i) xf[i] = gldf(rs, l16, xo, i);
          }
          SCHED();
          BAR();
          v4u wk[PD], wv[PD];
          lptr pa = L + opv(BUF_A + tro), pb = L + opv(BUF_B + tro);
#pragma unroll
          for (int i = 0; i < PD; ++i) { wk[i] = trr(pa + 2 * i * KB); wv[i] = trr(pa + (2 * i + 1) * KB); }
          float s0 = 0.f, s1 = 0.f, q0 = 0.f, q1 = 0.f;
          sfor<32>([&](auto S_) AI {
            constexpr int s = decltype(S_)::v, n = s + PD;
            const v4u ck = wk[s % PD], cv = wv[s % PD];
            if constexpr (n == 16) BAR();
            aK = mfma(ck, xf[s], aK); PINA(aK); SCHED();
            if constexpr (n < 16) wk[s % PD] = trr(pa + 2 * n * KB); else if constexpr (n < 32) wk[s % PD] = trr(pb + 2 * (n - 16) * KB);
            SCHED();
            if constexpr (FIRST) {
              if constexpr (s + XW1 < 32) xf[s + XW1] = xraw(s + XW1);
              if constexpr (s + XW1 == 32) { const GAS v4u* xs = (const GAS v4u*)(p.x + (size_t)(ttr * 32 + r32) * 512 + ftile * 32 + hf * 8); res[0] = xs[0]; res[1] = xs[2]; }
            } else if constexpr (s + WIN < 32) xf[s + WIN] = gldf(rs, l16, xo, s + WIN);
            HOLD(ck); SCHED();
            aV = mfma(xf[s], cv, aV); PINA(aV); SCHED();
            if constexpr (n < 16) wv[s % PD] = trr(pa + (2 * n + 1) * KB); else if constexpr (n < 32) wv[s % PD] = trr(pb + (2 * (n - 16) + 1) * KB);
            if constexpr (FIRST && (s & 3) == 0 && s < 28) xpose(s / 4 + 1);
            HOLD(cv); SCHED();
            if constexpr (WO_EARLY && !FIRST && s == 16) lds_add(L, SY_X0, lane);
            if constexpr (s >= 16) { aQ = mfma(wq[(s - 16) % PQ], xf[s - 16], aQ); PINA(aQ); SCHED(); }
            if constexpr (n >= 16) { wq[(n - 16) % PQ] = trr(pc + (n - 16) * KB); if constexpr (s >= 16) HOLD(wq[(s - 16) % PQ]); SCHED(); }
            if constexpr (FIRST) {
              const unsigned e0 = xf[s].x, e1 = xf[s].y, e2 = xf[s].z, e3 = xf[s].w;
              s0 = dot2(e0, 0x3f803f80u, s0); q0 = dot2(e0, e0, q0); s1 = dot2(e1, 0x3f803f80u, s1); q1 = dot2(e1, e1, q1);
              s0 = dot2(e2, 0x3f803f80u, s0); q0 = dot2(e2, e2, q0); s1 = dot2(e3, 0x3f803f80u, s1); q1 = dot2(e3, e3, q1);
            } else if constexpr (s == 6) finish_stats(st, mean, rstd);
            SCHED();
          });
          if constexpr (FIRST) finish_stats2(s0 + s1, q0 + q1, mean, rstd);
        };
        if (layer == 0) kv_pass(IC<1>{}); else kv_pass(IC<0>{});
        if (lane < 32) *(LAS float*)(L + L_RS + w * 128 + lane * 4) = rstd;
        const v4u xe = mean_frag(mean);
        STAMP(sb + 1);
        BAR();
        const v4u wxk = ldsr(L + L_X + l16), wxv = ldsr(L + L_X + KB + l16);
        f32x4 rsv[4];
#pragma unroll
        for (int qd = 0; qd < 4; ++qd) rsv[qd] = *(LAS f32x4*)(L + L_RS + w * 128 + qd * 32 + hf * 16);
        lptr kvl = L + opv(L_KV + w * 4096 + l16);
        const int kvo = ops(act + A_KV + (me * 4 + w) * 4096);
        v4u kvf;
        SCHED();
        sfor<17>([&](auto S_) AI {
          constexpr int s = 16 + decltype(S_)::v, n = s + PQ - 1;
          aQ = mfma(wq[s % PQ], s < 32 ? xf[s < 32 ? s : 0] : xe, aQ);
          PINA(aQ); SCHED();
          if constexpr (n < 32) wq[n % PQ] = trr(pc + n * KB);
          else if constexpr (n == 32) wq[n % PQ] = ldsr(L + L_X + 2 * KB + l16);
          HOLD(wq[s % PQ]); SCHED();
          if constexpr (s == 16) { aK = mfma(wxk, xe, aK); aV = mfma(xe, wxv, aV); PINA(aK); PINA(aV); }
          if constexpr (s >= 18 && s < 26) {
            constexpr int e = s - 18, f = e >> 1, o = (f & 1) * 8 + (e & 1) * 4;
            if constexpr (f < 2) { kvf[(e & 1) * 2] = pk(fm(aK[o], rstd), fm(aK[o + 1], rstd)); kvf[(e & 1) * 2 + 1] = pk(fm(aK[o + 2], rstd), fm(aK[o + 3], rstd)); }
            else { const f32x4 r4 = rsv[o >> 2]; kvf[(e & 1) * 2] = pk(fm(aV[o], r4[0]), fm(aV[o + 1], r4[1])); kvf[(e & 1) * 2 + 1] = pk(fm(aV[o + 2], r4[2]), fm(aV[o + 3], r4[3])); }
            if constexpr (e & 1) { gst(rs, kvf, l16 + f * KB, kvo); ldsw(kvl + f * KB, kvf); }
          }
          SCHED();
        });
        const float qs = rstd * (0.25f * LOG2E);
        ldsw(L + L_QB + w * KB + l16, v4u{pk(fm(aQ[8], qs), fm(aQ[9], qs)), pk(fm(aQ[10], qs), fm(aQ[11], qs)), pk(fm(aQ[12], qs), fm(aQ[13], qs)), pk(fm(aQ[14], qs), fm(aQ[15], qs))});
        Qa = v4u{pk(fm(aQ[0], qs), fm(aQ[1], qs)), pk(fm(aQ[2], qs), fm(aQ[3], qs)), pk(fm(aQ[4], qs), fm(aQ[5], qs)), pk(fm(aQ[6], qs), fm(aQ[7], qs))};
        STAMP(sb + 2);
        BARL();
        STAMP(sb + 3);
      }
      {
        LANE_VALS();
        FILL_DEF();
        auto kvpub = [&]() AI { WAITV(0); if (lds_last(L, SY_KV, lane)) set_flag(ws, fl + F_KV, me, epoch); };
        kvpub();
        if (w == 0 && g >= 1) {
          __builtin_amdgcn_s_sleep(PD_KV); poll(flag_ptr(ws, fl + F_KV, me - 16), lane == 0, epoch, dead);
          const int src = act + A_KV + (me - 16) * 16 * KB;
          dma4<0, BUF_B + 16 * KB>(rs, src, 0, l16); dma4<4 * KB, BUF_B + 20 * KB>(rs, src, 0, l16);
          dma4<8 * KB, BUF_B + 24 * KB>(rs, src, 0, l16); dma4<12 * KB, BUF_B + 28 * KB>(rs, src, 0, l16);
        }
        STAMP(sb + 19);
        const v4u of = attn_item<0>(L, Qa, lane, w, g, []() {}, [&]() AI {
          STAMP(sb + 4);
          WAITV(0);
          BAR();
          STAMP(sb + 5);
          fill0();
        }, layer == 0, fill1);
        gst(rs, of, l16, act + A_OT + (tta * 32 + 2 * m) * KB);
        STAMP(sb + 6);
        WAITV(0);
        BAR();
        if (tid == 0) set_flag(ws, fl + F_O, me, epoch);
        STAMP(sb + 7);
        if (w == 0) { __builtin_amdgcn_s_sleep(PD_O); poll(flag_ptr(ws, fl + F_O, g * 16 + (lane & 15)), lane < 16, epoch, dead); lds_set(L, SY_GO, 3 * layer + 1); }
        else lds_wait(L, SY_GO, 3 * layer + 1);
        STAMP(sb + 8);
      }
      {
        LANE_VALS();
        const int oo = ops(act + A_OT + (ttr * 32 + 16 * kh) * KB);
#pragma unroll
        for (int i = 0; i < 16; ++i) xf[i] = gldf(rs, l16, oo, i);
        SCHED();
        f32x16 keep = {}, send = {};
        const int tro = tr_off(lane);
        lptr k0 = L + opv(BUF_D + 3 * kh * KB + tro), s0 = L + opv(BUF_D + (1 + kh) * KB + tro);
        lptr k1 = L + opv(BUF_A + 3 * kh * KB + tro), s1 = L + opv(BUF_A + (1 + kh) * KB + tro);
        constexpr int PD = 3;
        v4u wk[PD], wsd[PD];
#pragma unroll
        for (int i = 0; i < PD; ++i) { wk[i] = trr(k0 + i * 4 * KB); wsd[i] = trr(s0 + i * 4 * KB); }
        sfor<16>([&](auto S_) AI {
          constexpr int s = decltype(S_)::v, n = s + PD;
          const v4u cs = wsd[s % PD], ck = wk[s % PD];
          send = mfma(cs, xf[s], send); PINA(send); SCHED();
          if constexpr (n < 8) wsd[s % PD] = trr(s0 + n * 4 * KB); else if constexpr (n < 16) wsd[s % PD] = trr(s1 + (n - 8) * 4 * KB);
          HOLD(cs); SCHED();
          keep = mfma(ck, xf[s], keep); PINA(keep); SCHED();
          if constexpr (n < 8) wk[s % PD] = trr(k0 + n * 4 * KB); else if constexpr (n < 16) wk[s % PD] = trr(k1 + (n - 8) * 4 * KB);
          HOLD(ck); HOLD(xf[s]); SCHED();
        });
        send_tile(L, send, w, lane);
        lds_set(L, SY_EX + 4 * w, 2 * layer + 1);
        STAMP(sb + 9);
        lds_wait(L, SY_EX + 4 * (w ^ 2), 2 * layer + 1);
        float v[16];
        finish_tile(L, keep, res, v, w, lane);
        const int so = act + A_X1T + (ttr * 32 + 2 * ftile) * KB;
        gst(rs, res[0], l16, so); gst(rs, res[1], l16 + KB, so);
        store_stats(rs, v, lane, act + A_ST1 + ttr * 4096 + (ftile >> 1) * 512 + (ftile & 1) * 8);
        STAMP(sb + 10);
        WAITV(0);
        if (lds_last(L, SY_DR, lane)) set_flag(ws, fl + F_X1, me, epoch);
        if (w == 0) {
          STAMP(sb + 11);
          __builtin_amdgcn_s_sleep(PD_X1); poll(flag_ptr(ws, fl + F_X1, g * 16 + (lane & 15)), lane < 16, epoch, dead);
          lds_set(L, SY_GO, 3 * layer + 2);
        } else lds_wait(L, SY_GO, 3 * layer + 2);
        STAMP(sb + 12);
      }
      {
        LANE_VALS();
        constexpr int WIN = 12;
        v4u st[4];
        const int sto = ops(act + A_ST1 + tta * 4096), xo = ops(act + A_X1T + tta * 32 * KB);
#pragma unroll
        for (int i = 0; i < 4; ++i) xf[i] = gldf(rs, l16, xo, i);
#pragma unroll
        for (int i = 0; i < 4; ++i) st[i] = gld(rs, hf * 2048 + r32 * 16 + i * 512, sto);
#pragma unroll
        for (int i = 4; i < WIN; ++i) xf[i] = gldf(rs, l16, xo, i);
        SCHED();
        lds_wait(L, SY_HWL, 4 * layer + 4);
        f32x16 acc[3];
        acc[0] = f32x16{}; acc[1] = f32x16{}; acc[2] = f32x16{};
        float mean, rstd, c1, sd; v4u xe;
        lptr pbuf[4] = {L + BUF_A + l16, L + opv(BUF_B + l16), L + opv(BUF_C + l16), L + opv(BUF_D + l16)};
        constexpr int PQ = 4;
        v4u wq[PQ];
        {
          constexpr int PD = 3;
          v4u w0[PD], w1[PD];
          lptr p0 = pbuf[2], p1 = pbuf[1];
#pragma unroll
          for (int i = 0; i < PD; ++i) { w0[i] = ldsr(p0 + i * KB); w1[i] = ldsr(p1 + i * KB); }
          sfor<33>([&](auto S_) AI {
            constexpr int s = decltype(S_)::v, n = s + PD;
            if constexpr (n == 17) BAR();
            const v4u xa = s < 32 ? xf[s < 32 ? s : 0] : xe;
            const v4u c0 = w0[s % PD], cc1 = w1[s % PD];
            acc[0] = mfma(c0, xa, acc[0]); PINA(acc[0]); SCHED();
            if constexpr (n < 33) w0[s % PD] = ldsr(p0 + n * KB);
            SCHED();
            if constexpr (s + WIN < 32) xf[s + WIN] = gldf(rs, l16, xo, s + WIN);
            HOLD(c0); SCHED();
            acc[1] = mfma(cc1, xa, acc[1]); PINA(acc[1]); SCHED();
            if constexpr (n < 33) w1[s % PD] = ldsr(p1 + n * KB);
            HOLD(cc1); SCHED();
            if constexpr (s >= 17) { acc[2] = mfma(wq[(s - 17) % PQ], xf[s - 17], acc[2]); PINA(acc[2]); SCHED(); }
            if constexpr (n >= 17) { wq[(n - 17) % PQ] = ldsr(pbuf[3] + (n - 17) * KB); if constexpr (s >= 17) HOLD(wq[(s - 17) % PQ]); SCHED(); }
            if constexpr (s == 6) { finish_stats(st, mean, rstd); xe = mean_frag(mean); c1 = -LOG2E * rstd; sd = __builtin_amdgcn_rcpf(rstd); }
            SCHED();
          });
        }
        STAMP(sb + 13);
        const int ho = ops(act + A_HT + (tta * 112 + 7 * m) * KB);
        float ev[8]; v4u hw4;
        auto epi = [&](const f32x16& a, int k, int tile) AI {
          const int e = k >> 1;
          if (!(k & 1)) ev[e] = __builtin_fmaf(__builtin_amdgcn_exp2f(a[e] * c1), sd, sd);
          else {
            ev[e] = fm(a[8 + e], __builtin_amdgcn_rcpf(ev[e]));
            if (e & 1) hw4[e >> 1] = pk(ev[e - 1], ev[e]);
            if (e == 7) gst(rs, hw4, l16 + (tile & 3) * KB, ho + (tile >> 2) * 4 * KB);
          }
        };
        constexpr int DWIN = 16;
        v4u hfr[32];
        const int hb = ops(act + A_HT + (ttr * 112 + 56 * kh) * KB), fbo = ops(fb);
        auto hld = [&](int s) AI {
          const int t = s >> 3, jj = s & 7;
          return gld(rs, l16 + (t & 3) * KB, hb + (((jj + fbo) & 7) * 7 + (t & 4)) * KB);
        };
        auto hpub = [&](unsigned nt) AI { if (lds_last(L, SY_H, lane)) set_flag(ws, fl + F_H + nt - 2, me, epoch * 16u + layer * 8u + nt); };
        sfor<149>([&](auto U_) AI {
          constexpr int u = decltype(U_)::v + 16, t = 2 + u / 33, s = u % 33, n = s + PQ - 1;
          constexpr int bufs[8] = {2, 1, 3, 0, 2, 1, 3, 0};
          const v4u cq = wq[u % PQ];
          if constexpr (u == 17) { BAR(); STAMP(sb + 27); }
          if constexpr (n == 33 && t < 6) { if constexpr (PUBMODE == 0 && t >= 4) WAITV(1); BAR(); }
          if constexpr (PUBMODE == 1 && t == 4 && s == HPUBS) { WAITV(1); hpub(2); }
          if constexpr (PUBMODE == 1 && t >= 5 && s == HPUBS) { WAITV(0); hpub(t - 1); }
          if constexpr (EARLY_FREE && t >= 3 && t <= 5 && s == 1) BAR();
          if constexpr (t == 6 && s == 17) { if constexpr (PUBMODE == 0) WAITV(1); BAR(); }
          if constexpr (s == 0) acc[t % 3] = mfma(cq, xf[0], f32x16{});
          else acc[t % 3] = mfma(cq, s < 32 ? xf[s < 32 ? s : 0] : xe, acc[t % 3]);
          PINA(acc[t % 3]); SCHED();
          if constexpr (n < 33) wq[(u + PQ - 1) % PQ] = ldsr(pbuf[bufs[t]] + n * KB);
          else if constexpr (t < 6) wq[(u + PQ - 1) % PQ] = ldsr(pbuf[bufs[t + 1]] + (n - 33) * KB);
          SCHED();
          if constexpr (t == 2) { if constexpr (s >= 17) epi(acc[0], s - 17, 0); }
          else if constexpr (t == 3) { if constexpr (s < 16) epi(acc[1], s, 1); if constexpr (s >= 17) epi(acc[2], s - 17, 2); }
          else if constexpr (s >= 1 && s < 17) epi(acc[(t - 1) % 3], s - 1, t - 1);
          if constexpr (t == 6 && s >= 17 && (s - 17) % (16 / HPRE) == 0) hfr[(s - 17) / (16 / HPRE)] = hld((s - 17) / (16 / HPRE));
          if constexpr (s == 32 && t < 6) STAMP(sb + 21 + t);
          HOLD(cq); if constexpr (t == 6 && s < 32) HOLD(xf[s < 32 ? s : 0]);
          SCHED();
        });
        STAMP(sb + 14);
        BAR();
        STAMP(sb + 15);
        f32x16 keep = {}, send = {};
        constexpr int PD = PDD;
        static constexpr int dbufs[7] = {0, 2, 1, 3, 0, 2, 1};
        const int ko = 3 * kh * KB, sdo = (1 - 2 * kh) * KB;
        lptr kb[4] = {pbuf[0] + ko, pbuf[1] + ko, pbuf[2] + ko, pbuf[3] + ko};
        lptr sp[4] = {kb[0] + sdo, kb[1] + sdo, kb[2] + sdo, kb[3] + sdo};
        v4u wk[PD], wsd[PD];
#pragma unroll
        for (int i = 0; i < PD; ++i) { wk[i] = ldsr(kb[dbufs[0]] + i * 4 * KB); wsd[i] = ldsr(sp[dbufs[0]] + i * 4 * KB); }
        constexpr auto nld = [](int s) { int c = 0; for (int i = 0; i < s; ++i) c += (i + HPRE < 24 && i < 13) + (i >= 13 && i < 45) + (HPRE == 8 && i < 6 && (i & 1)); return c; };
        sfor<56>([&](auto S_) AI {
          constexpr int s = decltype(S_)::v, n = s + PD;
          constexpr int ld = s + HPRE < 24 && s < 13 ? s + HPRE : -1, ld2 = (s >= 13 && s < 45) ? s + 11 : -1, ld3 = (HPRE == 8 && s < 6 && (s & 1)) ? 21 + s / 2 : -1;
          const v4u ck = wk[s % PD], cs = wsd[s % PD];
          if constexpr (n < 56 && (n & 7) == 0) {
            if constexpr (n == 8 && H6S >= 8 - PD) { if constexpr (HPRE == 16) WAITV(16); else WAITV(8); }
            if constexpr (n == 24 && H7S >= 24 - PD) asm volatile("s_waitcnt vmcnt(%0)" :: "n"(nld(24 - PD) - nld(16 / E6)) : "memory");
            BAR();
            if constexpr (n >= 24 && n <= 40) STAMP(sb + 17 + n / 8);
            if constexpr (n == 8 || n == 16) STAMP(58 + 2 * layer + n / 16);
          }
          send = mfma(cs, hfr[s % 32], send); PINA(send); SCHED();
          if constexpr (n < 56) wsd[s % PD] = ldsr(sp[dbufs[n >> 3]] + (n & 7) * 4 * KB);
          SCHED();
          if constexpr (ld >= 0) hfr[ld % 32] = hld(ld);
          if constexpr (ld2 >= 0) hfr[ld2 % 32] = hld(ld2);
          if constexpr (ld3 >= 0) hfr[ld3 % 32] = hld(ld3);
          if constexpr (s < 16 / E6) { sfor<E6 / 2>([&](auto K_) AI { epi(acc[6 % 3], E6 * s + decltype(K_)::v, 6); }); }
          HOLD(cs); SCHED();
          keep = mfma(ck, hfr[s % 32], keep); PINA(keep); SCHED();
          if constexpr (n < 56) wk[s % PD] = ldsr(kb[dbufs[n >> 3]] + (n & 7) * 4 * KB);
          HOLD(ck); HOLD(hfr[s % 32]); SCHED();
          if constexpr (s < 16 / E6) { sfor<E6 / 2>([&](auto K_) AI { epi(acc[6 % 3], E6 * s + E6 / 2 + decltype(K_)::v, 6); }); }
          if constexpr (H6S < 8 - PD && s == H6S) { if constexpr (HPRE == 16) WAITV(16); else WAITV(8); hpub(6); }
          if constexpr (H7S < 24 - PD && s == H7S) { asm volatile("s_waitcnt vmcnt(%0)" :: "n"(nld(H7S + 1) - nld(16 / E6)) : "memory"); hpub(7); }
          SCHED();
        });
        send_tile(L, send, w, lane);
        lds_set(L, SY_EX + 4 * w, 2 * layer + 2);
        STAMP(sb + 16);
        lds_wait(L, SY_EX + 4 * (w ^ 2), 2 * layer + 2);
        float v[16];
        finish_tile(L, keep, res, v, w, lane);
        if (layer == 0) {
          const int nact = act + A_LAYER;
          const int so = nact + A_XT + (ttr * 32 + 2 * ftile) * KB;
          gst(rs, res[0], l16, so); gst(rs, res[1], l16 + KB, so);
          store_stats(rs, v, lane, nact + A_ST0 + ttr * 4096 + (ftile >> 1) * 512 + (ftile & 1) * 8);
          STAMP(sb + 17);
          WAITV(0);
          if (lds_last(L, SY_DR, lane)) set_flag(ws, fl + F_X2, me, epoch);
          if (w == 0) {
            STAMP(sb + 18);
            __builtin_amdgcn_s_sleep(PD_X2); poll(flag_ptr(ws, fl + F_X2, g * 16 + (lane & 15)), lane < 16, epoch, dead);
            lds_set(L, SY_GO, 3);
          } else lds_wait(L, SY_GO, 3);
        } else {
          GAS v4u* od = (GAS v4u*)(p.out + (size_t)(ttr * 32 + r32) * 512 + ftile * 32 + hf * 8);
          od[0] = res[0]; od[2] = res[1];
          STAMP(sb + 17);
        }
      }
    }
  } else {
    const int hw = wave - 4;
    const int lb = hw * 8 * KB;
    const int wc = WS_WC + m * 231 * KB + lb, wdn = WS_WD + fb * 224 * KB + lb;
    const rsrc_t rw = __builtin_amdgcn_make_buffer_rsrc((void*)p.wqkv, 0, 512 * 1536 * 2, 0x00020000);
    constexpr int KS = 16 * 1536 * 2;
    const int l16h = (tid & 63) * 16, vraw = ((tid & 63) >> 2) * 3072 + (tid & 3) * 16;
    const int wkv = hw * 4 * KS + 64 * m, wqq = hw * 8 * KS + 64 * m;
#define DMA(EXTRA, base, G, BUF) dma_chunk<EXTRA, G, BUF>(rs, base, lb, l16h)
#define DMA_Q() sfor<8>([&](auto I_) AI { constexpr int i = decltype(I_)::v; dma1p<i * KS, BUF_C + i * KB>(rw, wqq, lb, vraw); })
#define STAGE(arr, base, G) do { const int o_ = ops(base) + (G); _Pragma("unroll") for (int i_ = 0; i_ < 8; ++i_) arr[i_] = gldf(rs, l16, o_, i_); } while (0)
#define ZERO(arr) do { _Pragma("unroll") for (int i_ = 0; i_ < 8; ++i_) arr[i_] = v4u{0u, 0u, 0u, 0u}; } while (0)
#define PIN(arr) asm volatile("" : "+v"(arr[0]), "+v"(arr[1]), "+v"(arr[2]), "+v"(arr[3]), "+v"(arr[4]), "+v"(arr[5]), "+v"(arr[6]), "+v"(arr[7]))
#define PUT(arr, BUF) do { if (DBG_NOPUT) { PIN(arr); break; } lptr d_ = L + opv((BUF) + lb + l16); _Pragma("unroll") for (int i_ = 0; i_ < 8; ++i_) ldsw(d_ + i_ * KB, arr[i_]); } while (0)
#define GET(arr, BUF) do { lptr d_ = L + opv((BUF) + lb + l16); _Pragma("unroll") for (int i_ = 0; i_ < 8; ++i_) arr[i_] = ldsr(d_ + i_ * KB); } while (0)
    const int vpd = ((MAPV == 0 ? (g & 1) * 2 + th : g & 3) * 64 + (tid & 63)) * 128, vpc = ((MAPV == 0 ? (g & 1) * 2 : g & 3) * 64 + (tid & 63)) * 128;
#define PFC(c) do { if (hw == 3) touch<(c) * CH>(rs, wc - lb, vpc); if (MAPV == 0 && hw == 2) touch<(c) * CH + 8 * KB>(rs, wc - lb, vpc); } while (0)
#define PFD(c) do { if (hw == 3) touch<(c) * 32 * KB>(rs, wdn - lb, vpd); } while (0)
#define HFLAG(nt) do { if (tid == 320) set_flag(ws, fl + F_H + (nt) - 2, me, epoch * 16u + layer * 8u + (nt)); } while (0)
#define BARL8() asm volatile("s_waitcnt lgkmcnt(8)\n\ts_barrier" ::: "memory")
#define DBG_NOPUT 0
#define HISSUE(nt) do { if (hw == 0) hpoll_issue<nt>(rs, hco, lane); } while (0)
#define HGATE(nt, younger) do { if (hw == 0) { WAITV(younger); hgate<nt>(rs, L, hco, lane, epoch * 16u + layer * 8u, dead); } } while (0)
    v4u a0[8], a1[8];
    v4u uw[8];
    {
      LANE_VALS();
      sfor<16>([&](auto I_) AI { constexpr int i = decltype(I_)::v; dma1p<((i >> 3) * 16 + ((i & 7) >> 1)) * KS + ((i & 1) ? 2048 : 1024), (i >> 3) * CH + (i & 7) * KB>(rw, wkv, lb, vraw); });
      DMA_Q();
      STAMP(0);
      WAITV(16); BAR();
      STAMP(1);
      __builtin_amdgcn_s_setprio(HPRIO);
      const int tro = tr_off(lane);
      float c[6] = {0.f, 0.f, 0.f, 0.f, 0.f, 0.f};
      {
        lptr ta = L + opv(BUF_A + lb + tro);
#pragma unroll
        for (int i = 0; i < 4; ++i) { c[0] = sum8(trr(ta + 2 * i * KB), c[0]); c[1] = sum8(trr(ta + (2 * i + 1) * KB), c[1]); }
      }
      GET(a0, BUF_A); PIN(a0);
      WAITV(0); BAR();
      STAMP(2);
      {
        lptr tb = L + opv(BUF_B + lb + tro), tc = L + opv(BUF_C + lb + tro);
#pragma unroll
        for (int i = 0; i < 4; ++i) {
          c[3] = sum8(trr(tb + 2 * i * KB), c[3]); c[4] = sum8(trr(tb + (2 * i + 1) * KB), c[4]);
          c[2] = sum8(trr(tc + 2 * i * KB), c[2]); c[5] = sum8(trr(tc + (2 * i + 1) * KB), c[5]);
        }
        const unsigned hk = hilo(xhalf_sum(c[0] + c[3])), hv = hilo(xhalf_sum(c[1] + c[4])), hq = hilo(xhalf_sum(c[2] + c[5]));
        if (lane < 32) {
          lptr xp = L + L_X + (hw >> 1) * 512 + (hw & 1) * 8 + l16;
          *(LAS v2u*)xp = v2u{hk, hk}; *(LAS v2u*)(xp + KB) = v2u{hv, hv}; *(LAS v2u*)(xp + 2 * KB) = v2u{hq, hq};
        }
      }
      STAMP(5);
      {
        const int row = lane >> 3, pc = lane & 7, r = b < 168 ? b - 112 : 0;
        const bool gu = b < 112;
        const u16* s0 = gu ? p.wg + (size_t)((b & 3) * 128 + 32 * hw + row) * 1792 + 64 * (b >> 2) + 8 * pc : p.wd + (size_t)((r % 7) * 256 + 64 * hw + row) * 512 + 64 * (r / 7) + 8 * pc;
        const u16* s1 = gu ? s0 + (p.wu - p.wg) : s0 + 32 * 512;
        const int st8 = gu ? 8 * 1792 : 8 * 512;
#pragma unroll
        for (int i = 0; i < 8; ++i) uw[i] = *(const GAS v4u*)((i < 4 ? s0 : s1) + (i & 3) * st8);
        SCHED();
      }
      GET(a1, BUF_B); PIN(a1);
      __builtin_amdgcn_s_setprio(0);
      BARL();
      STAMP(3);
    }
    const unsigned epoch = __builtin_amdgcn_readfirstlane(*(volatile LAS unsigned*)(L + L_SY + SY_EP));
#pragma unroll 1
    for (int layer = 0; layer < 2; ++layer) {
      LANE_VALS();
      const int act = WS_ACT + layer * A_LAYER;
      const int fl = layer * F_PER_LAYER;
      const int sb = 2 + layer * 28;
      (void)sb;
      v4u d2[8], d3[8], d4[8], d5[8], d6[8];
      if (layer == 1) {
        BARL();
      if (b == 0 && hw == 2) {
        for (unsigned n = 0; n < (1u << 16); ++n) {
          gu32* f = flag_ptr(ws, F_P2, lane);
          const unsigned v0 = __hip_atomic_load(f, RLX_AGENT), v1 = __hip_atomic_load(f + 16 * FLAG_STRIDE, RLX_AGENT);
          const unsigned v2 = __hip_atomic_load(f + 32 * FLAG_STRIDE, RLX_AGENT), v3 = __hip_atomic_load(f + 48 * FLAG_STRIDE, RLX_AGENT);
          if (__all(v0 == epoch && v1 == epoch && v2 == epoch && v3 == epoch)) break;
        }
        if (lane == 0) __hip_atomic_store((gu32*)(ws + WS_EPOCH), epoch, RLX_AGENT);
      }
        STAMP(sb + 0);
        WAITV(0);

        BAR();
        if (WO_EARLY) {
          const rsrc_t ro = __builtin_amdgcn_make_buffer_rsrc((void*)p.wo, 0, 512 * 512 * 2, 0x00020000);
          const int so_ = hw * 2 * 16 * KB + fb * 128, vo_ = (lane >> 2) * KB + (lane & 3) * 16;
          lds_wait(L, SY_X0, 4);
          __builtin_amdgcn_s_sleep(WO_SLEEP);
          sfor<16>([&](auto I_) AI { constexpr int i = decltype(I_)::v; dma1p<((i >> 3) * 8 + ((i & 7) >> 2)) * 16 * KB + ((i >> 1) & 1) * 256 * KB + (i & 1) * 64, (i < 8 ? BUF_D : BUF_A) + (i & 7) * KB>(ro, so_, lb, vo_); });
        }

        BAR();
        STAMP(sb + 1);
      }

      STAMP(sb + 2);
      BAR();

      {
        const int tq = 3 - hw;
        const v4u Qb = ldsr(L + L_QB + tq * KB + l16);
        FILL_DEF();
        auto unit = [&]() AI {
          if (layer == 0 && b < 168) {
            const int row = lane >> 3, pc = lane & 7, r = b - 112, ufb = r / 7;
            const bool gu = b < 112;
            lptr d0 = L + BUF_D + row * 64 + (gu ? (pc >> 1) * 8 * KB + 2 * hw * KB + (pc & 1) * 16 : ((hw >> 1) * 2 + (pc >> 2)) * 8 * KB + (hw & 1) * 4 * KB + (pc & 3) * 16);
            lptr d1 = d0 + (gu ? 32 : 2 * KB);
            if (UNIT_W == 0) {
#pragma unroll
              for (int i = 0; i < 8; ++i) ldsw((i < 4 ? d0 : d1) + ((i & 3) >> 1) * KB + (i & 1) * 512, uw[i]);
              lds_add(L, SY_U2, lane);
            }
            lds_wait(L, SY_U2, 4);
            const int ku0 = gu ? 8 * (b & 3) : 8 * (2 * (r % 7) + (hw >> 1));
            const int ubase = gu ? WS_WC + (((b >> 2) * 4 + hw) * 33 + ku0) * KB : WS_WD + (ufb * 224 + (hw & 1)) * KB;
            float c0 = 0.f, c1 = 0.f;
            int j = ku0 / 7, t = ku0 % 7;
            lptr tp = L + BUF_D + lb + tr_off(lane);
#pragma unroll
            for (int u = 0; u < 8; ++u) {
              const v4u f = trr(tp + u * KB);
              gst(rs, f, l16, ubase + (gu ? u : (t * 8 + ((j - ufb) & 7)) * 4 + (j >> 3) * 2) * KB);
              if (++t == 7) { t = 0; ++j; }
              if (u & 1) c1 = sum8(f, c1); else c0 = sum8(f, c0);
            }
            if (gu) { const unsigned wd = hilo(xhalf_sum(c0 + c1)); if (lane < 32) gst2(rs, v2u{wd, wd}, l16, ubase + (32 - ku0) * KB + ((b & 3) >> 1) * 512 + (b & 1) * 8); }
          }
        };
        auto unit_w = [&]() AI {
          if (UNIT_W == 1 && layer == 0 && b < 168) {
            const int row = lane >> 3, pc = lane & 7;
            const bool gu = b < 112;
            lptr d0 = L + BUF_D + row * 64 + (gu ? (pc >> 1) * 8 * KB + 2 * hw * KB + (pc & 1) * 16 : ((hw >> 1) * 2 + (pc >> 2)) * 8 * KB + (hw & 1) * 4 * KB + (pc & 3) * 16);
            lptr d1 = d0 + (gu ? 32 : 2 * KB);
#pragma unroll
            for (int i = 0; i < 8; ++i) ldsw((i < 4 ? d0 : d1) + ((i & 3) >> 1) * KB + (i & 1) * 512, uw[i]);
            lds_add(L, SY_U2, lane);
          }
        };
        if (hw == 3 && g >= 2) {
          __builtin_amdgcn_s_sleep(PD_KV + 4); poll(flag_ptr(ws, fl + F_KV, me - 32), lane == 0, epoch, dead);
          const int s2 = act + A_KV + (me - 32) * 16 * KB;
          dma4<0, BUF_B>(rs, s2, 0, l16); dma4<4 * KB, BUF_B + 4 * KB>(rs, s2, 0, l16);
          dma4<8 * KB, BUF_B + 8 * KB>(rs, s2, 0, l16); dma4<12 * KB, BUF_B + 12 * KB>(rs, s2, 0, l16);
        }
        const v4u of = attn_item<1>(L, Qb, lane, tq, g, unit_w, [&]() AI {
          if (hw == 3) WAITV(0);
          unit();
          STAMP(sb + 4);
          BARL();

          fill0();
        }, layer == 0, fill1);
        gst(rs, of, l16, act + A_OT + ((4 * g + tq) * 32 + 2 * m + 1) * KB);
        STAMP(sb + 6);
        WAITV(0);

        BAR();
      }
      if (layer == 0) {
        if (hw == 0) {
          if (lane == 0) set_flag(ws, F_P2, me, epoch);
          int qq;
          if (lane < 7) qq = 112 + 7 * fb + lane;
          else { int n = (7 * m) / 4 + ((lane - 7) >> 2); n = n > 27 ? 27 : n; qq = 4 * n + ((lane - 7) & 3); }
          poll(flag_ptr(ws, F_P2, lane < 19 ? wg_index(qq) : 0), lane < 19, epoch, dead);
          lds_set(L, SY_U, 1);
        } else lds_wait(L, SY_U, 1);
        DMA(true, wc, 0, BUF_C);
      }
      if (hw == 1) ot<OT_O, A_OT>(rs, act + (4 * g * 32 + 2 * m) * KB, (lane >> 4) * 32 * KB + (lane & 15) * 128);
      DMA(true, wc, 1 * CH, BUF_B);
      STAMP(sb + 7);
      lds_wait(L, SY_GO, 3 * layer + 1);
      __builtin_amdgcn_s_sleep(14);
      WAITV(0);
      lds_add(L, SY_HWL, lane);
      STAGE(d6, wdn, 6 * 32 * KB); STAGE(d5, wdn, 5 * 32 * KB);
      STAMP(sb + 9);
      lds_wait(L, SY_DR, 8 * layer + 4);
      const int vx = (lane >> 5) * 32 * KB + (lane & 31) * 128, vs = lane < 8 ? (lane >> 2) * 4096 + (lane & 3) * 128 : WS_BYTES;
      const int xoo = ((4 * g + 2 * th) * 32 + 4 * fb) * KB, soo = (4 * g + 2 * th) * 4096 + fb * 512;
      if (hw == 1) ot<OT_X, A_X1T>(rs, act + xoo, vx);
      if (hw == 2) ot<OT_X, A_ST1>(rs, act + soo, vs);
      DMA(true, wc, 2 * CH, BUF_D); DMA(true, wc, 3 * CH, BUF_A);
      STAMP(sb + 11);
      WAITV(8); BAR();

      BAR();
      STAMP(sb + 13);
      DMA(true, wc, 4 * CH, BUF_C); DMA(true, wc, 5 * CH, BUF_B);
      WAITV(16); BAR();
      STAGE(d4, wdn, 4 * 32 * KB); STAGE(d3, wdn, 3 * 32 * KB);
      const int hoo = act + A_HT + ((4 * g + 2 * (hw - 2)) * 112 + 7 * m) * KB;
      const int vh = (lane & 24) ? WS_BYTES : (lane >> 5) * 112 * KB + (lane & 7) * 128;
      const int hco = WS_FLAGS + ((fl + F_H) * 256 + g * 16) * FLAG_STRIDE;

      WAITV(24); BAR();

      DMA(true, wc, 6 * CH, BUF_D); STAGE(d2, wdn, 2 * 32 * KB);
      WAITV(32); BAR();
      if (PUBMODE == 0) HFLAG(3);
      DMA(false, wdn, 0 * 32 * KB, BUF_A);
      WAITV(16); BAR();
      if (PUBMODE == 0) HFLAG(4);
      HISSUE(L2);
      DMA(false, wdn, 1 * 32 * KB, BUF_C);
      HGATE(L2, 8);
      STAMP(sb + 14);
      BAR();
      WAITV(0); PIN(d6); PIN(d5); PIN(d4); PIN(d3); PIN(d2);
      if (PUBMODE == 0) HFLAG(5);
      if (P4AT == 0) HISSUE(4);
      BAR();
      if (P4AT == 1) HISSUE(4);
      PUT(d2, BUF_B);

      if (P5AT == 0) HISSUE(5);
      BAR();
      if (H6S >= 8 - PDD) HFLAG(6);
      if (P5AT == 1) HISSUE(5);
      PUT(d3, BUF_D);
      HGATE(4, 1);
      STAMP(sb + 15);
      BARL8();
      PUT(d4, BUF_A);

      HGATE(5, 0);
      if (P6AT == 0) HISSUE(6);
      BARL8();
      if (H7S >= 24 - PDD) HFLAG(7);
      if (P6AT == 1) HISSUE(6);
      PUT(d5, BUF_C);
      HGATE(6, 0);
      if (P7AT == 0) HISSUE(7);
      BARL8();
      if (P7AT == 1) HISSUE(7);
      PUT(d6, BUF_B);

      if (P7AT == 2) HISSUE(7);
      HGATE(7, 0);
      STAMP(sb + 16);
      BARL8();
      BARL();
      if (layer == 0) {
        PUT(a0, BUF_A);
        lds_wait4(L, 2);
        PUT(a1, BUF_B);
        DMA_Q();
        lds_wait(L, SY_DR, 8);
        if (hw == 1) ot<OT_X, A_LAYER + A_XT>(rs, act + xoo, vx);
        if (hw == 2) ot<OT_X, A_LAYER + A_ST0>(rs, act + soo, vs);
      }
      ZERO(uw);
    }
  }
}
}

extern "C" void kernel_launch(void* const* d_in, const int* in_sizes, int n_in, void* d_out, int out_size, void* d_ws, size_t ws_size,
                              hipStream_t stream) {
  Params p;
  p.x = (const u16*)d_in[0]; p.wqkv = (const u16*)d_in[1]; p.wo = (const u16*)d_in[2];
  p.wg = (const u16*)d_in[4]; p.wu = (const u16*)d_in[5]; p.wd = (const u16*)d_in[6];
  p.out = (u16*)d_out; p.ws = (unsigned char*)d_ws;
  (void)hipFuncSetAttribute((const void*)fused_layers, hipFuncAttributeMaxDynamicSharedMemorySize, LDS_BYTES);
  hipLaunchKernelGGL(fused_layers, dim3(NWG), dim3(NTHR), LDS_BYTES, stream, p);
}
```

```cpp
#define HPRE 8 _E6=4 _PUBMODE=0 _H7S=15 _H6S=5 _HI_EARLY=0 _WO_SLEEP=12 b3=
#define HI_EARLY 1
#define L2 (PUBMODE == 0 ? 3 : 2)
#define L3 (PUBMODE == 0 ? 3 : 4)
#define UNIT_W 1
#define HPRIO 3
#define UNIT_AT 0
#define POLL_FIRST 0
#define START_ORDER 0
#define QSLEEP 8
#include <hip/hip_runtime.h>
#include <hip/hip_bf16.h>
#include <utility>

#define POLL_SLEEP 0
#define UNIT_EARLY 1
#define EARLY_FREE 0
#define HPUBS 10
#define E6 2
#define H7S 19
#define XW0 8
#define XW1 16
#define H6S 5
#define PUBMODE 1
#define PDD 3
#define PD_O 18
#define PD_X1 18
#define PD_X2 18
#define PD_KV 10
#define HSLEEP 2
#define POLLK 0
#define HPOLLBITS "sc1"
#define HD5 0
#define HD6 0
#define HD7 0
#define WO_EARLY 1
#define WO_SLEEP 6
#define KVWIN 12
#define PROF 0

extern "C" __device__ float llvm_fmul_legacy(float, float) __asm("llvm.amdgcn.fmul.legacy");
namespace {
typedef unsigned short u16;
typedef unsigned v4u __attribute__((ext_vector_type(4)));
typedef unsigned v2u __attribute__((ext_vector_type(2)));
typedef short v4s __attribute__((ext_vector_type(4)));
typedef float f32x4 __attribute__((ext_vector_type(4)));
typedef float f32x16 __attribute__((ext_vector_type(16)));
typedef __bf16 v2bf __attribute__((ext_vector_type(2)));
typedef __bf16 v8bf __attribute__((ext_vector_type(8)));
#define LAS __attribute__((address_space(3)))
#define GAS __attribute__((address_space(1)))
#define DI __device__ __forceinline__
typedef GAS unsigned gu32;
typedef LAS unsigned char* lptr;
#define RLX_AGENT __ATOMIC_RELAXED, __HIP_MEMORY_SCOPE_AGENT

constexpr int NWG = 256, NTHR = 512;
constexpr int KB = 1024;
constexpr int FLAG_STRIDE = 256;
enum { F_P2 = 0, F_KV = 1, F_O = 2, F_X1 = 3, F_X2 = 4, F_H = 5, F_PER_LAYER = 10, F_KINDS = 21 };
constexpr int WS_EPOCH = 0;
constexpr int WS_FLAGS = 4 * KB;
constexpr int WS_WC = WS_FLAGS + F_KINDS * 256 * FLAG_STRIDE;
constexpr int WS_WD = WS_WC + 16 * 231 * KB;
constexpr int WS_ACT = WS_WD + 8 * 224 * KB;
constexpr int A_XT = 0, A_ST0 = A_XT + 2048 * KB, A_KV = A_ST0 + 256 * KB, A_OT = A_KV + 4096 * KB, A_X1T = A_OT + 2048 * KB,
              A_ST1 = A_X1T + 2048 * KB, A_HT = A_ST1 + 256 * KB, A_LAYER = A_HT + 64 * 112 * KB;
constexpr int WS_DBG = WS_ACT + 2 * A_LAYER;
constexpr int WS_BYTES = WS_DBG + 256 * KB * 2;
constexpr int CH = 33 * KB;
constexpr int BUF_A = 0, BUF_B = CH, BUF_C = 2 * CH, BUF_D = 3 * CH, SP = 4 * CH;
constexpr int L_KV = SP;
constexpr int L_QB = SP + 16 * KB;
constexpr int L_ONES = SP + 20 * KB;
constexpr int L_SY = L_ONES + 64;
constexpr int SY_GO = 0, SY_DR = 4, SY_HWL = 8, SY_KV = 12, SY_EX = 16, SY_U = 32, SY_X0 = 36, SY_U2 = 40, SY_H = 44, SY_EP = 48;
constexpr int L_RS = L_ONES + 128;
constexpr int L_POLL = L_ONES + 640;
constexpr int L_PF = L_ONES + 1088;
constexpr int L_X = SP + 22 * KB;
constexpr int L_DBG = SP + 25 * KB;
constexpr int LDS_BYTES = SP + (PROF == 1 ? 27 : 25) * KB;

constexpr float NEG_BIG = -1e30f;
constexpr float LOG2E = 1.4426950408889634f;
constexpr int AUX_SC1 = 16;

struct Params {
  const u16 *x, *wqkv, *wo, *wg, *wu, *wd;
  u16* out;
  unsigned char* ws;
};

template <int I> struct IC { static constexpr int v = I; };
template <class F, int... Is> DI void sfor_impl(F& f, std::integer_sequence<int, Is...>) { (f(IC<Is>{}), ...); }
template <int N, class F> DI void sfor(F f) { sfor_impl(f, std::make_integer_sequence<int, N>{}); }
#define AI __attribute__((always_inline))
DI f32x16 mfma(v4u a, v4u b, f32x16 c) {
  return __builtin_amdgcn_mfma_f32_32x32x16_bf16(__builtin_bit_cast(v8bf, a), __builtin_bit_cast(v8bf, b), c, 0, 0, 0);
}
DI unsigned pk(float a, float b) { v2bf v = {(__bf16)a, (__bf16)b}; return __builtin_bit_cast(unsigned, v); }
DI float fm(float a, float b) { return llvm_fmul_legacy(a, b); }
DI float bflo(unsigned w) { return __uint_as_float(w << 16); }
DI float bfhi(unsigned w) { return __uint_as_float(w & 0xffff0000u); }
DI float xhalf_sum(float v) { auto r = __builtin_amdgcn_permlane32_swap(__float_as_uint(v), __float_as_uint(v), false, false); return __uint_as_float(r[0]) + __uint_as_float(r[1]); }
DI float xhalf_max(float v) { auto r = __builtin_amdgcn_permlane32_swap(__float_as_uint(v), __float_as_uint(v), false, false); return __builtin_fmaxf(__uint_as_float(r[0]), __uint_as_float(r[1])); }
DI v4u ldsr(lptr p) { return *(LAS v4u*)p; }
DI void ldsw(lptr p, v4u v) { *(LAS v4u*)p = v; }
typedef __amdgpu_buffer_rsrc_t rsrc_t;
DI v4u gld(rsrc_t rs, int voff, int soff) { return __builtin_amdgcn_raw_buffer_load_b128(rs, voff, soff, AUX_SC1); }
DI v4u gldf(rsrc_t rs, int l16, int soff, int i) { return __builtin_amdgcn_raw_buffer_load_b128(rs, l16 + (i & 3) * KB, soff + (i >> 2) * 4 * KB, AUX_SC1); }
DI int opv(int x) { asm volatile("" : "+v"(x)); return x; }
DI int ops(int x) { asm volatile("" : "+s"(x)); return x; }
DI void gst(rsrc_t rs, v4u v, int voff, int soff) {
  __builtin_amdgcn_raw_buffer_store_b128(v, rs, voff, soff, AUX_SC1);
  asm volatile("s_nop 1" :: "v"(v) : "memory");
}
DI void gst2(rsrc_t rs, v2u v, int voff, int soff) { __builtin_amdgcn_raw_buffer_store_b64(v, rs, voff, soff, AUX_SC1); }
#define SCHED() __builtin_amdgcn_sched_barrier(0)
#define PINA(a) asm volatile("" : "+v"(a))
#define BAR() asm volatile("s_barrier" ::: "memory")
#define BARL() asm volatile("s_waitcnt lgkmcnt(0)\n\ts_barrier" ::: "memory")
#define WAITV(n) asm volatile("s_waitcnt vmcnt(" #n ")" ::: "memory")

template <int G, int LO> DI void dma4(rsrc_t rs, int sb, int lb, int voff) {
  unsigned keep, so;
  asm volatile("s_mov_b32 %0, m0\n\ts_add_u32 m0, %5, %7\n\ts_add_u32 %1, %4, %6\n\ts_nop 2\n\t"
               "buffer_load_dwordx4 %2, %3, %1 offen sc1 lds\n\tbuffer_load_dwordx4 %2, %3, %1 offen offset:1024 sc1 lds\n\t"
               "buffer_load_dwordx4 %2, %3, %1 offen offset:2048 sc1 lds\n\tbuffer_load_dwordx4 %2, %3, %1 offen offset:3072 sc1 lds\n\t"
               "s_mov_b32 m0, %0" : "=&s"(keep), "=&s"(so) : "v"(voff), "s"(rs), "s"(sb), "s"(lb), "i"(G), "i"(LO) : "memory", "scc");
}
template <int G, int LO> DI void dma1(rsrc_t rs, int sb, int lb, int voff) {
  unsigned keep, so;
  asm volatile("s_mov_b32 %0, m0\n\ts_add_u32 m0, %5, %7\n\ts_add_u32 %1, %4, %6\n\ts_nop 2\n\t"
               "buffer_load_dwordx4 %2, %3, %1 offen sc1 lds\n\t"
               "s_mov_b32 m0, %0" : "=&s"(keep), "=&s"(so) : "v"(voff), "s"(rs), "s"(sb), "s"(lb), "i"(G), "i"(LO) : "memory", "scc");
}
template <int G, int LO> DI void dma1p(rsrc_t rs, int sb, int lb, int voff) {
  unsigned keep, so;
  asm volatile("s_mov_b32 %0, m0\n\ts_add_u32 m0, %5, %7\n\ts_add_u32 %1, %4, %6\n\ts_nop 2\n\t"
               "buffer_load_dwordx4 %2, %3, %1 offen lds\n\t"
               "s_mov_b32 m0, %0" : "=&s"(keep), "=&s"(so) : "v"(voff), "s"(rs), "s"(sb), "s"(lb), "i"(G), "i"(LO) : "memory", "scc");
}
DI int tr_off(int lane) { const int gi = lane >> 4, q4 = (lane & 15) >> 2, pp = lane & 3; return (8 * (gi >> 1) + q4) * 64 + (gi & 1) * 32 + (((pp & 1) << 1) | (pp >> 1)) * 8; }
DI v4u trr(lptr p) {
  const v2u lo = __builtin_bit_cast(v2u, __builtin_amdgcn_ds_read_tr16_b64_v4i16((LAS v4s*)p));
  const v2u hi = __builtin_bit_cast(v2u, __builtin_amdgcn_ds_read_tr16_b64_v4i16((LAS v4s*)(p + 256)));
  return v4u{lo.x, lo.y, hi.x, hi.y};
}
DI float dot2(unsigned a, unsigned b, float c) { return __builtin_amdgcn_fdot2_f32_bf16(__builtin_bit_cast(v2bf, a), __builtin_bit_cast(v2bf, b), c, false); }
DI float sum8(v4u f, float c) {
#pragma unroll
  for (int e = 0; e < 4; ++e) { const unsigned fe = f[e]; c = dot2(fe, 0x3f803f80u, c); }
  return c;
}
DI unsigned hilo(float c) { return pk(c, c - bflo(pk(c, c))); }
template <int G> DI void touch(rsrc_t rs, int sb, int voff) {
  unsigned keep, so;
  asm volatile("s_mov_b32 %0, m0\n\ts_mov_b32 m0, %6\n\ts_add_u32 %1, %4, %5\n\ts_nop 2\n\t"
               "buffer_load_dword %2, %3, %1 offen sc1 lds\n\t"
               "s_mov_b32 m0, %0" : "=&s"(keep), "=&s"(so) : "v"(voff), "s"(rs), "s"(sb), "i"(G), "i"(L_PF) : "memory", "scc");
}
#define OTM 0
enum { OT_KV = 1, OT_O = 2, OT_X = 4, OT_H = 8 };
template <int K, int G> DI void ot(rsrc_t rs, int sb, int voff) { if (OTM & K) touch<G>(rs, sb, voff); }
template <bool EXTRA, int G, int LBUF> DI void dma_chunk(rsrc_t rs, int sb, int lb, int voff) {
  dma4<G, LBUF>(rs, sb, lb, voff);
  dma4<G + 4 * KB, LBUF + 4 * KB>(rs, sb, lb, voff);
  if (EXTRA && lb == 0) dma1<G + 32 * KB, LBUF + 32 * KB>(rs, sb, lb, voff);
}

#define MAPV 1
__host__ __device__ inline int wg_index(int b) {
  if (MAPV == 0) return ((b & 7) * 2 + (b >> 7)) * 16 + ((b >> 3) & 15);
  return (4 * ((b & 7) >> 1) + (b >> 6)) * 16 + 8 * (b & 1) + ((b >> 3) & 7);
}
DI gu32* flag_ptr(unsigned char* ws, int kind, int idx) { return (gu32*)(ws + WS_FLAGS + (size_t)(kind * 256 + idx) * FLAG_STRIDE); }
DI void set_flag(unsigned char* ws, int kind, int idx, unsigned epoch) { __hip_atomic_store(flag_ptr(ws, kind, idx), epoch, RLX_AGENT); }
DI void poll(gu32* f, bool active, unsigned epoch, bool& dead) {
  if (!dead) {
    const unsigned long long t0 = __builtin_amdgcn_s_memrealtime();
#pragma unroll 1
    for (unsigned spins = 1;; ++spins) {
      unsigned v = epoch;
      if (active) v = __hip_atomic_load(f, RLX_AGENT);
      if (__all(v == epoch)) break;
      if ((spins & 63u) == 0u && __builtin_amdgcn_s_memrealtime() - t0 > 2000000ull) { dead = true; break; }
      active = v != epoch;
      if (POLL_SLEEP) __builtin_amdgcn_s_sleep(POLL_SLEEP);
    }
  }
  __builtin_amdgcn_fence(__ATOMIC_ACQUIRE, "wavefront");
}

DI void hpoll_issue(rsrc_t rs, int so, int voff) {
  unsigned keep;
  asm volatile("s_mov_b32 %0, m0\n\ts_mov_b32 m0, %4\n\ts_nop 2\n\tbuffer_load_dword %1, %2, %3 offen " HPOLLBITS " lds\n\ts_mov_b32 m0, %0"
               : "=&s"(keep) : "v"(voff), "s"(rs), "s"(so), "i"(L_POLL) : "memory");
}
DI void hgate(rsrc_t rs, lptr L, int so, int voff, int lane, unsigned want, bool& dead) {
  if (!dead) {
    const unsigned long long t0 = __builtin_amdgcn_s_memrealtime();
#pragma unroll 1
    for (unsigned spins = 1;; ++spins) {
      if (__all(*(volatile LAS unsigned*)(L + L_POLL + (lane & 15) * 4) == want)) break;
      if ((spins & 63u) == 0u && __builtin_amdgcn_s_memrealtime() - t0 > 2000000ull) { dead = true; break; }
      if (HSLEEP) __builtin_amdgcn_s_sleep(HSLEEP);
      hpoll_issue(rs, so, voff);
      WAITV(0);
    }
  }
  asm volatile("" ::: "memory");
}
DI void lds_set(lptr L, int word, unsigned v) { asm volatile("" ::: "memory"); *(volatile LAS unsigned*)(L + L_SY + word) = v; asm volatile("" ::: "memory"); }
DI void lds_add(lptr L, int word, int lane) {
  asm volatile("" ::: "memory");
  if (lane == 0) __hip_atomic_fetch_add((LAS unsigned*)(L + L_SY + word), 1u, __ATOMIC_RELAXED, __HIP_MEMORY_SCOPE_WORKGROUP);
  asm volatile("" ::: "memory");
}
DI bool lds_last(lptr L, int word, int lane) {
  asm volatile("" ::: "memory");
  unsigned o = 0;
  if (lane == 0) o = __hip_atomic_fetch_add((LAS unsigned*)(L + L_SY + word), 1u, __ATOMIC_RELAXED, __HIP_MEMORY_SCOPE_WORKGROUP);
  asm volatile("" ::: "memory");
  return (o & 3u) == 3u;
}
DI void lds_wait(lptr L, int word, unsigned v) {
#pragma unroll 1
  for (unsigned n = 0; n < (1u << 22); ++n)
    if ((unsigned)__builtin_amdgcn_readfirstlane(*(volatile LAS unsigned*)(L + L_SY + word)) >= v) break;
  asm volatile("" ::: "memory");
}
DI void lds_wait4(lptr L, unsigned v) {
#pragma unroll 1
  for (unsigned n = 0; n < (1u << 22); ++n) {
    const v4u e = *(volatile LAS v4u*)(L + L_SY + SY_EX);
    const unsigned a = e.x < e.y ? e.x : e.y, b = e.z < e.w ? e.z : e.w;
    if ((unsigned)__builtin_amdgcn_readfirstlane(a < b ? a : b) >= v) break;
    __builtin_amdgcn_s_sleep(1);
  }
  asm volatile("" ::: "memory");
}

#define STAMP(i) do {} while (0)

DI void finish_stats2(float s, float q, float& mean, float& rstd) {
  s = xhalf_sum(s); q = xhalf_sum(q);
  mean = s * (1.f / 512.f);
  const float var = __builtin_fmaxf(q * (1.f / 512.f) - mean * mean, 0.f);
  rstd = __builtin_amdgcn_rsqf(var + 1e-5f);
}
DI void finish_stats(const v4u (&st)[4], float& mean, float& rstd) {
  float s = 0.f, q = 0.f;
#pragma unroll
  for (int i = 0; i < 4; ++i) { s += __uint_as_float(st[i].x) + __uint_as_float(st[i].z); q += __uint_as_float(st[i].y) + __uint_as_float(st[i].w); }
  finish_stats2(s, q, mean, rstd);
}
DI v4u mean_frag(float mean) {
  const float nm = -mean;
  const unsigned hh = pk(nm, nm);
  const float lo = nm - bflo(hh);
  const unsigned ll = pk(lo, lo);
  return v4u{hh, ll, hh, ll};
}
DI void store_stats(rsrc_t rs, const float (&v)[16], int lane, int soff) {
  float s = 0.f, q = 0.f;
#pragma unroll
  for (int i = 0; i < 16; ++i) { s += v[i]; q = __builtin_fmaf(v[i], v[i], q); }
  s = xhalf_sum(s); q = xhalf_sum(q);
  if (lane < 32) gst2(rs, v2u{__float_as_uint(s), __float_as_uint(q)}, lane * 16, soff);
}

struct Attn { f32x16 O; float m; };
DI float max16(const f32x16& s, float m) {
#pragma unroll
  for (int i = 0; i < 16; i += 2) m = __builtin_fmaxf(__builtin_fmaxf(m, s[i]), s[i + 1]);
  return m;
}
DI f32x16 pv(const f32x16& s, v4u v0, v4u v1, f32x16 O) {
  float p[16];
#pragma unroll
  for (int i = 0; i < 16; ++i) p[i] = __builtin_amdgcn_exp2f(s[i]);
  const v4u p0 = {pk(p[0], p[1]), pk(p[2], p[3]), pk(p[4], p[5]), pk(p[6], p[7])};
  const v4u p1 = {pk(p[8], p[9]), pk(p[10], p[11]), pk(p[12], p[13]), pk(p[14], p[15])};
  O = mfma(v0, p0, O);
  O = mfma(v1, p1, O);
  return O;
}
template <bool FIRST>
DI void attn_chunk(Attn& A, v4u Q, lptr kp, lptr vp, int vstride, int t0, int t1, int tm, const f32x16& mask) {
  const f32x16 zero = {};
  float mx = FIRST ? NEG_BIG : A.m;
  f32x16 sm = zero;
  if (tm >= 0) { sm = mfma(ldsr(kp + tm * 4096), Q, mask); mx = max16(sm, mx); }
  for (int t = t0; t < t1; ++t) { const f32x16 s = mfma(ldsr(kp + t * 4096), Q, zero); mx = max16(s, mx); }
  mx = xhalf_max(mx);
  if (FIRST) A.O = zero;
  else { const float al = __builtin_amdgcn_exp2f(A.m - mx); A.O *= al; }
  A.m = mx;
  if (tm >= 0) { A.O = pv(sm - mx, ldsr(vp + tm * vstride), ldsr(vp + tm * vstride + 1024), A.O); }
  f32x16 nm;
#pragma unroll
  for (int i = 0; i < 16; ++i) nm[i] = -mx;
  for (int t = t0; t < t1; ++t) {
    const f32x16 s = mfma(ldsr(kp + t * 4096), Q, nm);
    A.O = pv(s, ldsr(vp + t * vstride), ldsr(vp + t * vstride + 1024), A.O);
  }
}
DI float opf(float x) { asm volatile("" : "+v"(x)); return x; }
struct Fast { f32x16 O, nm; };
DI void fast_diag(Fast& F, v4u Q, lptr kp, lptr vp, const f32x16& mask) {
  const v4u k = ldsr(kp), v0 = ldsr(vp), v1 = ldsr(vp + 1024);
  const f32x16 s = mfma(k, Q, mask);
  float m0 = __builtin_fmaxf(s[0], s[1]), m1 = __builtin_fmaxf(s[2], s[3]);
#pragma unroll
  for (int i = 4; i < 16; i += 4) { m0 = __builtin_fmaxf(__builtin_fmaxf(m0, s[i]), s[i + 1]); m1 = __builtin_fmaxf(__builtin_fmaxf(m1, s[i + 2]), s[i + 3]); }
  const float mx = xhalf_max(__builtin_fmaxf(m0, m1));
  f32x16 d;
#pragma unroll
  for (int i = 0; i < 16; ++i) { d[i] = opf(s[i] - mx); F.nm[i] = -mx; }
  F.O = pv(d, v0, v1, f32x16{});
}
DI v4u exp8(const f32x16& s, int o) {
  float p[8];
#pragma unroll
  for (int i = 0; i < 8; ++i) p[i] = __builtin_amdgcn_exp2f(s[o + i]);
  return v4u{pk(p[0], p[1]), pk(p[2], p[3]), pk(p[4], p[5]), pk(p[6], p[7])};
}
template <class Hook>
DI void fast_run(Fast& F, v4u Q, lptr kp, lptr vp, int vstride, int t0, int t1, const f32x16& c0, bool hk, Hook hook) {
  lptr kq = kp + t0 * 4096, vq = vp + t0 * vstride;
  v4u k = ldsr(kq), va0 = ldsr(vq), va1 = ldsr(vq + 1024), vb0, vb1;
  f32x16 sa = mfma(k, Q, c0), sb;
  k = ldsr(kq + 4096);
  auto half = [&](const f32x16& sc, f32x16& sn, v4u vc0, v4u vc1, v4u& vn0, v4u& vn1, lptr kn) AI {
    v4u p0 = exp8(sc, 0);
    PINA(p0);
    SCHED();
    sn = mfma(k, Q, F.nm); PINA(sn); SCHED();
    vq += vstride; vn0 = ldsr(vq); vn1 = ldsr(vq + 1024); k = ldsr(kn);
    SCHED();
    float e[4];
#pragma unroll
    for (int i = 0; i < 4; ++i) e[i] = __builtin_amdgcn_exp2f(sc[8 + i]);
    asm volatile("" : "+v"(e[0]), "+v"(e[1]), "+v"(e[2]), "+v"(e[3]));
    SCHED();
    F.O = mfma(vc0, p0, F.O); PINA(F.O); SCHED();
    const v4u p1 = {pk(e[0], e[1]), pk(e[2], e[3]), pk(__builtin_amdgcn_exp2f(sc[12]), __builtin_amdgcn_exp2f(sc[13])), pk(__builtin_amdgcn_exp2f(sc[14]), __builtin_amdgcn_exp2f(sc[15]))};
    SCHED();
    F.O = mfma(vc1, p1, F.O); PINA(F.O); SCHED();
  };
  int n = t1 - t0;
#pragma unroll 1
  for (; n >= 2; n -= 2) {
    half(sa, sb, va0, va1, vb0, vb1, kq + 2 * 4096);
    kq += 2 * 4096;
    half(sb, sa, vb0, vb1, va0, va1, kq + 4096);
    if (hk) { hook(); hk = false; }
  }
  if (n) F.O = pv(sa, va0, va1, F.O);
  if (hk) hook();
}
DI void make_mask(f32x16& mk, int lane, bool old) {
  const int d = (lane & 31) - 4 * (lane >> 5);
#pragma unroll
  for (int i = 0; i < 16; ++i) { const bool le = ((i & 3) + 8 * (i >> 2)) <= d; mk[i] = (le != old) ? 0.f : NEG_BIG; }
}
template <int HD> DI v4u attn_out(const f32x16& O) {
  const float r = __builtin_amdgcn_rcpf(O[HD ? 0 : 8]);
  const int o = HD ? 8 : 0;
  return v4u{pk(O[o] * r, O[o + 1] * r), pk(O[o + 2] * r, O[o + 3] * r), pk(O[o + 4] * r, O[o + 5] * r), pk(O[o + 6] * r, O[o + 7] * r)};
}
template <int HD, class Diag, class Mid, class Hook>
DI v4u attn_item(lptr L, v4u Q, int lane, int tq, int g, Diag diag, Mid mid, bool hk, Hook hook) {
  const int l16 = lane * 16;
  const bool vl = ((lane >> 4) & 1) == HD;
  const int vstride = vl ? 4096 : 0;
  lptr kl = L + L_KV + HD * KB + l16, vloc = vl ? L + L_KV + 2 * KB + l16 : L + L_ONES;
  lptr kr = L + BUF_B + HD * KB + l16, vrem = vl ? L + BUF_B + 2 * KB + l16 : L + L_ONES;
  Fast F; f32x16 mk;
  make_mask(mk, lane, false);
  fast_diag(F, Q, kl + tq * 4096, vloc + tq * vstride, mk);
  diag();
  if (tq > 0) fast_run(F, Q, kl, vloc, vstride, 0, tq, F.nm, false, []() {});
  f32x16 c0;
#pragma unroll
  for (int i = 0; i < 16; ++i) c0[i] = opf(g >= 2 ? __builtin_fminf(F.nm[0], NEG_BIG - mk[i] * 2.f) : F.nm[0]);
  mid();
  if (g >= 1) fast_run(F, Q, kr, vrem, vstride, g >= 2 ? tq : 4, 8, c0, hk, hook);
  else if (hk) hook();
  if (__any(!(F.O[HD ? 0 : 8] <= 1.8446744e19f))) {
    Attn A;
    make_mask(mk, opv(lane), false);
    attn_chunk<true>(A, Q, kl, vloc, vstride, 0, tq, tq, mk);
    if (g >= 1) { make_mask(mk, opv(lane), true); attn_chunk<false>(A, Q, kr, vrem, vstride, g >= 2 ? tq + 1 : 4, 8, g >= 2 ? tq : -1, mk); }
    F.O = A.O;
  }
  return attn_out<HD>(F.O);
}

DI void finish_tile(lptr L, const f32x16& keep, v4u (&res)[2], float (&v)[16], int w, int lane) {
  lptr xp = L + L_KV + (w ^ 2) * 4096 + lane * 16;
#pragma unroll
  for (int qd = 0; qd < 4; ++qd) {
    const f32x4 r = *(LAS f32x4*)(xp + qd * 1024);
#pragma unroll
    for (int j = 0; j < 4; ++j) v[4 * qd + j] = keep[4 * qd + j] + r[j];
  }
#pragma unroll
  for (int e = 0; e < 2; ++e)
#pragma unroll
    for (int j = 0; j < 4; ++j) { v[8 * e + 2 * j] += bflo(res[e][j]); v[8 * e + 2 * j + 1] += bfhi(res[e][j]); }
#pragma unroll
  for (int e = 0; e < 2; ++e) res[e] = v4u{pk(v[8 * e], v[8 * e + 1]), pk(v[8 * e + 2], v[8 * e + 3]), pk(v[8 * e + 4], v[8 * e + 5]), pk(v[8 * e + 6], v[8 * e + 7])};
}
DI void send_tile(lptr L, const f32x16& send, int w, int lane) {
  lptr xp = L + L_KV + w * 4096 + lane * 16;
#pragma unroll
  for (int qd = 0; qd < 4; ++qd) *(LAS f32x4*)(xp + qd * 1024) = f32x4{send[4 * qd], send[4 * qd + 1], send[4 * qd + 2], send[4 * qd + 3]};
}

__global__ void __launch_bounds__(NTHR, 2) fused_layers(Params p) {
  extern __shared__ __attribute__((aligned(16))) unsigned char smem[];
  lptr L = (lptr)smem;
  const int tid = threadIdx.x, wave = __builtin_amdgcn_readfirstlane(tid >> 6);
  const int b = blockIdx.x, me = wg_index(b), g = me >> 4, m = me & 15;
  unsigned char* ws = p.ws;
  const rsrc_t rs = __builtin_amdgcn_make_buffer_rsrc(ws, 0, WS_BYTES, 0x00020000);
  const int th = m >> 3, fb = m & 7;
  bool dead = false;
#define LANE_VALS() const int lane = opv(tid) & 63, r32 = lane & 31, hf = lane >> 5, l16 = lane * 16; (void)r32; (void)hf; (void)l16
  if (tid < 12) { if (tid < 8) *(LAS unsigned*)(L + L_ONES + (tid >> 2) * 1024 + (tid & 3) * 4) = 0x3f803f80u; *(LAS unsigned*)(L + L_SY + tid * 4) = 0u; }
  BARL();

#define FILL_DEF() \
  auto fill_wo = [&](int nd) AI { \
    const rsrc_t ro = __builtin_amdgcn_make_buffer_rsrc((void*)p.wo, 0, 512 * 512 * 2, 0x00020000); \
    const int so_ = (wave * 2 + nd) * 16 * KB + fb * 128, lo_ = (wave < 4 ? BUF_D : BUF_A - 32 * KB) + (wave * 8 + nd * 4) * KB, vo_ = (lane >> 2) * KB + (lane & 3) * 16; \
    dma1p<0, 0>(ro, so_, lo_, vo_); dma1p<64, KB>(ro, so_, lo_, vo_); dma1p<256 * KB, 2 * KB>(ro, so_, lo_, vo_); dma1p<256 * KB + 64, 3 * KB>(ro, so_, lo_, vo_); \
  }; \
  auto fill0 = [&]() AI { \
    if (layer == 0) fill_wo(0); \
    else { dma4<0, BUF_C>(rs, WS_WC + (m * 231 + wave * 4) * KB, wave * 4 * KB, l16); if (wave == 7) dma1<32 * KB, BUF_C + 32 * KB>(rs, WS_WC + m * 231 * KB, 0, l16); } \
  }; \
  auto fill1 = [&]() AI { fill_wo(1); }
  if (wave < 4) {
    const int w = wave;
    const int ttr = 4 * g + 2 * th + (w & 1), kh = w >> 1, ftile = 2 * fb + kh;
    const int tta = 4 * g + w;
    v4u res[2];
    unsigned epoch;
    STAMP(0);
#pragma unroll 1
    for (int layer = 0; layer < 2; ++layer) {
      const int act = WS_ACT + layer * A_LAYER;
      const int fl = layer * F_PER_LAYER;
      const int sb = 2 + layer * 28;
      (void)sb;
      v4u xf[32];
      v4u Qa;
      {
        LANE_VALS();
        STAMP(sb + 0);
        f32x16 aK = {}, aV = {}, aQ = {};
        float mean, rstd;
        const int tro = tr_off(lane);
        constexpr int PQ = 4;
        v4u wq[PQ];
        lptr pc = L + opv(BUF_C + tro);
        auto kv_pass = [&](auto FIRST_) AI {
          constexpr bool FIRST = decltype(FIRST_)::v;
          constexpr int WIN = KVWIN, PD = 3;
          v4u st[4];
          const int sto = ops(act + A_ST0 + tta * 4096), xo = ops(FIRST ? tta * 32 * KB : act + A_XT + tta * 32 * KB);
          lptr tw = L + opv(L_KV + w * 4096 + (lane >> 3) * 128 + (((lane & 7) ^ (lane >> 3)) * 16)), tr = L + opv(L_KV + w * 4096 + r32 * 128);
          const int sw = (r32 & 7) ^ hf;
          auto xpose = [&](int j) AI {
#pragma unroll
            for (int i = 0; i < 4; ++i) ldsw(tw + i * KB, xf[4 * j + i]);
#pragma unroll
            for (int e = 0; e < 4; ++e) xf[4 * j + e] = ldsr(tr + ((2 * e) ^ sw) * 16);
          };
          const rsrc_t rx = __builtin_amdgcn_make_buffer_rsrc((void*)p.x, 0, 2048 * 1024, 0x00020000);
          const int vx = (lane >> 3) * KB + (lane & 7) * 16;
          auto xraw = [&](int i) AI { return __builtin_amdgcn_raw_buffer_load_b128(rx, vx + (i >> 2) * 128, xo + (i & 3) * 8 * KB, 0); };
          if constexpr (FIRST) {
            const unsigned epoch_v = __hip_atomic_load((gu32*)(ws + WS_EPOCH), RLX_AGENT);
#pragma unroll
            for (int i = 0; i < XW0; ++i) xf[i] = xraw(i);
            SCHED();
            epoch = __builtin_amdgcn_readfirstlane(epoch_v) + 1u;
            if (w == 0) lds_set(L, SY_EP, epoch);
            xpose(0);
#pragma unroll
            for (int i = XW0; i < XW1; ++i) xf[i] = xraw(i);
            asm volatile("s_waitcnt lgkmcnt(0)" ::: "memory");
          } else {
#pragma unroll
            for (int i = 0; i < 4; ++i) xf[i] = gldf(rs, l16, xo, i);
#pragma unroll
            for (int i = 0; i < 4; ++i) st[i] = gld(rs, hf * 2048 + r32 * 16 + i * 512, sto);
#pragma unroll
            for (int i = 4; i < WIN; ++i) xf[i] = gldf(rs, l16, xo, i);
          }
          SCHED();
          BAR();
          v4u wk[PD], wv[PD];
          lptr pa = L + opv(BUF_A + tro), pb = L + opv(BUF_B + tro);
#pragma unroll
          for (int i = 0; i < PD; ++i) { wk[i] = trr(pa + 2 * i * KB); wv[i] = trr(pa + (2 * i + 1) * KB); }
          float s0 = 0.f, s1 = 0.f, q0 = 0.f, q1 = 0.f;
          sfor<32>([&](auto S_) AI {
            constexpr int s = decltype(S_)::v, n = s + PD;
            const v4u ck = wk[s % PD], cv = wv[s % PD];
            if constexpr (n == 16) BAR();
            aK = mfma(ck, xf[s], aK); PINA(aK); SCHED();
            if constexpr (n < 16) wk[s % PD] = trr(pa + 2 * n * KB); else if constexpr (n < 32) wk[s % PD] = trr(pb + 2 * (n - 16) * KB);
            SCHED();
            if constexpr (FIRST) {
              if constexpr (s + XW1 < 32) xf[s + XW1] = xraw(s + XW1);
              if constexpr (s + XW1 == 32) { const GAS v4u* xs = (const GAS v4u*)(p.x + (size_t)(ttr * 32 + r32) * 512 + ftile * 32 + hf * 8); res[0] = xs[0]; res[1] = xs[2]; }
            } else if constexpr (s + WIN < 32) xf[s + WIN] = gldf(rs, l16, xo, s + WIN);
            SCHED();
            aV = mfma(xf[s], cv, aV); PINA(aV); SCHED();
            if constexpr (n < 16) wv[s % PD] = trr(pa + (2 * n + 1) * KB); else if constexpr (n < 32) wv[s % PD] = trr(pb + (2 * (n - 16) + 1) * KB);
            if constexpr (FIRST && (s & 3) == 0 && s < 28) xpose(s / 4 + 1);
            SCHED();
            if constexpr (WO_EARLY && !FIRST && s == 16) lds_add(L, SY_X0, lane);
            if constexpr (s >= 16) { aQ = mfma(wq[(s - 16) % PQ], xf[s - 16], aQ); PINA(aQ); SCHED(); }
            if constexpr (n >= 16) { wq[(n - 16) % PQ] = trr(pc + (n - 16) * KB); SCHED(); }
            if constexpr (FIRST) {
              const unsigned e0 = xf[s].x, e1 = xf[s].y, e2 = xf[s].z, e3 = xf[s].w;
              s0 = dot2(e0, 0x3f803f80u, s0); q0 = dot2(e0, e0, q0); s1 = dot2(e1, 0x3f803f80u, s1); q1 = dot2(e1, e1, q1);
              s0 = dot2(e2, 0x3f803f80u, s0); q0 = dot2(e2, e2, q0); s1 = dot2(e3, 0x3f803f80u, s1); q1 = dot2(e3, e3, q1);
            } else if constexpr (s == 6) finish_stats(st, mean, rstd);
            SCHED();
          });
          if constexpr (FIRST) finish_stats2(s0 + s1, q0 + q1, mean, rstd);
        };
        if (layer == 0) kv_pass(IC<1>{}); else kv_pass(IC<0>{});
        if (lane < 32) *(LAS float*)(L + L_RS + w * 128 + lane * 4) = rstd;
        const v4u xe = mean_frag(mean);
        STAMP(sb + 1);
        BAR();
        const v4u wxk = ldsr(L + L_X + l16), wxv = ldsr(L + L_X + KB + l16);
        f32x4 rsv[4];
#pragma unroll
        for (int qd = 0; qd < 4; ++qd) rsv[qd] = *(LAS f32x4*)(L + L_RS + w * 128 + qd * 32 + hf * 16);
        lptr kvl = L + opv(L_KV + w * 4096 + l16);
        const int kvo = ops(act + A_KV + (me * 4 + w) * 4096);
        v4u kvf;
        SCHED();
        sfor<17>([&](auto S_) AI {
          constexpr int s = 16 + decltype(S_)::v, n = s + PQ - 1;
          aQ = mfma(wq[s % PQ], s < 32 ? xf[s < 32 ? s : 0] : xe, aQ);
          PINA(aQ); SCHED();
          if constexpr (n < 32) wq[n % PQ] = trr(pc + n * KB);
          else if constexpr (n == 32) wq[n % PQ] = ldsr(L + L_X + 2 * KB + l16);
          SCHED();
          if constexpr (s == 16) { aK = mfma(wxk, xe, aK); aV = mfma(xe, wxv, aV); PINA(aK); PINA(aV); }
          if constexpr (s >= 18 && s < 26) {
            constexpr int e = s - 18, f = e >> 1, o = (f & 1) * 8 + (e & 1) * 4;
            if constexpr (f < 2) { kvf[(e & 1) * 2] = pk(fm(aK[o], rstd), fm(aK[o + 1], rstd)); kvf[(e & 1) * 2 + 1] = pk(fm(aK[o + 2], rstd), fm(aK[o + 3], rstd)); }
            else { const f32x4 r4 = rsv[o >> 2]; kvf[(e & 1) * 2] = pk(fm(aV[o], r4[0]), fm(aV[o + 1], r4[1])); kvf[(e & 1) * 2 + 1] = pk(fm(aV[o + 2], r4[2]), fm(aV[o + 3], r4[3])); }
            if constexpr (e & 1) { gst(rs, kvf, l16 + f * KB, kvo); ldsw(kvl + f * KB, kvf); }
          }
          SCHED();
        });
        const float qs = rstd * (0.25f * LOG2E);
        ldsw(L + L_QB + w * KB + l16, v4u{pk(fm(aQ[8], qs), fm(aQ[9], qs)), pk(fm(aQ[10], qs), fm(aQ[11], qs)), pk(fm(aQ[12], qs), fm(aQ[13], qs)), pk(fm(aQ[14], qs), fm(aQ[15], qs))});
        Qa = v4u{pk(fm(aQ[0], qs), fm(aQ[1], qs)), pk(fm(aQ[2], qs), fm(aQ[3], qs)), pk(fm(aQ[4], qs), fm(aQ[5], qs)), pk(fm(aQ[6], qs), fm(aQ[7], qs))};
        STAMP(sb + 2);
        BARL();
        STAMP(sb + 3);
      }
      {
        LANE_VALS();
        FILL_DEF();
        auto kvpub = [&]() AI { WAITV(0); if (lds_last(L, SY_KV, lane)) set_flag(ws, fl + F_KV, me, epoch); };
        kvpub();
        if (w == 0 && g >= 1) {
          __builtin_amdgcn_s_sleep(PD_KV); poll(flag_ptr(ws, fl + F_KV, me - 16), lane == 0, epoch, dead);
          const int src = act + A_KV + (me - 16) * 16 * KB;
          dma4<0, BUF_B + 16 * KB>(rs, src, 0, l16); dma4<4 * KB, BUF_B + 20 * KB>(rs, src, 0, l16);
          dma4<8 * KB, BUF_B + 24 * KB>(rs, src, 0, l16); dma4<12 * KB, BUF_B + 28 * KB>(rs, src, 0, l16);
        }
        STAMP(sb + 19);
        const v4u of = attn_item<0>(L, Qa, lane, w, g, []() {}, [&]() AI {
          STAMP(sb + 4);
          WAITV(0);
          BAR();
          STAMP(sb + 5);
          fill0();
        }, layer == 0, fill1);
        gst(rs, of, l16, act + A_OT + (tta * 32 + 2 * m) * KB);
        STAMP(sb + 6);
        WAITV(0);
        BAR();
        if (tid == 0) set_flag(ws, fl + F_O, me, epoch);
        STAMP(sb + 7);
        if (w == 0) { __builtin_amdgcn_s_sleep(PD_O); poll(flag_ptr(ws, fl + F_O, g * 16 + (lane & 15)), lane < 16, epoch, dead); lds_set(L, SY_GO, 3 * layer + 1); }
        else lds_wait(L, SY_GO, 3 * layer + 1);
        STAMP(sb + 8);
      }
      {
        LANE_VALS();
        const int oo = ops(act + A_OT + (ttr * 32 + 16 * kh) * KB);
#pragma unroll
        for (int i = 0; i < 16; ++i) xf[i] = gldf(rs, l16, oo, i);
        SCHED();
        f32x16 keep = {}, send = {};
        const int tro = tr_off(lane);
        lptr k0 = L + opv(BUF_D + 3 * kh * KB + tro), s0 = L + opv(BUF_D + (1 + kh) * KB + tro);
        lptr k1 = L + opv(BUF_A + 3 * kh * KB + tro), s1 = L + opv(BUF_A + (1 + kh) * KB + tro);
        constexpr int PD = 3;
        v4u wk[PD], wsd[PD];
#pragma unroll
        for (int i = 0; i < PD; ++i) { wk[i] = trr(k0 + i * 4 * KB); wsd[i] = trr(s0 + i * 4 * KB); }
        sfor<16>([&](auto S_) AI {
          constexpr int s = decltype(S_)::v, n = s + PD;
          send = mfma(wsd[s % PD], xf[s], send); PINA(send); SCHED();
          if constexpr (n < 8) wsd[s % PD] = trr(s0 + n * 4 * KB); else if constexpr (n < 16) wsd[s % PD] = trr(s1 + (n - 8) * 4 * KB);
          SCHED();
          keep = mfma(wk[s % PD], xf[s], keep); PINA(keep); SCHED();
          if constexpr (n < 8) wk[s % PD] = trr(k0 + n * 4 * KB); else if constexpr (n < 16) wk[s % PD] = trr(k1 + (n - 8) * 4 * KB);
          SCHED();
        });
        send_tile(L, send, w, lane);
        lds_set(L, SY_EX + 4 * w, 2 * layer + 1);
        STAMP(sb + 9);
        lds_wait(L, SY_EX + 4 * (w ^ 2), 2 * layer + 1);
        float v[16];
        finish_tile(L, keep, res, v, w, lane);
        const int so = act + A_X1T + (ttr * 32 + 2 * ftile) * KB;
        gst(rs, res[0], l16, so); gst(rs, res[1], l16 + KB, so);
        store_stats(rs, v, lane, act + A_ST1 + ttr * 4096 + (ftile >> 1) * 512 + (ftile & 1) * 8);
        STAMP(sb + 10);
        WAITV(0);
        if (lds_last(L, SY_DR, lane)) set_flag(ws, fl + F_X1, me, epoch);
        if (w == 0) {
          STAMP(sb + 11);
          __builtin_amdgcn_s_sleep(PD_X1); poll(flag_ptr(ws, fl + F_X1, g * 16 + (lane & 15)), lane < 16, epoch, dead);
          lds_set(L, SY_GO, 3 * layer + 2);
        } else lds_wait(L, SY_GO, 3 * layer + 2);
        STAMP(sb + 12);
      }
      {
        LANE_VALS();
        constexpr int WIN = 12;
        v4u st[4];
        const int sto = ops(act + A_ST1 + tta * 4096), xo = ops(act + A_X1T + tta * 32 * KB);
#pragma unroll
        for (int i = 0; i < 4; ++i) xf[i] = gldf(rs, l16, xo, i);
#pragma unroll
        for (int i = 0; i < 4; ++i) st[i] = gld(rs, hf * 2048 + r32 * 16 + i * 512, sto);
#pragma unroll
        for (int i = 4; i < WIN; ++i) xf[i] = gldf(rs, l16, xo, i);
        SCHED();
        lds_wait(L, SY_HWL, 4 * layer + 4);
        f32x16 acc[3];
        acc[0] = f32x16{}; acc[1] = f32x16{}; acc[2] = f32x16{};
        float mean, rstd, c1, sd; v4u xe;
        lptr pbuf[4] = {L + BUF_A + l16, L + opv(BUF_B + l16), L + opv(BUF_C + l16), L + opv(BUF_D + l16)};
        constexpr int PQ = 4;
        v4u wq[PQ];
        {
          constexpr int PD = 3;
          v4u w0[PD], w1[PD];
          lptr p0 = pbuf[2], p1 = pbuf[1];
#pragma unroll
          for (int i = 0; i < PD; ++i) { w0[i] = ldsr(p0 + i * KB); w1[i] = ldsr(p1 + i * KB); }
          sfor<33>([&](auto S_) AI {
            constexpr int s = decltype(S_)::v, n = s + PD;
            if constexpr (n == 17) BAR();
            const v4u xa = s < 32 ? xf[s < 32 ? s : 0] : xe;
            acc[0] = mfma(w0[s % PD], xa, acc[0]); PINA(acc[0]); SCHED();
            if constexpr (n < 33) w0[s % PD] = ldsr(p0 + n * KB);
            SCHED();
            if constexpr (s + WIN < 32) xf[s + WIN] = gldf(rs, l16, xo, s + WIN);
            SCHED();
            acc[1] = mfma(w1[s % PD], xa, acc[1]); PINA(acc[1]); SCHED();
            if constexpr (n < 33) w1[s % PD] = ldsr(p1 + n * KB);
            SCHED();
            if constexpr (s >= 17) { acc[2] = mfma(wq[(s - 17) % PQ], xf[s - 17], acc[2]); PINA(acc[2]); SCHED(); }
            if constexpr (n >= 17) { wq[(n - 17) % PQ] = ldsr(pbuf[3] + (n - 17) * KB); SCHED(); }
            if constexpr (s == 6) { finish_stats(st, mean, rstd); xe = mean_frag(mean); c1 = -LOG2E * rstd; sd = __builtin_amdgcn_rcpf(rstd); }
            SCHED();
          });
        }
        STAMP(sb + 13);
        const int ho = ops(act + A_HT + (tta * 112 + 7 * m) * KB);
        float ev[8]; v4u hw4;
        auto epi = [&](const f32x16& a, int k, int tile) AI {
          const int e = k >> 1;
          if (!(k & 1)) ev[e] = __builtin_fmaf(__builtin_amdgcn_exp2f(a[e] * c1), sd, sd);
          else {
            ev[e] = fm(a[8 + e], __builtin_amdgcn_rcpf(ev[e]));
            if (e & 1) hw4[e >> 1] = pk(ev[e - 1], ev[e]);
            if (e == 7) gst(rs, hw4, l16 + (tile & 3) * KB, ho + (tile >> 2) * 4 * KB);
          }
        };
        constexpr int DWIN = 16;
        v4u hfr[32];
        const int hb = ops(act + A_HT + (ttr * 112 + 56 * kh) * KB), fbo = ops(fb);
        auto hld = [&](int s) AI {
          const int t = s >> 3, jj = s & 7;
          return gld(rs, l16 + (t & 3) * KB, hb + (((jj + fbo) & 7) * 7 + (t & 4)) * KB);
        };
        auto hpub = [&](unsigned nt) AI { if (lds_last(L, SY_H, lane)) set_flag(ws, fl + F_H + nt - 2, me, epoch * 8u + nt); };
        sfor<149>([&](auto U_) AI {
          constexpr int u = decltype(U_)::v + 16, t = 2 + u / 33, s = u % 33, n = s + PQ - 1;
          constexpr int bufs[8] = {2, 1, 3, 0, 2, 1, 3, 0};
          const v4u cq = wq[u % PQ];
          if constexpr (u == 17) { BAR(); STAMP(sb + 27); }
          if constexpr (n == 33 && t < 6) { if constexpr (PUBMODE == 0 && t >= 4) WAITV(1); BAR(); }
          if constexpr (PUBMODE == 1 && t == 4 && s == HPUBS) { WAITV(1); hpub(2); }
          if constexpr (PUBMODE == 1 && t >= 5 && s == HPUBS) { WAITV(0); hpub(t - 1); }
          if constexpr (EARLY_FREE && t >= 3 && t <= 5 && s == 1) BAR();
          if constexpr (t == 6 && s == 17) { if constexpr (PUBMODE == 0) WAITV(1); BAR(); }
          if constexpr (s == 0) acc[t % 3] = mfma(cq, xf[0], f32x16{});
          else acc[t % 3] = mfma(cq, s < 32 ? xf[s < 32 ? s : 0] : xe, acc[t % 3]);
          PINA(acc[t % 3]); SCHED();
          if constexpr (n < 33) wq[(u + PQ - 1) % PQ] = ldsr(pbuf[bufs[t]] + n * KB);
          else if constexpr (t < 6) wq[(u + PQ - 1) % PQ] = ldsr(pbuf[bufs[t + 1]] + (n - 33) * KB);
          SCHED();
          if constexpr (t == 2) { if constexpr (s >= 17) epi(acc[0], s - 17, 0); }
          else if constexpr (t == 3) { if constexpr (s < 16) epi(acc[1], s, 1); if constexpr (s >= 17) epi(acc[2], s - 17, 2); }
          else if constexpr (s >= 1 && s < 17) epi(acc[(t - 1) % 3], s - 1, t - 1);
          if constexpr (t == 6 && s >= 17 && (s - 17) % (16 / HPRE) == 0) hfr[(s - 17) / (16 / HPRE)] = hld((s - 17) / (16 / HPRE));
          if constexpr (s == 32 && t < 6) STAMP(sb + 21 + t);
          SCHED();
        });
        STAMP(sb + 14);
        BAR();
        STAMP(sb + 15);
        f32x16 keep = {}, send = {};
        constexpr int PD = PDD;
        static constexpr int dbufs[7] = {0, 2, 1, 3, 0, 2, 1};
        const int ko = 3 * kh * KB, sdo = (1 - 2 * kh) * KB;
        lptr kb[4] = {pbuf[0] + ko, pbuf[1] + ko, pbuf[2] + ko, pbuf[3] + ko};
        lptr sp[4] = {kb[0] + sdo, kb[1] + sdo, kb[2] + sdo, kb[3] + sdo};
        v4u wk[PD], wsd[PD];
#pragma unroll
        for (int i = 0; i < PD; ++i) { wk[i] = ldsr(kb[dbufs[0]] + i * 4 * KB); wsd[i] = ldsr(sp[dbufs[0]] + i * 4 * KB); }
        constexpr auto nld = [](int s) { int c = 0; for (int i = 0; i < s; ++i) c += (i + HPRE < 24 && i < 13) + (i >= 13 && i < 45) + (HPRE == 8 && i < 6 && (i & 1)); return c; };
        sfor<56>([&](auto S_) AI {
          constexpr int s = decltype(S_)::v, n = s + PD;
          constexpr int ld = s + HPRE < 24 && s < 13 ? s + HPRE : -1, ld2 = (s >= 13 && s < 45) ? s + 11 : -1, ld3 = (HPRE == 8 && s < 6 && (s & 1)) ? 21 + s / 2 : -1;
          const v4u ck = wk[s % PD], cs = wsd[s % PD];
          if constexpr (n < 56 && (n & 7) == 0) {
            if constexpr (n == 8 && H6S >= 8 - PD) { if constexpr (HPRE == 16) WAITV(16); else WAITV(8); }
            if constexpr (n == 24 && H7S >= 24 - PD) asm volatile("s_waitcnt vmcnt(%0)" :: "n"(nld(24 - PD) - nld(16 / E6)) : "memory");
            BAR();
            if constexpr (n >= 24 && n <= 40) STAMP(sb + 17 + n / 8);
          }
          send = mfma(cs, hfr[s % 32], send); PINA(send); SCHED();
          if constexpr (n < 56) wsd[s % PD] = ldsr(sp[dbufs[n >> 3]] + (n & 7) * 4 * KB);
          SCHED();
          if constexpr (ld >= 0) hfr[ld % 32] = hld(ld);
          if constexpr (ld2 >= 0) hfr[ld2 % 32] = hld(ld2);
          if constexpr (ld3 >= 0) hfr[ld3 % 32] = hld(ld3);
          if constexpr (s < 16 / E6) { sfor<E6 / 2>([&](auto K_) AI { epi(acc[6 % 3], E6 * s + decltype(K_)::v, 6); }); }
          SCHED();
          keep = mfma(ck, hfr[s % 32], keep); PINA(keep); SCHED();
          if constexpr (n < 56) wk[s % PD] = ldsr(kb[dbufs[n >> 3]] + (n & 7) * 4 * KB);
          SCHED();
          if constexpr (s < 16 / E6) { sfor<E6 / 2>([&](auto K_) AI { epi(acc[6 % 3], E6 * s + E6 / 2 + decltype(K_)::v, 6); }); }
          if constexpr (H6S < 8 - PD && s == H6S) { if constexpr (HPRE == 16) WAITV(16); else WAITV(8); hpub(6); }
          if constexpr (H7S < 24 - PD && s == H7S) { asm volatile("s_waitcnt vmcnt(%0)" :: "n"(nld(H7S + 1) - nld(16 / E6)) : "memory"); hpub(7); }
          SCHED();
        });
        send_tile(L, send, w, lane);
        lds_set(L, SY_EX + 4 * w, 2 * layer + 2);
        STAMP(sb + 16);
        lds_wait(L, SY_EX + 4 * (w ^ 2), 2 * layer + 2);
        float v[16];
        finish_tile(L, keep, res, v, w, lane);
        if (layer == 0) {
          const int nact = act + A_LAYER;
          const int so = nact + A_XT + (ttr * 32 + 2 * ftile) * KB;
          gst(rs, res[0], l16, so); gst(rs, res[1], l16 + KB, so);
          store_stats(rs, v, lane, nact + A_ST0 + ttr * 4096 + (ftile >> 1) * 512 + (ftile & 1) * 8);
          STAMP(sb + 17);
          WAITV(0);
          if (lds_last(L, SY_DR, lane)) set_flag(ws, fl + F_X2, me, epoch);
          if (w == 0) {
            STAMP(sb + 18);
            __builtin_amdgcn_s_sleep(PD_X2); poll(flag_ptr(ws, fl + F_X2, g * 16 + (lane & 15)), lane < 16, epoch, dead);
            lds_set(L, SY_GO, 3);
          } else lds_wait(L, SY_GO, 3);
        } else {
          GAS v4u* od = (GAS v4u*)(p.out + (size_t)(ttr * 32 + r32) * 512 + ftile * 32 + hf * 8);
          od[0] = res[0]; od[2] = res[1];
          STAMP(sb + 17);
        }
      }
    }
  } else {
    const int hw = wave - 4;
    const int lb = hw * 8 * KB;
    const int wc = WS_WC + m * 231 * KB + lb, wdn = WS_WD + fb * 224 * KB + lb;
    const rsrc_t rw = __builtin_amdgcn_make_buffer_rsrc((void*)p.wqkv, 0, 512 * 1536 * 2, 0x00020000);
    constexpr int KS = 16 * 1536 * 2;
    const int l16h = (tid & 63) * 16, vraw = ((tid & 63) >> 2) * 3072 + (tid & 3) * 16;
    const int wkv = hw * 4 * KS + 64 * m, wqq = hw * 8 * KS + 64 * m;
#define DMA(EXTRA, base, G, BUF) dma_chunk<EXTRA, G, BUF>(rs, base, lb, l16h)
#define DMA_Q() sfor<8>([&](auto I_) AI { constexpr int i = decltype(I_)::v; dma1p<i * KS, BUF_C + i * KB>(rw, wqq, lb, vraw); })
#define STAGE(arr, base, G) do { const int o_ = ops(base) + (G); _Pragma("unroll") for (int i_ = 0; i_ < 8; ++i_) arr[i_] = gldf(rs, l16, o_, i_); } while (0)
#define ZERO(arr) do { _Pragma("unroll") for (int i_ = 0; i_ < 8; ++i_) arr[i_] = v4u{0u, 0u, 0u, 0u}; } while (0)
#define PIN(arr) asm volatile("" : "+v"(arr[0]), "+v"(arr[1]), "+v"(arr[2]), "+v"(arr[3]), "+v"(arr[4]), "+v"(arr[5]), "+v"(arr[6]), "+v"(arr[7]))
#define PUT(arr, BUF) do { if (DBG_NOPUT) { PIN(arr); break; } lptr d_ = L + opv((BUF) + lb + l16); _Pragma("unroll") for (int i_ = 0; i_ < 8; ++i_) ldsw(d_ + i_ * KB, arr[i_]); } while (0)
#define GET(arr, BUF) do { lptr d_ = L + opv((BUF) + lb + l16); _Pragma("unroll") for (int i_ = 0; i_ < 8; ++i_) arr[i_] = ldsr(d_ + i_ * KB); } while (0)
    const int vpd = ((MAPV == 0 ? (g & 1) * 2 + th : g & 3) * 64 + (tid & 63)) * 128, vpc = ((MAPV == 0 ? (g & 1) * 2 : g & 3) * 64 + (tid & 63)) * 128;
#define PFC(c) do { if (hw == 3) touch<(c) * CH>(rs, wc - lb, vpc); if (MAPV == 0 && hw == 2) touch<(c) * CH + 8 * KB>(rs, wc - lb, vpc); } while (0)
#define PFD(c) do { if (hw == 3) touch<(c) * 32 * KB>(rs, wdn - lb, vpd); } while (0)
#define HFLAG(nt) do { if (tid == 320) set_flag(ws, fl + F_H + (nt) - 2, me, epoch * 8u + (nt)); } while (0)
#define BARL8() asm volatile("s_waitcnt lgkmcnt(8)\n\ts_barrier" ::: "memory")
#define DBG_NOPUT 0
#define HISSUE(nt) do { if (hw == 0) hpoll_issue(rs, hco + ((nt) - 2) * 256 * FLAG_STRIDE, vc); } while (0)
#define HGATE(nt, younger) do { if (hw == 0) { WAITV(younger); hgate(rs, L, hco + ((nt) - 2) * 256 * FLAG_STRIDE, vc, lane, epoch * 8u + (nt), dead); } } while (0)
    v4u a0[8], a1[8];
    v4u uw[8];
    {
      LANE_VALS();
      sfor<16>([&](auto I_) AI { constexpr int i = decltype(I_)::v; dma1p<((i >> 3) * 16 + ((i & 7) >> 1)) * KS + ((i & 1) ? 2048 : 1024), (i >> 3) * CH + (i & 7) * KB>(rw, wkv, lb, vraw); });
      DMA_Q();
      STAMP(0);
      WAITV(16); BAR();
      STAMP(1);
      __builtin_amdgcn_s_setprio(HPRIO);
      const int tro = tr_off(lane);
      float c[6] = {0.f, 0.f, 0.f, 0.f, 0.f, 0.f};
      {
        lptr ta = L + opv(BUF_A + lb + tro);
#pragma unroll
        for (int i = 0; i < 4; ++i) { c[0] = sum8(trr(ta + 2 * i * KB), c[0]); c[1] = sum8(trr(ta + (2 * i + 1) * KB), c[1]); }
      }
      GET(a0, BUF_A); PIN(a0);
      WAITV(0); BAR();
      STAMP(2);
      {
        lptr tb = L + opv(BUF_B + lb + tro), tc = L + opv(BUF_C + lb + tro);
#pragma unroll
        for (int i = 0; i < 4; ++i) {
          c[3] = sum8(trr(tb + 2 * i * KB), c[3]); c[4] = sum8(trr(tb + (2 * i + 1) * KB), c[4]);
          c[2] = sum8(trr(tc + 2 * i * KB), c[2]); c[5] = sum8(trr(tc + (2 * i + 1) * KB), c[5]);
        }
        const unsigned hk = hilo(xhalf_sum(c[0] + c[3])), hv = hilo(xhalf_sum(c[1] + c[4])), hq = hilo(xhalf_sum(c[2] + c[5]));
        if (lane < 32) {
          lptr xp = L + L_X + (hw >> 1) * 512 + (hw & 1) * 8 + l16;
          *(LAS v2u*)xp = v2u{hk, hk}; *(LAS v2u*)(xp + KB) = v2u{hv, hv}; *(LAS v2u*)(xp + 2 * KB) = v2u{hq, hq};
        }
      }
      STAMP(5);
      {
        const int row = lane >> 3, pc = lane & 7, r = b < 168 ? b - 112 : 0;
        const bool gu = b < 112;
        const u16* s0 = gu ? p.wg + (size_t)((b & 3) * 128 + 32 * hw + row) * 1792 + 64 * (b >> 2) + 8 * pc : p.wd + (size_t)((r % 7) * 256 + 64 * hw + row) * 512 + 64 * (r / 7) + 8 * pc;
        const u16* s1 = gu ? s0 + (p.wu - p.wg) : s0 + 32 * 512;
        const int st8 = gu ? 8 * 1792 : 8 * 512;
#pragma unroll
        for (int i = 0; i < 8; ++i) uw[i] = *(const GAS v4u*)((i < 4 ? s0 : s1) + (i & 3) * st8);
        SCHED();
      }
      GET(a1, BUF_B); PIN(a1);
      __builtin_amdgcn_s_setprio(0);
      BARL();
      STAMP(3);
    }
    const unsigned epoch = __builtin_amdgcn_readfirstlane(*(volatile LAS unsigned*)(L + L_SY + SY_EP));
#pragma unroll 1
    for (int layer = 0; layer < 2; ++layer) {
      LANE_VALS();
      const int act = WS_ACT + layer * A_LAYER;
      const int fl = layer * F_PER_LAYER;
      const int sb = 2 + layer * 28;
      (void)sb;
      v4u d2[8], d3[8], d4[8], d5[8], d6[8];
      if (layer == 1) {
        BARL();
      if (b == 0 && hw == 2) {
        for (unsigned n = 0; n < (1u << 16); ++n) {
          gu32* f = flag_ptr(ws, F_P2, lane);
          const unsigned v0 = __hip_atomic_load(f, RLX_AGENT), v1 = __hip_atomic_load(f + 16 * FLAG_STRIDE, RLX_AGENT);
          const unsigned v2 = __hip_atomic_load(f + 32 * FLAG_STRIDE, RLX_AGENT), v3 = __hip_atomic_load(f + 48 * FLAG_STRIDE, RLX_AGENT);
          if (__all(v0 == epoch && v1 == epoch && v2 == epoch && v3 == epoch)) break;
        }
        if (lane == 0) __hip_atomic_store((gu32*)(ws + WS_EPOCH), epoch, RLX_AGENT);
      }
        STAMP(sb + 0);
        WAITV(0);

        BAR();
        if (WO_EARLY) {
          const rsrc_t ro = __builtin_amdgcn_make_buffer_rsrc((void*)p.wo, 0, 512 * 512 * 2, 0x00020000);
          const int so_ = hw * 2 * 16 * KB + fb * 128, vo_ = (lane >> 2) * KB + (lane & 3) * 16;
          lds_wait(L, SY_X0, 4);
          __builtin_amdgcn_s_sleep(WO_SLEEP);
          sfor<16>([&](auto I_) AI { constexpr int i = decltype(I_)::v; dma1p<((i >> 3) * 8 + ((i & 7) >> 2)) * 16 * KB + ((i >> 1) & 1) * 256 * KB + (i & 1) * 64, (i < 8 ? BUF_D : BUF_A) + (i & 7) * KB>(ro, so_, lb, vo_); });
        }

        BAR();
        STAMP(sb + 1);
      }

      STAMP(sb + 2);
      BAR();

      {
        const int tq = 3 - hw;
        const v4u Qb = ldsr(L + L_QB + tq * KB + l16);
        FILL_DEF();
        auto unit = [&]() AI {
          if (layer == 0 && b < 168) {
            const int row = lane >> 3, pc = lane & 7, r = b - 112, ufb = r / 7;
            const bool gu = b < 112;
            lptr d0 = L + BUF_D + row * 64 + (gu ? (pc >> 1) * 8 * KB + 2 * hw * KB + (pc & 1) * 16 : ((hw >> 1) * 2 + (pc >> 2)) * 8 * KB + (hw & 1) * 4 * KB + (pc & 3) * 16);
            lptr d1 = d0 + (gu ? 32 : 2 * KB);
            if (UNIT_W == 0) {
#pragma unroll
              for (int i = 0; i < 8; ++i) ldsw((i < 4 ? d0 : d1) + ((i & 3) >> 1) * KB + (i & 1) * 512, uw[i]);
              lds_add(L, SY_U2, lane);
            }
            lds_wait(L, SY_U2, 4);
            const int ku0 = gu ? 8 * (b & 3) : 8 * (2 * (r % 7) + (hw >> 1));
            const int ubase = gu ? WS_WC + (((b >> 2) * 4 + hw) * 33 + ku0) * KB : WS_WD + (ufb * 224 + (hw & 1)) * KB;
            float c0 = 0.f, c1 = 0.f;
            int j = ku0 / 7, t = ku0 % 7;
            lptr tp = L + BUF_D + lb + tr_off(lane);
#pragma unroll
            for (int u = 0; u < 8; ++u) {
              const v4u f = trr(tp + u * KB);
              gst(rs, f, l16, ubase + (gu ? u : (t * 8 + ((j - ufb) & 7)) * 4 + (j >> 3) * 2) * KB);
              if (++t == 7) { t = 0; ++j; }
              if (u & 1) c1 = sum8(f, c1); else c0 = sum8(f, c0);
            }
            if (gu) { const unsigned wd = hilo(xhalf_sum(c0 + c1)); if (lane < 32) gst2(rs, v2u{wd, wd}, l16, ubase + (32 - ku0) * KB + ((b & 3) >> 1) * 512 + (b & 1) * 8); }
          }
        };
        auto unit_w = [&]() AI {
          if (UNIT_W == 1 && layer == 0 && b < 168) {
            const int row = lane >> 3, pc = lane & 7;
            const bool gu = b < 112;
            lptr d0 = L + BUF_D + row * 64 + (gu ? (pc >> 1) * 8 * KB + 2 * hw * KB + (pc & 1) * 16 : ((hw >> 1) * 2 + (pc >> 2)) * 8 * KB + (hw & 1) * 4 * KB + (pc & 3) * 16);
            lptr d1 = d0 + (gu ? 32 : 2 * KB);
#pragma unroll
            for (int i = 0; i < 8; ++i) ldsw((i < 4 ? d0 : d1) + ((i & 3) >> 1) * KB + (i & 1) * 512, uw[i]);
            lds_add(L, SY_U2, lane);
          }
        };
        if (hw == 3 && g >= 2) {
          __builtin_amdgcn_s_sleep(PD_KV + 4); poll(flag_ptr(ws, fl + F_KV, me - 32), lane == 0, epoch, dead);
          const int s2 = act + A_KV + (me - 32) * 16 * KB;
          dma4<0, BUF_B>(rs, s2, 0, l16); dma4<4 * KB, BUF_B + 4 * KB>(rs, s2, 0, l16);
          dma4<8 * KB, BUF_B + 8 * KB>(rs, s2, 0, l16); dma4<12 * KB, BUF_B + 12 * KB>(rs, s2, 0, l16);
        }
        const v4u of = attn_item<1>(L, Qb, lane, tq, g, unit_w, [&]() AI {
          if (hw == 3) WAITV(0);
          unit();
          STAMP(sb + 4);
          BARL();
          if (layer == 1) STAGE(d6, wdn, 6 * 32 * KB); else ZERO(d6);
          fill0();
        }, layer == 0, fill1);
        gst(rs, of, l16, act + A_OT + ((4 * g + tq) * 32 + 2 * m + 1) * KB);
        STAMP(sb + 6);
        WAITV(0);

        BAR();
      }
      if (layer == 0) {
        if (hw == 0) {
          if (lane == 0) set_flag(ws, F_P2, me, epoch);
          int qq;
          if (lane < 7) qq = 112 + 7 * fb + lane;
          else { int n = (7 * m) / 4 + ((lane - 7) >> 2); n = n > 27 ? 27 : n; qq = 4 * n + ((lane - 7) & 3); }
          poll(flag_ptr(ws, F_P2, lane < 19 ? wg_index(qq) : 0), lane < 19, epoch, dead);
          lds_set(L, SY_U, 1);
        } else lds_wait(L, SY_U, 1);
        DMA(true, wc, 0, BUF_C);
      }
      if (hw == 1) ot<OT_O, A_OT>(rs, act + (4 * g * 32 + 2 * m) * KB, (lane >> 4) * 32 * KB + (lane & 15) * 128);
      DMA(true, wc, 1 * CH, BUF_B); if (layer == 1) STAGE(d3, wdn, 3 * 32 * KB); else ZERO(d3);
      STAMP(sb + 7);
      lds_wait(L, SY_GO, 3 * layer + 1);
      __builtin_amdgcn_s_sleep(14);
      WAITV(0);
      lds_add(L, SY_HWL, lane);
      STAGE(d5, wdn, 5 * 32 * KB); STAGE(d4, wdn, 4 * 32 * KB);
      STAMP(sb + 9);
      lds_wait(L, SY_DR, 8 * layer + 4);
      const int vx = (lane >> 5) * 32 * KB + (lane & 31) * 128, vs = lane < 8 ? (lane >> 2) * 4096 + (lane & 3) * 128 : WS_BYTES;
      const int xoo = ((4 * g + 2 * th) * 32 + 4 * fb) * KB, soo = (4 * g + 2 * th) * 4096 + fb * 512;
      if (hw == 1) ot<OT_X, A_X1T>(rs, act + xoo, vx);
      if (hw == 2) ot<OT_X, A_ST1>(rs, act + soo, vs);
      DMA(true, wc, 2 * CH, BUF_D);
      STAMP(sb + 11);
      WAITV(0); BAR();
      DMA(true, wc, 3 * CH, BUF_A); STAGE(d2, wdn, 2 * 32 * KB);
      BAR();
      STAMP(sb + 13);
      DMA(true, wc, 4 * CH, BUF_C);
      WAITV(16); BAR();
      DMA(true, wc, 5 * CH, BUF_B); if (layer == 0) STAGE(d6, wdn, 6 * 32 * KB);
      const int hoo = act + A_HT + ((4 * g + 2 * (hw - 2)) * 112 + 7 * m) * KB;
      const int vh = (lane & 24) ? WS_BYTES : (lane >> 5) * 112 * KB + (lane & 7) * 128;
      const int hco = WS_FLAGS + ((fl + F_H) * 256 + g * 16) * FLAG_STRIDE, vc = lane < 16 ? lane * FLAG_STRIDE : WS_BYTES;

      WAITV(8); BAR();

      DMA(true, wc, 6 * CH, BUF_D); if (layer == 0) STAGE(d3, wdn, 3 * 32 * KB);
      WAITV(8); BAR();
      if (PUBMODE == 0) HFLAG(3);
      DMA(false, wdn, 0 * 32 * KB, BUF_A);
      WAITV(8); BAR();
      if (PUBMODE == 0) HFLAG(4);
      HISSUE(L2);
      DMA(false, wdn, 1 * 32 * KB, BUF_C);
      HGATE(L2, 8);
      STAMP(sb + 14);
      BAR();
      WAITV(0); PIN(d5); PIN(d4); PIN(d2); PIN(d6); PIN(d3);
      if (PUBMODE == 0) HFLAG(5);
      if (L3 != L2) { HISSUE(L3); HGATE(L3, 0); }
      BAR();
      PUT(d2, BUF_B);

      if (L3 != 4) HISSUE(4);
      BAR();
      if (H6S >= 8 - PDD) HFLAG(6);
      PUT(d3, BUF_D);
      if (L3 != 4) HGATE(4, 0);
      if (HI_EARLY == 1) HISSUE(5);
      STAMP(sb + 15);
      BARL8();
      if (HI_EARLY == 0) HISSUE(5);
      PUT(d4, BUF_A);

      if (HI_EARLY == 2 && hw == 0) { __builtin_amdgcn_s_sleep(HD5); HISSUE(5); }
      HGATE(5, 0);
      if (HI_EARLY == 1) HISSUE(6);
      BARL8();
      if (H7S >= 24 - PDD) HFLAG(7);
      if (HI_EARLY == 0) HISSUE(6);
      PUT(d5, BUF_C);
      if (HI_EARLY == 2 && hw == 0) { __builtin_amdgcn_s_sleep(HD6); HISSUE(6); }
      HGATE(6, 0);
      if (HI_EARLY == 1) HISSUE(7);
      BARL8();
      if (HI_EARLY == 0) HISSUE(7);
      PUT(d6, BUF_B);

      if (HI_EARLY == 2 && hw == 0) { __builtin_amdgcn_s_sleep(HD7); HISSUE(7); }
      HGATE(7, 0);
      STAMP(sb + 16);
      BARL8();
      BARL();
      if (layer == 0) {
        PUT(a0, BUF_A);
        lds_wait4(L, 2);
        PUT(a1, BUF_B);
        DMA_Q();
        lds_wait(L, SY_DR, 8);
        if (hw == 1) ot<OT_X, A_LAYER + A_XT>(rs, act + xoo, vx);
        if (hw == 2) ot<OT_X, A_LAYER + A_ST0>(rs, act + soo, vs);
      }
      ZERO(uw);
    }
  }
}
}

extern "C" void kernel_launch(void* const* d_in, const int* in_sizes, int n_in, void* d_out, int out_size, void* d_ws, size_t ws_size,
                              hipStream_t stream) {
  Params p;
  p.x = (const u16*)d_in[0]; p.wqkv = (const u16*)d_in[1]; p.wo = (const u16*)d_in[2];
  p.wg = (const u16*)d_in[4]; p.wu = (const u16*)d_in[5]; p.wd = (const u16*)d_in[6];
  p.out = (u16*)d_out; p.ws = (unsigned char*)d_ws;
  (void)hipFuncSetAttribute((const void*)fused_layers, hipFuncAttributeMaxDynamicSharedMemorySize, LDS_BYTES);
  hipLaunchKernelGGL(fused_layers, dim3(NWG), dim3(NTHR), LDS_BYTES, stream, p);
}
```

```cpp
#define QAT 0
#define PGSH 20
#define ULOAD 0
#define P2HOOK 1
#define PD_P2 16
#define WARMC 0
#define WARM0 0
#define P4AT 1
#define P5AT 1
#define P6AT 0
#define P7AT 1
#define HI_EARLY 1
#define L2 (PUBMODE == 0 ? 3 : 2)
#define L3 (PUBMODE == 0 ? 3 : 4)
#define UNIT_W 1
#define HPRIO 3
#define UNIT_AT 0
#define POLL_FIRST 0
#define START_ORDER 0
#define QSLEEP 8
#include <hip/hip_runtime.h>
#include <hip/hip_bf16.h>
#include <utility>

#define POLL_SLEEP 0
#define UNIT_EARLY 1
#define EARLY_FREE 0
#define HPRE 8
#define HPUBS 10
#define E6 4
#define H7S 21
#define XW0 8
#define XW1 16
#define H6S 5
#define PUBMODE 0
#define PDD 3
#define PD_O 12
#define PD_X1 15
#define PD_X2 14
#define PD_KV 6
#define HSLEEP 2
#define POLLK 0
#define HPOLLBITS "sc1"
#define HD5 0
#define HD6 0
#define HD7 0
#define WO_EARLY 1
#define WO_SLEEP 0
#define KVPUB 1
#define CWIN 12
#define SL1 8
#define UE 2
#define HWSLP 0
#define RO 0
#define RX 0
#define RH 1
#define RF 0
#define FLAGW 0
#define CWSLP 0
#define CWSLP0 0
#define P2LATE 0
#define QS 16
#define IMG 0
#define KVWIN 16
#define PROF 0

extern "C" __device__ float llvm_fmul_legacy(float, float) __asm("llvm.amdgcn.fmul.legacy");
namespace {
typedef unsigned short u16;
typedef unsigned v4u __attribute__((ext_vector_type(4)));
typedef unsigned v2u __attribute__((ext_vector_type(2)));
typedef short v4s __attribute__((ext_vector_type(4)));
typedef float f32x4 __attribute__((ext_vector_type(4)));
typedef float f32x16 __attribute__((ext_vector_type(16)));
typedef __bf16 v2bf __attribute__((ext_vector_type(2)));
typedef __bf16 v8bf __attribute__((ext_vector_type(8)));
#define LAS __attribute__((address_space(3)))
#define GAS __attribute__((address_space(1)))
#define DI __device__ __forceinline__
typedef GAS unsigned gu32;
typedef LAS unsigned char* lptr;
#define RLX_AGENT __ATOMIC_RELAXED, __HIP_MEMORY_SCOPE_AGENT

constexpr int NWG = 256, NTHR = 512;
constexpr int KB = 1024;
constexpr int FLAG_STRIDE = 256;
enum { F_P2 = 0, F_KV = 1  , F_O = 3, F_X1 = 4, F_X2 = 5, F_H = 6  , F_P3 = 21, F_KINDS = 22 };
constexpr int WS_EPOCH = 0;
constexpr int WS_FLAGS = 4 * KB;
constexpr int WS_WC = WS_FLAGS + F_KINDS * 256 * FLAG_STRIDE;
constexpr int WS_WD = WS_WC + 16 * 231 * KB;
constexpr int WS_IQ = WS_WD + 8 * 224 * KB;
constexpr int WS_IO = WS_IQ + 16 * 32 * KB;
constexpr int WS_ACT = WS_IO + 8 * 64 * KB;
constexpr int A_OT = WS_ACT, O_LAYER = RO ? 0 : 2048 * KB, A_XT = A_OT + 4096 * KB, X_STEP = RX ? 0 : 2048 * KB, A_ST = A_XT + 6144 * KB, S_STEP = RX ? 0 : 256 * KB,
              A_HT = A_ST + 768 * KB, H_LAYER = RH ? 0 : 64 * 112 * KB, A_KV = A_HT + 2 * 64 * 112 * KB, KV_LAYER = 4096 * KB, F_LAYER = RF ? 0 : 9;
constexpr int WS_DBG = A_KV + 2 * KV_LAYER;
constexpr int WS_BYTES = WS_DBG + 256 * KB * 2;
constexpr int CH = 33 * KB;
constexpr int BUF_A = 0, BUF_B = CH, BUF_C = 2 * CH, BUF_D = 3 * CH, SP = 4 * CH;
constexpr int L_KV = SP;
constexpr int L_QB = SP + 16 * KB;
constexpr int L_ONES = SP + 20 * KB;
constexpr int L_SY = L_ONES + 64;
constexpr int SY_GO = 0, SY_DR = 4, SY_HWL = 8, SY_KV = 12, SY_EX = 16, SY_U = 32, SY_X0 = 36, SY_U2 = 40, SY_H = 44, SY_EP = 48, SY_P2 = 52, SY_Q = 56, SY_I = 60;
constexpr int L_RS = L_ONES + 128;
constexpr int L_POLL = L_ONES + 640;
constexpr int L_PF = L_ONES + 1088;
constexpr int L_X = SP + 22 * KB;
constexpr int L_DBG = SP + 25 * KB;
constexpr int LDS_BYTES = SP + (PROF == 1 ? 27 : 25) * KB;

constexpr int CODE_BYTES = 48 * KB;
#define SUM_LIMIT 1.8446744e19f
constexpr float NEG_BIG = -1e30f;
constexpr float LOG2E = 1.4426950408889634f;
constexpr int AUX_SC1 = 16;

struct Params {
  const u16 *x, *wqkv, *wo, *wg, *wu, *wd;
  u16* out;
  unsigned char* ws;
};

template <int I> struct IC { static constexpr int v = I; };
template <class F, int... Is> DI void sfor_impl(F& f, std::integer_sequence<int, Is...>) { (f(IC<Is>{}), ...); }
template <int N, class F> DI void sfor(F f) { sfor_impl(f, std::make_integer_sequence<int, N>{}); }
#define AI __attribute__((always_inline))
DI f32x16 mfma(v4u a, v4u b, f32x16 c) {
  return __builtin_amdgcn_mfma_f32_32x32x16_bf16(__builtin_bit_cast(v8bf, a), __builtin_bit_cast(v8bf, b), c, 0, 0, 0);
}
DI unsigned pk(float a, float b) { v2bf v = {(__bf16)a, (__bf16)b}; return __builtin_bit_cast(unsigned, v); }
DI float fm(float a, float b) { return llvm_fmul_legacy(a, b); }
DI float bflo(unsigned w) { return __uint_as_float(w << 16); }
DI float bfhi(unsigned w) { return __uint_as_float(w & 0xffff0000u); }
DI float xhalf_sum(float v) { auto r = __builtin_amdgcn_permlane32_swap(__float_as_uint(v), __float_as_uint(v), false, false); return __uint_as_float(r[0]) + __uint_as_float(r[1]); }
DI float xhalf_max(float v) { auto r = __builtin_amdgcn_permlane32_swap(__float_as_uint(v), __float_as_uint(v), false, false); return __builtin_fmaxf(__uint_as_float(r[0]), __uint_as_float(r[1])); }
DI v4u ldsr(lptr p) { return *(LAS v4u*)p; }
DI void ldsw(lptr p, v4u v) { *(LAS v4u*)p = v; }
typedef __amdgpu_buffer_rsrc_t rsrc_t;
DI v4u gld(rsrc_t rs, int voff, int soff) { return __builtin_amdgcn_raw_buffer_load_b128(rs, voff, soff, AUX_SC1); }
DI v4u gldf(rsrc_t rs, int l16, int soff, int i) { return __builtin_amdgcn_raw_buffer_load_b128(rs, l16 + (i & 3) * KB, soff + (i >> 2) * 4 * KB, AUX_SC1); }
DI v4u gld_stats(rsrc_t rs, int voff, int soff) {
  const v2u a = __builtin_amdgcn_raw_buffer_load_b64(rs, voff, soff, AUX_SC1), b = __builtin_amdgcn_raw_buffer_load_b64(rs, voff + 256, soff, AUX_SC1);
  return v4u{a.x, a.y, b.x, b.y};
}
DI int opv(int x) { asm volatile("" : "+v"(x)); return x; }
DI int ops(int x) { asm volatile("" : "+s"(x)); return x; }
DI void gst(rsrc_t rs, v4u v, int voff, int soff) {
  __builtin_amdgcn_raw_buffer_store_b128(v, rs, voff, soff, AUX_SC1);
  asm volatile("s_nop 1" :: "v"(v) : "memory");
}
DI void gst2(rsrc_t rs, v2u v, int voff, int soff) { __builtin_amdgcn_raw_buffer_store_b64(v, rs, voff, soff, AUX_SC1); }
#define SCHED() __builtin_amdgcn_sched_barrier(0)
#define PINA(a) asm volatile("" : "+v"(a))
#define HOLD(a) do {} while (0)
#define BAR() asm volatile("s_barrier" ::: "memory")
#define BARL() asm volatile("s_waitcnt lgkmcnt(0)\n\ts_barrier" ::: "memory")
#define WAITV(n) asm volatile("s_waitcnt vmcnt(" #n ")" ::: "memory")

template <int G, int LO> DI void dma4(rsrc_t rs, int sb, int lb, int voff) {
  unsigned keep, so;
  asm volatile("s_mov_b32 %0, m0\n\ts_add_u32 m0, %5, %7\n\ts_add_u32 %1, %4, %6\n\ts_nop 2\n\t"
               "buffer_load_dwordx4 %2, %3, %1 offen sc1 lds\n\tbuffer_load_dwordx4 %2, %3, %1 offen offset:1024 sc1 lds\n\t"
               "buffer_load_dwordx4 %2, %3, %1 offen offset:2048 sc1 lds\n\tbuffer_load_dwordx4 %2, %3, %1 offen offset:3072 sc1 lds\n\t"
               "s_mov_b32 m0, %0" : "=&s"(keep), "=&s"(so) : "v"(voff), "s"(rs), "s"(sb), "s"(lb), "i"(G), "i"(LO) : "memory", "scc");
}
template <int G, int LO> DI void dma1(rsrc_t rs, int sb, int lb, int voff) {
  unsigned keep, so;
  asm volatile("s_mov_b32 %0, m0\n\ts_add_u32 m0, %5, %7\n\ts_add_u32 %1, %4, %6\n\ts_nop 2\n\t"
               "buffer_load_dwordx4 %2, %3, %1 offen sc1 lds\n\t"
               "s_mov_b32 m0, %0" : "=&s"(keep), "=&s"(so) : "v"(voff), "s"(rs), "s"(sb), "s"(lb), "i"(G), "i"(LO) : "memory", "scc");
}
template <int G, int LO> DI void dma1p(rsrc_t rs, int sb, int lb, int voff) {
  unsigned keep, so;
  asm volatile("s_mov_b32 %0, m0\n\ts_add_u32 m0, %5, %7\n\ts_add_u32 %1, %4, %6\n\ts_nop 2\n\t"
               "buffer_load_dwordx4 %2, %3, %1 offen lds\n\t"
               "s_mov_b32 m0, %0" : "=&s"(keep), "=&s"(so) : "v"(voff), "s"(rs), "s"(sb), "s"(lb), "i"(G), "i"(LO) : "memory", "scc");
}
DI int tr_off(int lane) { const int gi = lane >> 4, q4 = (lane & 15) >> 2, pp = lane & 3; return (8 * (gi >> 1) + q4) * 64 + (gi & 1) * 32 + (((pp & 1) << 1) | (pp >> 1)) * 8; }
DI v4u trr(lptr p) {
  const v2u lo = __builtin_bit_cast(v2u, __builtin_amdgcn_ds_read_tr16_b64_v4i16((LAS v4s*)p));
  const v2u hi = __builtin_bit_cast(v2u, __builtin_amdgcn_ds_read_tr16_b64_v4i16((LAS v4s*)(p + 256)));
  return v4u{lo.x, lo.y, hi.x, hi.y};
}
DI float dot2(unsigned a, unsigned b, float c) { return __builtin_amdgcn_fdot2_f32_bf16(__builtin_bit_cast(v2bf, a), __builtin_bit_cast(v2bf, b), c, false); }
DI float sum8(v4u f, float c) {
#pragma unroll
  for (int e = 0; e < 4; ++e) { const unsigned fe = f[e]; c = dot2(fe, 0x3f803f80u, c); }
  return c;
}
DI unsigned hilo(float c) { return pk(c, c - bflo(pk(c, c))); }
template <int G> DI void touch(rsrc_t rs, int sb, int voff) {
  unsigned keep, so;
  asm volatile("s_mov_b32 %0, m0\n\ts_mov_b32 m0, %6\n\ts_add_u32 %1, %4, %5\n\ts_nop 2\n\t"
               "buffer_load_dword %2, %3, %1 offen sc1 lds\n\t"
               "s_mov_b32 m0, %0" : "=&s"(keep), "=&s"(so) : "v"(voff), "s"(rs), "s"(sb), "i"(G), "i"(L_PF) : "memory", "scc");
}
#define OTM 0
enum { OT_KV = 1, OT_O = 2, OT_X = 4, OT_H = 8 };
template <int K, int G> DI void ot(rsrc_t rs, int sb, int voff) { if (OTM & K) touch<G>(rs, sb, voff); }
template <bool EXTRA, int G, int LBUF> DI void dma_chunk(rsrc_t rs, int sb, int lb, int voff) {
  dma4<G, LBUF>(rs, sb, lb, voff);
  dma4<G + 4 * KB, LBUF + 4 * KB>(rs, sb, lb, voff);
  if (EXTRA && lb == 0) dma1<G + 32 * KB, LBUF + 32 * KB>(rs, sb, lb, voff);
}

#define MAPV 1
__host__ __device__ inline int wg_index(int b) {
  if (MAPV == 0) return ((b & 7) * 2 + (b >> 7)) * 16 + ((b >> 3) & 15);
  return (4 * ((b & 7) >> 1) + (b >> 6)) * 16 + 8 * (b & 1) + ((b >> 3) & 7);
}
DI gu32* flag_ptr(unsigned char* ws, int kind, int idx) { return (gu32*)(ws + WS_FLAGS + (size_t)(kind * 256 + idx) * FLAG_STRIDE); }
DI void set_flag(unsigned char* ws, int kind, int idx, unsigned epoch) { __hip_atomic_store(flag_ptr(ws, kind, idx), epoch, RLX_AGENT); }
DI void put_flag(rsrc_t rs, unsigned char* ws, int kind, int idx, unsigned v, int lane) {
  if (FLAGW) { if (lane < 32) __builtin_amdgcn_raw_buffer_store_b32(v, rs, lane * 4, WS_FLAGS + (kind * 256 + idx) * FLAG_STRIDE, AUX_SC1); }
  else if (lane == 0) set_flag(ws, kind, idx, v);
}
DI void poll(gu32* f, bool active, unsigned epoch, bool& dead) {
  if (!dead) {
    const unsigned long long t0 = __builtin_amdgcn_s_memrealtime();
#pragma unroll 1
    for (unsigned spins = 1;; ++spins) {
      unsigned v = epoch;
      if (active) v = __hip_atomic_load(f, RLX_AGENT);
      if (__all(v == epoch)) break;
      if ((spins & 63u) == 0u && __builtin_amdgcn_s_memrealtime() - t0 > 2000000ull) { dead = true; break; }
      active = v != epoch;
      if (POLL_SLEEP) __builtin_amdgcn_s_sleep(POLL_SLEEP);
    }
  }
  __builtin_amdgcn_fence(__ATOMIC_ACQUIRE, "wavefront");
}

template <int NT> DI void hpoll_issue(rsrc_t rs, int so, int lane) {
  unsigned keep, o;
  if (lane < 16)
    asm volatile("s_mov_b32 %0, m0\n\ts_mov_b32 m0, %5\n\ts_add_u32 %1, %4, %6\n\ts_nop 2\n\tbuffer_load_dword %2, %3, %1 offen sc1 lds\n\ts_mov_b32 m0, %0"
                 : "=&s"(keep), "=&s"(o) : "v"(lane * FLAG_STRIDE), "s"(rs), "s"(so), "i"(L_POLL + (NT - 2) * 64), "i"((NT - 2) * 256 * FLAG_STRIDE) : "memory", "scc");
}
template <int NT> DI void hgate(rsrc_t rs, lptr L, int so, int lane, unsigned tag, bool& dead) {
  if (!dead) {
    const unsigned long long t0 = __builtin_amdgcn_s_memrealtime();
#pragma unroll 1
    for (unsigned spins = 1;; ++spins) {
      if (__all(*(volatile LAS unsigned*)(L + L_POLL + (NT - 2) * 64 + (lane & 15) * 4) == tag + NT)) break;
      if ((spins & 63u) == 0u && __builtin_amdgcn_s_memrealtime() - t0 > 2000000ull) { dead = true; break; }
      if (HSLEEP) __builtin_amdgcn_s_sleep(HSLEEP);
      hpoll_issue<NT>(rs, so, lane);
      WAITV(0);
    }
  }
  asm volatile("" ::: "memory");
}
DI void lds_set(lptr L, int word, unsigned v) { asm volatile("" ::: "memory"); *(volatile LAS unsigned*)(L + L_SY + word) = v; asm volatile("" ::: "memory"); }
DI void lds_add(lptr L, int word, int lane) {
  asm volatile("" ::: "memory");
  if (lane == 0) __hip_atomic_fetch_add((LAS unsigned*)(L + L_SY + word), 1u, __ATOMIC_RELAXED, __HIP_MEMORY_SCOPE_WORKGROUP);
  asm volatile("" ::: "memory");
}
DI bool lds_last(lptr L, int word, int lane) {
  asm volatile("" ::: "memory");
  unsigned o = 0;
  if (lane == 0) o = __hip_atomic_fetch_add((LAS unsigned*)(L + L_SY + word), 1u, __ATOMIC_RELAXED, __HIP_MEMORY_SCOPE_WORKGROUP);
  asm volatile("" ::: "memory");
  return (__builtin_amdgcn_readfirstlane(o) & 3u) == 3u;
}
template <int SLP = 0> DI void lds_wait(lptr L, int word, unsigned v) {
#pragma unroll 1
  for (unsigned n = 0; n < (1u << 22); ++n) {
    if ((unsigned)__builtin_amdgcn_readfirstlane(*(volatile LAS unsigned*)(L + L_SY + word)) >= v) break;
    if (SLP) __builtin_amdgcn_s_sleep(SLP);
  }
  asm volatile("" ::: "memory");
}
DI void lds_wait4(lptr L, unsigned v) {
#pragma unroll 1
  for (unsigned n = 0; n < (1u << 22); ++n) {
    const v4u e = *(volatile LAS v4u*)(L + L_SY + SY_EX);
    const unsigned a = e.x < e.y ? e.x : e.y, b = e.z < e.w ? e.z : e.w;
    if ((unsigned)__builtin_amdgcn_readfirstlane(a < b ? a : b) >= v) break;
    __builtin_amdgcn_s_sleep(1);
  }
  asm volatile("" ::: "memory");
}

#define STAMP(i) do {} while (0)

template <int N> DI void merge_halves(float pivot, float s, float q, float& mean, float& m2) {
  const float mu = __builtin_fmaf(s, 1.f / N, pivot), d2 = __builtin_fmaf(-s * (1.f / N), s, q);
  const auto r = __builtin_amdgcn_permlane32_swap(__float_as_uint(mu), __float_as_uint(mu), false, false);
  const float ma = __uint_as_float(r[0]), mb = __uint_as_float(r[1]), dm = ma - mb;
  mean = 0.5f * (ma + mb);
  m2 = __builtin_fmaf(dm * (0.5f * N), dm, xhalf_sum(d2));
}
DI float rstd_of(float m2) { return __builtin_amdgcn_rsqf(__builtin_fmaxf(m2, 0.f) * (1.f / 512.f) + 1e-5f); }
DI void finish_stats(const v4u (&st)[4], float& mean, float& rstd) {
  const float pivot = __uint_as_float(st[0].x);
  float s = 0.f, q = 0.f, m2 = 0.f;
#pragma unroll
  for (int i = 0; i < 4; ++i) {
    const float d0 = __uint_as_float(st[i].x) - pivot, d1 = __uint_as_float(st[i].z) - pivot;
    s += d0 + d1; q = __builtin_fmaf(d0, d0, __builtin_fmaf(d1, d1, q)); m2 += __uint_as_float(st[i].y) + __uint_as_float(st[i].w);
  }
  float between;
  merge_halves<8>(pivot, s, q, mean, between);
  rstd = rstd_of(__builtin_fmaf(between, 32.f, xhalf_sum(m2)));
}
DI v4u mean_frag(float mean) {
  const float nm = -mean;
  const unsigned hh = pk(nm, nm);
  const float lo = nm - bflo(hh);
  const unsigned ll = pk(lo, lo);
  return v4u{hh, ll, hh, ll};
}
DI void store_stats(rsrc_t rs, const float (&v)[16], int lane, int soff) {
  float s0 = 0.f, s1 = v[1] - v[0], q0 = 0.f, q1 = s1 * s1;
#pragma unroll
  for (int i = 2; i < 16; i += 2) {
    const float d0 = v[i] - v[0], d1 = v[i + 1] - v[0];
    s0 += d0; q0 = __builtin_fmaf(d0, d0, q0); s1 += d1; q1 = __builtin_fmaf(d1, d1, q1);
  }
  float mean, m2;
  merge_halves<16>(v[0], s0 + s1, q0 + q1, mean, m2);
  if (lane < 32) gst2(rs, v2u{__float_as_uint(mean), __float_as_uint(m2)}, lane * 8, soff);
}

struct Attn { f32x16 O; float m; };
DI float max16(const f32x16& s, float m) {
#pragma unroll
  for (int i = 0; i < 16; i += 2) m = __builtin_fmaxf(__builtin_fmaxf(m, s[i]), s[i + 1]);
  return m;
}
DI f32x16 pv(const f32x16& s, v4u v0, v4u v1, f32x16 O) {
  float p[16];
#pragma unroll
  for (int i = 0; i < 16; ++i) p[i] = __builtin_amdgcn_exp2f(s[i]);
  const v4u p0 = {pk(p[0], p[1]), pk(p[2], p[3]), pk(p[4], p[5]), pk(p[6], p[7])};
  const v4u p1 = {pk(p[8], p[9]), pk(p[10], p[11]), pk(p[12], p[13]), pk(p[14], p[15])};
  O = mfma(v0, p0, O);
  O = mfma(v1, p1, O);
  return O;
}
template <bool FIRST>
DI void attn_chunk(Attn& A, v4u Q, lptr kp, lptr vp, int vstride, int t0, int t1, int tm, const f32x16& mask) {
  const f32x16 zero = {};
  float mx = FIRST ? NEG_BIG : A.m;
  f32x16 sm = zero;
  if (tm >= 0) { sm = mfma(ldsr(kp + tm * 4096), Q, mask); mx = max16(sm, mx); }
  for (int t = t0; t < t1; ++t) { const f32x16 s = mfma(ldsr(kp + t * 4096), Q, zero); mx = max16(s, mx); }
  mx = xhalf_max(mx);
  if (FIRST) A.O = zero;
  else { const float al = __builtin_amdgcn_exp2f(A.m - mx); A.O *= al; }
  A.m = mx;
  if (tm >= 0) { A.O = pv(sm - mx, ldsr(vp + tm * vstride), ldsr(vp + tm * vstride + 1024), A.O); }
  f32x16 nm;
#pragma unroll
  for (int i = 0; i < 16; ++i) nm[i] = -mx;
  for (int t = t0; t < t1; ++t) {
    const f32x16 s = mfma(ldsr(kp + t * 4096), Q, nm);
    A.O = pv(s, ldsr(vp + t * vstride), ldsr(vp + t * vstride + 1024), A.O);
  }
}
DI float opf(float x) { asm volatile("" : "+v"(x)); return x; }
struct Fast { f32x16 O, nm; };
DI void fast_diag(Fast& F, v4u Q, lptr kp, lptr vp, const f32x16& mask) {
  const v4u k = ldsr(kp), v0 = ldsr(vp), v1 = ldsr(vp + 1024);
  const f32x16 s = mfma(k, Q, mask);
  float m0 = __builtin_fmaxf(s[0], s[1]), m1 = __builtin_fmaxf(s[2], s[3]);
#pragma unroll
  for (int i = 4; i < 16; i += 4) { m0 = __builtin_fmaxf(__builtin_fmaxf(m0, s[i]), s[i + 1]); m1 = __builtin_fmaxf(__builtin_fmaxf(m1, s[i + 2]), s[i + 3]); }
  const float mx = xhalf_max(__builtin_fmaxf(m0, m1));
  f32x16 d;
#pragma unroll
  for (int i = 0; i < 16; ++i) { d[i] = opf(s[i] - mx); F.nm[i] = -mx; }
  F.O = pv(d, v0, v1, f32x16{});
}
DI v4u exp8(const f32x16& s, int o) {
  float p[8];
#pragma unroll
  for (int i = 0; i < 8; ++i) p[i] = __builtin_amdgcn_exp2f(s[o + i]);
  return v4u{pk(p[0], p[1]), pk(p[2], p[3]), pk(p[4], p[5]), pk(p[6], p[7])};
}
template <class Hook>
DI void fast_run(Fast& F, v4u Q, lptr kp, lptr vp, int vstride, int t0, int t1, const f32x16& c0, bool hk, Hook hook) {
  lptr kq = kp + t0 * 4096, vq = vp + t0 * vstride;
  v4u k = ldsr(kq), va0 = ldsr(vq), va1 = ldsr(vq + 1024), vb0, vb1;
  f32x16 sa = mfma(k, Q, c0), sb;
  k = ldsr(kq + 4096);
  auto half = [&](const f32x16& sc, f32x16& sn, v4u vc0, v4u vc1, v4u& vn0, v4u& vn1, lptr kn) AI {
    v4u p0 = exp8(sc, 0);
    PINA(p0);
    SCHED();
    sn = mfma(k, Q, F.nm); PINA(sn); SCHED();
    const v4u ko = k;
    vq += vstride; vn0 = ldsr(vq); vn1 = ldsr(vq + 1024); k = ldsr(kn);
    HOLD(ko); SCHED();
    float e[4];
#pragma unroll
    for (int i = 0; i < 4; ++i) e[i] = __builtin_amdgcn_exp2f(sc[8 + i]);
    asm volatile("" : "+v"(e[0]), "+v"(e[1]), "+v"(e[2]), "+v"(e[3]));
    SCHED();
    F.O = mfma(vc0, p0, F.O); PINA(F.O); SCHED();
    const v4u p1 = {pk(e[0], e[1]), pk(e[2], e[3]), pk(__builtin_amdgcn_exp2f(sc[12]), __builtin_amdgcn_exp2f(sc[13])), pk(__builtin_amdgcn_exp2f(sc[14]), __builtin_amdgcn_exp2f(sc[15]))};
    SCHED();
    F.O = mfma(vc1, p1, F.O); PINA(F.O); SCHED();
  };
  int n = t1 - t0;
#pragma unroll 1
  for (; n >= 2; n -= 2) {
    half(sa, sb, va0, va1, vb0, vb1, kq + 2 * 4096);
    kq += 2 * 4096;
    half(sb, sa, vb0, vb1, va0, va1, kq + 4096);
    if (hk) { hook(); hk = false; }
  }
  if (n) F.O = pv(sa, va0, va1, F.O);
  if (hk) hook();
}
DI void make_mask(f32x16& mk, int lane, bool old) {
  const int d = (lane & 31) - 4 * (lane >> 5);
#pragma unroll
  for (int i = 0; i < 16; ++i) { const bool le = ((i & 3) + 8 * (i >> 2)) <= d; mk[i] = (le != old) ? 0.f : NEG_BIG; }
}
template <int HD> DI v4u attn_out(const f32x16& O) {
  const float r = __builtin_amdgcn_rcpf(O[HD ? 0 : 8]);
  const int o = HD ? 8 : 0;
  return v4u{pk(O[o] * r, O[o + 1] * r), pk(O[o + 2] * r, O[o + 3] * r), pk(O[o + 4] * r, O[o + 5] * r), pk(O[o + 6] * r, O[o + 7] * r)};
}
template <int HD, class Diag, class Mid, class Hook>
DI v4u attn_item(lptr L, v4u Q, int lane, int tq, int g, Diag diag, Mid mid, bool hk, Hook hook) {
  const int l16 = lane * 16;
  const bool vl = ((lane >> 4) & 1) == HD;
  const int vstride = vl ? 4096 : 0;
  lptr kl = L + L_KV + HD * KB + l16, vloc = vl ? L + L_KV + 2 * KB + l16 : L + L_ONES;
  lptr kr = L + BUF_B + HD * KB + l16, vrem = vl ? L + BUF_B + 2 * KB + l16 : L + L_ONES;
  Fast F; f32x16 mk;
  make_mask(mk, lane, false);
  fast_diag(F, Q, kl + tq * 4096, vloc + tq * vstride, mk);
  diag();
  if (tq > 0) fast_run(F, Q, kl, vloc, vstride, 0, tq, F.nm, false, []() {});
  f32x16 c0;
#pragma unroll
  for (int i = 0; i < 16; ++i) c0[i] = opf(g >= 2 ? __builtin_fminf(F.nm[0], NEG_BIG - mk[i] * 2.f) : F.nm[0]);
  mid();
  if (g >= 1) fast_run(F, Q, kr, vrem, vstride, g >= 2 ? tq : 4, 8, c0, hk, hook);
  else if (hk) hook();
  if (__any(!(F.O[HD ? 0 : 8] <= SUM_LIMIT))) {
    Attn A;
    make_mask(mk, opv(lane), false);
    attn_chunk<true>(A, Q, kl, vloc, vstride, 0, tq, tq, mk);
    if (g >= 1) { make_mask(mk, opv(lane), true); attn_chunk<false>(A, Q, kr, vrem, vstride, g >= 2 ? tq + 1 : 4, 8, g >= 2 ? tq : -1, mk); }
    F.O = A.O;
  }
  return attn_out<HD>(F.O);
}

DI f32x16 unpack_res(const v4u (&res)[2]) {
  f32x16 a;
#pragma unroll
  for (int e = 0; e < 2; ++e)
#pragma unroll
    for (int j = 0; j < 4; ++j) { a[8 * e + 2 * j] = bflo(res[e][j]); a[8 * e + 2 * j + 1] = bfhi(res[e][j]); }
  return a;
}
DI void finish_tile(lptr xp, const f32x16& keep, v4u (&res)[2], float (&v)[16]) {
#pragma unroll
  for (int qd = 0; qd < 4; ++qd) {
    const f32x4 r = *(LAS f32x4*)(xp + qd * 1024);
#pragma unroll
    for (int j = 0; j < 4; ++j) v[4 * qd + j] = keep[4 * qd + j] + r[j];
  }
#pragma unroll
  for (int e = 0; e < 2; ++e) res[e] = v4u{pk(v[8 * e], v[8 * e + 1]), pk(v[8 * e + 2], v[8 * e + 3]), pk(v[8 * e + 4], v[8 * e + 5]), pk(v[8 * e + 6], v[8 * e + 7])};
}
DI void send_tile(lptr xp, const f32x16& send) {
#pragma unroll
  for (int qd = 0; qd < 4; ++qd) *(LAS f32x4*)(xp + qd * 1024) = f32x4{send[4 * qd], send[4 * qd + 1], send[4 * qd + 2], send[4 * qd + 3]};
}

__global__ void __launch_bounds__(NTHR, 2) fused_layers(Params p) {
  extern __shared__ __attribute__((aligned(16))) unsigned char smem[];
  lptr L = (lptr)smem;
  const int tid = threadIdx.x, wave0 = __builtin_amdgcn_readfirstlane(tid >> 6);
  const int b = blockIdx.x, me0 = wg_index(b);
  unsigned long long pc0;
  asm volatile("s_getpc_b64 %0" : "=s"(pc0));
  unsigned char* ws = p.ws;
  const rsrc_t rs = __builtin_amdgcn_make_buffer_rsrc(ws, 0, WS_BYTES, 0x00020000);
  bool dead = false;
#define LANE_VALS() const int lane = opv(tid) & 63, r32 = lane & 31, hf = lane >> 5, l16 = lane * 16; (void)r32; (void)hf; (void)l16
#define WG_VALS() const int me = ops(me0), wave = ops(wave0), g = me >> 4, m = me & 15, th = m >> 3, fb = m & 7; (void)g; (void)m; (void)th; (void)fb; (void)wave
#define CW_VALS() LANE_VALS(); WG_VALS(); const int w = wave, ttr = 4 * g + 2 * th + (w & 1), kh = w >> 1, ftile = 2 * fb + kh, tta = 4 * g + w, \
    akv = A_KV + ops(layer) * KV_LAYER, ao = A_OT + ops(layer) * O_LAYER, ax1 = A_XT + ops(layer) * 2 * X_STEP, as1 = A_ST + ops(layer) * 2 * S_STEP, ah = A_HT + ops(layer) * H_LAYER, fl = ops(layer) * F_LAYER; \
    (void)ao; (void)ax1; (void)as1; (void)ah; (void)fl; const unsigned tag = epoch * 16u + ops(layer) * 8u; (void)ttr; (void)kh; (void)ftile; (void)tta; (void)akv; (void)tag
  if (tid < 16) { if (tid < 8) *(LAS unsigned*)(L + L_ONES + (tid >> 2) * 1024 + (tid & 3) * 4) = 0x3f803f80u; *(LAS unsigned*)(L + L_SY + tid * 4) = 0u; }
  BARL();

#define FILL_DEF() \
  auto fill_wo = [&](int nd) AI { \
    const rsrc_t ro = __builtin_amdgcn_make_buffer_rsrc((void*)p.wo, 0, 512 * 512 * 2, 0x00020000); \
    const int so_ = (wave * 2 + nd) * 16 * KB + fb * 128, lo_ = (wave < 4 ? BUF_D : BUF_A - 32 * KB) + (wave * 8 + nd * 4) * KB, vo_ = (lane >> 2) * KB + (lane & 3) * 16; \
    dma1p<0, 0>(ro, so_, lo_, vo_); dma1p<64, KB>(ro, so_, lo_, vo_); dma1p<256 * KB, 2 * KB>(ro, so_, lo_, vo_); dma1p<256 * KB + 64, 3 * KB>(ro, so_, lo_, vo_); \
  }; \
  auto fill0 = [&]() AI { \
    if (layer == 0) fill_wo(0); \
    if (layer == 1) { dma4<0, BUF_C>(rs, WS_WC + (m * 231 + wave * 4) * KB, wave * 4 * KB, l16); if (wave == 7) dma1<32 * KB, BUF_C + 32 * KB>(rs, WS_WC + m * 231 * KB, 0, l16); } \
  }; \
  auto fill1 = [&]() AI { fill_wo(1); }
#define GROUP_WAIT(KIND, DELAY, SEQ) do { if (w == 0) { __builtin_amdgcn_s_sleep(DELAY); poll(flag_ptr(ws, (KIND), g * 16 + (lane & 15)), lane < 16, tag, dead); lds_set(L, SY_GO, (SEQ)); } \
    else { if (CWSLP0) __builtin_amdgcn_s_sleep(DELAY); lds_wait<CWSLP>(L, SY_GO, (SEQ)); } } while (0)
  if (wave0 < 4) {
    v4u res[2];
    unsigned epoch = 0;
    STAMP(0);
#pragma unroll 1
    for (int layer = 0; layer < 2; ++layer) {
      const int sb = 2 + layer * 28;
      (void)sb;
      v4u xf[32];
      v4u Qa;
      {
        CW_VALS();
        f32x16 aK = {}, aV = {}, aQ = {};
        float mean, rstd;
        const int tro = tr_off(lane);
        constexpr int PQ = QS ? 4 : 6;
        v4u wq[PQ], wxk, wxv, xe;
        f32x4 rsv[4];
        lptr pc = L + opv(BUF_C + tro), px = L + opv(L_X + l16), prs = L + opv(L_RS + w * 128 + hf * 16);
        lptr kvl = L + opv(L_KV + w * 4096 + l16);
        const int kvo = ops(akv + (me * 4 + w) * 4096);
        auto put_stats = [&]() AI {
          xe = mean_frag(mean);
          *(LAS float*)(L + L_RS + w * 128 + r32 * 4) = rstd;
        };
        auto kv_pass = [&](auto FIRST_) AI {
          constexpr bool FIRST = decltype(FIRST_)::v;
          constexpr int WIN = KVWIN, PD = 3;
          v4u st[4];
          const int sto = ops(A_ST + S_STEP + tta * 4096), xo = ops(FIRST ? tta * 32 * KB : A_XT + X_STEP + tta * 32 * KB);
          lptr tw = L + opv(L_KV + w * 4096 + (lane >> 3) * 128 + (((lane & 7) ^ (lane >> 3)) * 16)), tr = L + opv(L_KV + w * 4096 + r32 * 128);
          const int sw = (r32 & 7) ^ hf;
          auto xpose = [&](int j) AI {
#pragma unroll
            for (int i = 0; i < 4; ++i) ldsw(tw + i * KB, xf[4 * j + i]);
#pragma unroll
            for (int e = 0; e < 4; ++e) xf[4 * j + e] = ldsr(tr + ((2 * e) ^ sw) * 16);
          };
          const rsrc_t rx = __builtin_amdgcn_make_buffer_rsrc((void*)p.x, 0, 2048 * 1024, 0x00020000);
          const int vx = (lane >> 3) * KB + (lane & 7) * 16;
          auto xraw = [&](int i) AI { return __builtin_amdgcn_raw_buffer_load_b128(rx, vx + (i >> 2) * 128, xo + (i & 3) * 8 * KB, 0); };
          if constexpr (FIRST) {
            const unsigned epoch_v = __hip_atomic_load((gu32*)(ws + WS_EPOCH), RLX_AGENT);
            if (WARM0) __builtin_amdgcn_s_sleep(WARM0 + 2);
#pragma unroll
            for (int i = 0; i < XW0; ++i) xf[i] = xraw(i);
            SCHED();
            epoch = __builtin_amdgcn_readfirstlane(epoch_v) + 1u;
            if (w == 0) lds_set(L, SY_EP, epoch);
            xpose(0);
#pragma unroll
            for (int i = XW0; i < XW1; ++i) xf[i] = xraw(i);
            asm volatile("s_waitcnt lgkmcnt(0)" ::: "memory");
          }
          SCHED();
          BAR();
          v4u wk[PD], wv[PD];
          lptr pa = L + opv(BUF_A + tro), pb = L + opv(BUF_B + tro);
#pragma unroll
          for (int i = 0; i < PD; ++i) { wk[i] = trr(pa + 2 * i * KB); wv[i] = trr(pa + (2 * i + 1) * KB); }
          if constexpr (!FIRST) {
            const int vst = opv(hf * 2048 + r32 * 8);
            GROUP_WAIT(F_X2, PD_X2, 3);
            STAMP(sb + 0);
#pragma unroll
            for (int i = 0; i < 4; ++i) xf[i] = gldf(rs, l16, xo, i);
#pragma unroll
            for (int i = 0; i < 4; ++i) st[i] = gld_stats(rs, vst + i * 512, sto);
#pragma unroll
            for (int i = 4; i < WIN; ++i) xf[i] = gldf(rs, l16, xo, i);
            SCHED();
          }
          float s0 = 0.f, q0 = 0.f, q1 = 0.f, pivot = 0.f, gs = 0.f, gq = 0.f;
          sfor<32>([&](auto S_) AI {
            constexpr int s = decltype(S_)::v, n = s + PD;
            const v4u ck = wk[s % PD], cv = wv[s % PD];
            if constexpr (n == 16) BAR();
            aK = mfma(ck, xf[s], aK); PINA(aK); SCHED();
            if constexpr (n < 16) wk[s % PD] = trr(pa + 2 * n * KB); else if constexpr (n < 32) wk[s % PD] = trr(pb + 2 * (n - 16) * KB);
            SCHED();
            if constexpr (FIRST) {
              if constexpr (s + XW1 < 32) xf[s + XW1] = xraw(s + XW1);
              if constexpr (s + XW1 == 32) { const GAS v4u* xs = (const GAS v4u*)(p.x + (size_t)(ttr * 32 + r32) * 512 + ftile * 32 + hf * 8); res[0] = xs[0]; res[1] = xs[2]; }
            } else if constexpr (s + WIN < 32) xf[s + WIN] = gldf(rs, l16, xo, s + WIN);
            HOLD(ck); SCHED();
            aV = mfma(xf[s], cv, aV); PINA(aV); SCHED();
            if constexpr (n < 16) wv[s % PD] = trr(pa + (2 * n + 1) * KB); else if constexpr (n < 32) wv[s % PD] = trr(pb + (2 * (n - 16) + 1) * KB);
            if constexpr (FIRST && (s & 3) == 0 && s < 28) xpose(s / 4 + 1);
            HOLD(cv); SCHED();
            if constexpr (WO_EARLY && !FIRST && s == 16) lds_add(L, SY_X0, lane);
                        constexpr int QN = FIRST ? 0 : QS;
            if constexpr (FIRST && s == 32 - PQ) BAR();
            if constexpr (s >= 32 - QN) { aQ = mfma(wq[(s - 32 + QN) % PQ], xf[s - 32 + QN], aQ); PINA(aQ); SCHED(); }
            if constexpr (s >= 33 - PQ - QN) wq[(s - 33 + PQ + QN) % PQ] = trr(pc + (s - 33 + PQ + QN) * KB);
            if constexpr (s == 29) wxk = ldsr(px);
            if constexpr (s == 30) wxv = ldsr(px + KB);
            SCHED();
            if constexpr (FIRST) {
              if constexpr ((s & 1) == 0) gs = gq = 0.f;
#pragma unroll
              for (int e = 0; e < 4; ++e) { const unsigned u = xf[s][e]; gs = dot2(u, 0x3f803f80u, gs); gq = dot2(u, u, gq); }
              if constexpr (s & 1) {
                const float gm = gs * (1.f / 16.f);
                if constexpr (s == 1) pivot = gm;
                const float d = gm - pivot;
                q1 += __builtin_fmaf(-gm, gs, gq); s0 += d; q0 = __builtin_fmaf(d, d, q0);
              }
            } else if constexpr (s == 6) finish_stats(st, mean, rstd);
            else if constexpr (s == 8) put_stats();
            SCHED();
          });
          if constexpr (FIRST) {
            sfor<QS>([&](auto S_) AI {
              constexpr int s = decltype(S_)::v;
              aQ = mfma(wq[s % PQ], xf[s], aQ); PINA(aQ); SCHED();
              wq[(s + PQ - 1) % PQ] = trr(pc + (s + PQ - 1) * KB);
              if constexpr (s == 0) { float between; merge_halves<16>(pivot, s0, q0, mean, between); rstd = rstd_of(__builtin_fmaf(between, 16.f, xhalf_sum(q1))); }
              if constexpr (s == 3) put_stats();
              SCHED();
            });
            if constexpr (QS == 0) { { float between; merge_halves<16>(pivot, s0, q0, mean, between); rstd = rstd_of(__builtin_fmaf(between, 16.f, xhalf_sum(q1))); } put_stats(); }
          }
        };
        if (layer == 0) kv_pass(IC<1>{}); else kv_pass(IC<0>{});
        STAMP(sb + 1);
        v4u kvf;
        aK = mfma(wxk, xe, aK); aV = mfma(xe, wxv, aV); PINA(aK); PINA(aV);
        SCHED();
        auto q_pass = [&](auto Q0_) AI {
        constexpr int Q0 = decltype(Q0_)::v;
        sfor<33 - Q0>([&](auto S_) AI {
          constexpr int s = Q0 + decltype(S_)::v, n = s + PQ - 1;
          aQ = mfma(wq[s % PQ], s < 32 ? xf[s < 32 ? s : 0] : xe, aQ);
          PINA(aQ); SCHED();
          if constexpr (n < 32) wq[n % PQ] = trr(pc + n * KB);
          else if constexpr (n == 32) { if (layer == 0) lds_wait(L, SY_Q, 4); wq[n % PQ] = ldsr(px + 2 * KB); }
          HOLD(wq[s % PQ]); SCHED();
          if constexpr (s >= Q0 && s < Q0 + 4) rsv[s - Q0] = *(LAS f32x4*)(prs + (s - Q0) * 32);
          if constexpr (s >= Q0 + 1 && s < Q0 + 9) {
            constexpr int e = s - Q0 - 1, f = e >> 1, o = (f & 1) * 8 + (e & 1) * 4;
            if constexpr (f < 2) { kvf[(e & 1) * 2] = pk(fm(aK[o], rstd), fm(aK[o + 1], rstd)); kvf[(e & 1) * 2 + 1] = pk(fm(aK[o + 2], rstd), fm(aK[o + 3], rstd)); }
            else { const f32x4 r4 = rsv[o >> 2]; kvf[(e & 1) * 2] = pk(fm(aV[o], r4[0]), fm(aV[o + 1], r4[1])); kvf[(e & 1) * 2 + 1] = pk(fm(aV[o + 2], r4[2]), fm(aV[o + 3], r4[3])); }
            if constexpr (e & 1) { gst(rs, kvf, l16 + f * KB, kvo); ldsw(kvl + f * KB, kvf); }
          }
          SCHED();
        });
        };
        q_pass(IC<QS>{});
        const float qs = rstd * (0.25f * LOG2E);
        ldsw(L + L_QB + w * KB + l16, v4u{pk(fm(aQ[8], qs), fm(aQ[9], qs)), pk(fm(aQ[10], qs), fm(aQ[11], qs)), pk(fm(aQ[12], qs), fm(aQ[13], qs)), pk(fm(aQ[14], qs), fm(aQ[15], qs))});
        Qa = v4u{pk(fm(aQ[0], qs), fm(aQ[1], qs)), pk(fm(aQ[2], qs), fm(aQ[3], qs)), pk(fm(aQ[4], qs), fm(aQ[5], qs)), pk(fm(aQ[6], qs), fm(aQ[7], qs))};
        STAMP(sb + 2);
        if (KVPUB == 0) { WAITV(0); if (lds_last(L, SY_KV, lane)) put_flag(rs, ws, F_KV + layer, me, epoch * 16u + layer * 8u, lane); }
        BARL();
        STAMP(sb + 3);
      }
      {
        CW_VALS();
        FILL_DEF();
        auto kvpub = [&]() AI { WAITV(0); if (lds_last(L, SY_KV, lane)) put_flag(rs, ws, F_KV + layer, me, tag, lane); };
        if (KVPUB == 1) kvpub();
        if (w == 0 && g >= 1) {
          __builtin_amdgcn_s_sleep(PD_KV); poll(flag_ptr(ws, F_KV + layer, me - 16), lane == 0, tag, dead);
          const int src = akv + (me - 16) * 16 * KB;
          dma4<0, BUF_B + 16 * KB>(rs, src, 0, l16); dma4<4 * KB, BUF_B + 20 * KB>(rs, src, 0, l16);
          dma4<8 * KB, BUF_B + 24 * KB>(rs, src, 0, l16); dma4<12 * KB, BUF_B + 28 * KB>(rs, src, 0, l16);
        }
        STAMP(sb + 19);
        const v4u of = attn_item<0>(L, Qa, lane, w, g, []() {}, [&]() AI {
          STAMP(sb + 4);
          WAITV(0);
          BAR();
          STAMP(sb + 5);
          fill0();
        }, layer == 0, fill1);
        gst(rs, of, l16, ao + (tta * 32 + 2 * m) * KB);
        STAMP(sb + 6);
        WAITV(0);
        BAR();
        if (w == 0) put_flag(rs, ws, fl + F_O, me, tag, lane);
        STAMP(sb + 7);
      }
      {
        CW_VALS();
        const int oo = ops(ao + (ttr * 32 + 16 * kh) * KB);
        f32x16 keep = unpack_res(res), send = {};
        const int tro = tr_off(lane);
        lptr k0 = L + opv(BUF_D + 3 * kh * KB + tro), s0 = L + opv(BUF_D + (1 + kh) * KB + tro);
        lptr k1 = L + opv(BUF_A + 3 * kh * KB + tro), s1 = L + opv(BUF_A + (1 + kh) * KB + tro);
        lptr xs = L + opv(L_KV + w * 4096 + l16), xr = L + opv(L_KV + (w ^ 2) * 4096 + l16);
        const int so = ops(ax1 + (ttr * 32 + 2 * ftile) * KB), sto = ops(as1 + ttr * 4096 + ftile * 256);
        constexpr int PD = 3;
        v4u wk[PD], wsd[PD];
#pragma unroll
        for (int i = 0; i < PD; ++i) { wk[i] = trr(k0 + i * 4 * KB); wsd[i] = trr(s0 + i * 4 * KB); }
        PINA(keep);
        GROUP_WAIT(fl + F_O, PD_O, 3 * layer + 1);
        STAMP(sb + 8);
#pragma unroll
        for (int i = 0; i < 16; ++i) xf[i] = gldf(rs, l16, oo, i);
        SCHED();
        sfor<16>([&](auto S_) AI {
          constexpr int s = decltype(S_)::v, n = s + PD;
          const v4u cs = wsd[s % PD], ck = wk[s % PD];
          send = mfma(cs, xf[s], send); PINA(send); SCHED();
          if constexpr (n < 8) wsd[s % PD] = trr(s0 + n * 4 * KB); else if constexpr (n < 16) wsd[s % PD] = trr(s1 + (n - 8) * 4 * KB);
          HOLD(cs); SCHED();
          keep = mfma(ck, xf[s], keep); PINA(keep); SCHED();
          if constexpr (n < 8) wk[s % PD] = trr(k0 + n * 4 * KB); else if constexpr (n < 16) wk[s % PD] = trr(k1 + (n - 8) * 4 * KB);
          HOLD(ck); HOLD(xf[s]); SCHED();
        });
        send_tile(xs, send);
        lds_set(L, SY_EX + 4 * w, 2 * layer + 1);
        STAMP(sb + 9);
        lds_wait(L, SY_EX + 4 * (w ^ 2), 2 * layer + 1);
        float v[16];
        finish_tile(xr, keep, res, v);
        gst(rs, res[0], l16, so); gst(rs, res[1], l16 + KB, so);
        store_stats(rs, v, lane, sto);
        STAMP(sb + 10);
        WAITV(0);
        if (lds_last(L, SY_DR, lane)) put_flag(rs, ws, fl + F_X1, me, tag, lane);
        STAMP(sb + 11);
      }
      {
        CW_VALS();
        constexpr int WIN = CWIN;
        v4u st[4];
        const int sto = ops(as1 + tta * 4096), xo = ops(ax1 + tta * 32 * KB);
        const int vst = opv(hf * 2048 + r32 * 8);
        lds_wait(L, SY_HWL, 4 * layer + 4);
        f32x16 acc[3];
        acc[0] = f32x16{}; acc[1] = f32x16{}; acc[2] = f32x16{};
        float mean, rstd, c1, sd; v4u xe;
        lptr pbuf[4] = {L + BUF_A + l16, L + opv(BUF_B + l16), L + opv(BUF_C + l16), L + opv(BUF_D + l16)};
        constexpr int PQ = 4;
        v4u wq[PQ];
        {
          constexpr int PD = 3;
          v4u w0[PD], w1[PD];
          lptr p0 = pbuf[2], p1 = pbuf[1];
#pragma unroll
          for (int i = 0; i < PD; ++i) { w0[i] = ldsr(p0 + i * KB); w1[i] = ldsr(p1 + i * KB); }
          GROUP_WAIT(fl + F_X1, PD_X1, 3 * layer + 2);
          STAMP(sb + 12);
#pragma unroll
          for (int i = 0; i < 4; ++i) xf[i] = gldf(rs, l16, xo, i);
#pragma unroll
          for (int i = 0; i < 4; ++i) st[i] = gld_stats(rs, vst + i * 512, sto);
#pragma unroll
          for (int i = 4; i < WIN; ++i) xf[i] = gldf(rs, l16, xo, i);
          SCHED();
          sfor<33>([&](auto S_) AI {
            constexpr int s = decltype(S_)::v, n = s + PD;
            if constexpr (n == 17) BAR();
            const v4u xa = s < 32 ? xf[s < 32 ? s : 0] : xe;
            const v4u c0 = w0[s % PD], cc1 = w1[s % PD];
            acc[0] = mfma(c0, xa, acc[0]); PINA(acc[0]); SCHED();
            if constexpr (n < 33) w0[s % PD] = ldsr(p0 + n * KB);
            SCHED();
            if constexpr (s + WIN < 32) xf[s + WIN] = gldf(rs, l16, xo, s + WIN);
            HOLD(c0); SCHED();
            acc[1] = mfma(cc1, xa, acc[1]); PINA(acc[1]); SCHED();
            if constexpr (n < 33) w1[s % PD] = ldsr(p1 + n * KB);
            HOLD(cc1); SCHED();
            if constexpr (s >= 17) { acc[2] = mfma(wq[(s - 17) % PQ], xf[s - 17], acc[2]); PINA(acc[2]); SCHED(); }
            if constexpr (n >= 17) { wq[(n - 17) % PQ] = ldsr(pbuf[3] + (n - 17) * KB); if constexpr (s >= 17) HOLD(wq[(s - 17) % PQ]); SCHED(); }
            if constexpr (s == 6) { finish_stats(st, mean, rstd); xe = mean_frag(mean); c1 = -LOG2E * rstd; sd = __builtin_amdgcn_rcpf(rstd); }
            SCHED();
          });
        }
        STAMP(sb + 13);
        const int ho = ops(ah + (tta * 112 + 7 * m) * KB);
        float ev[8]; v4u hw4;
        auto epi = [&](const f32x16& a, int k, int tile) AI {
          const int e = k >> 1;
          if (!(k & 1)) ev[e] = __builtin_fmaf(__builtin_amdgcn_exp2f(a[e] * c1), sd, sd);
          else {
            ev[e] = fm(a[8 + e], __builtin_amdgcn_rcpf(ev[e]));
            if (e & 1) hw4[e >> 1] = pk(ev[e - 1], ev[e]);
            if (e == 7) gst(rs, hw4, l16 + (tile & 3) * KB, ho + (tile >> 2) * 4 * KB);
          }
        };
        constexpr int DWIN = 16;
        v4u hfr[32];
        const int hb = ops(ah + (ttr * 112 + 56 * kh) * KB), fbo = ops(fb);
        auto hld = [&](int s) AI {
          const int t = s >> 3, jj = s & 7;
          return gld(rs, l16 + (t & 3) * KB, hb + (((jj + fbo) & 7) * 7 + (t & 4)) * KB);
        };
        auto hpub = [&](unsigned nt) AI { if (lds_last(L, SY_H, lane)) put_flag(rs, ws, fl + F_H + nt - 2, me, tag + nt, lane); };
        sfor<149>([&](auto U_) AI {
          constexpr int u = decltype(U_)::v + 16, t = 2 + u / 33, s = u % 33, n = s + PQ - 1;
          constexpr int bufs[8] = {2, 1, 3, 0, 2, 1, 3, 0};
          const v4u cq = wq[u % PQ];
          if constexpr (u == 17) { BAR(); STAMP(sb + 27); }
          if constexpr (n == 33 && t < 6) { if constexpr (PUBMODE == 0 && t >= 4) WAITV(1); BAR(); }
          if constexpr (PUBMODE == 1 && t == 4 && s == HPUBS) { WAITV(1); hpub(2); }
          if constexpr (PUBMODE == 1 && t >= 5 && s == HPUBS) { WAITV(0); hpub(t - 1); }
          if constexpr (EARLY_FREE && t >= 3 && t <= 5 && s == 1) BAR();
          if constexpr (t == 6 && s == 17) { if constexpr (PUBMODE == 0) WAITV(1); BAR(); }
          if constexpr (s == 0) acc[t % 3] = mfma(cq, xf[0], f32x16{});
          else acc[t % 3] = mfma(cq, s < 32 ? xf[s < 32 ? s : 0] : xe, acc[t % 3]);
          PINA(acc[t % 3]); SCHED();
          if constexpr (n < 33) wq[(u + PQ - 1) % PQ] = ldsr(pbuf[bufs[t]] + n * KB);
          else if constexpr (t < 6) wq[(u + PQ - 1) % PQ] = ldsr(pbuf[bufs[t + 1]] + (n - 33) * KB);
          SCHED();
          if constexpr (t == 2) { if constexpr (s >= 17) epi(acc[0], s - 17, 0); }
          else if constexpr (t == 3) { if constexpr (s < 16) epi(acc[1], s, 1); if constexpr (s >= 17) epi(acc[2], s - 17, 2); }
          else if constexpr (s >= 1 && s < 17) epi(acc[(t - 1) % 3], s - 1, t - 1);
          if constexpr (t == 6 && s >= 17 && (s - 17) % (16 / HPRE) == 0) hfr[(s - 17) / (16 / HPRE)] = hld((s - 17) / (16 / HPRE));
          if constexpr (s == 32 && t < 6) STAMP(sb + 21 + t);
          HOLD(cq); if constexpr (t == 6 && s < 32) HOLD(xf[s < 32 ? s : 0]);
          SCHED();
        });
        STAMP(sb + 14);
        BAR();
        STAMP(sb + 15);
        f32x16 keep = unpack_res(res), send = {};
        constexpr int PD = PDD;
        static constexpr int dbufs[7] = {0, 2, 1, 3, 0, 2, 1};
        const int ko = 3 * kh * KB, sdo = (1 - 2 * kh) * KB;
        lptr kb[4] = {pbuf[0] + ko, pbuf[1] + ko, pbuf[2] + ko, pbuf[3] + ko};
        lptr sp[4] = {kb[0] + sdo, kb[1] + sdo, kb[2] + sdo, kb[3] + sdo};
        v4u wk[PD], wsd[PD];
#pragma unroll
        for (int i = 0; i < PD; ++i) { wk[i] = ldsr(kb[dbufs[0]] + i * 4 * KB); wsd[i] = ldsr(sp[dbufs[0]] + i * 4 * KB); }
        constexpr auto nld = [](int s) { int c = 0; for (int i = 0; i < s; ++i) c += (i + HPRE < 24 && i < 13) + (i >= 13 && i < 45) + (HPRE == 8 && i < 6 && (i & 1)); return c; };
        sfor<56>([&](auto S_) AI {
          constexpr int s = decltype(S_)::v, n = s + PD;
          constexpr int ld = s + HPRE < 24 && s < 13 ? s + HPRE : -1, ld2 = (s >= 13 && s < 45) ? s + 11 : -1, ld3 = (HPRE == 8 && s < 6 && (s & 1)) ? 21 + s / 2 : -1;
          const v4u ck = wk[s % PD], cs = wsd[s % PD];
          if constexpr (n < 56 && (n & 7) == 0) {
            if constexpr (n == 8 && H6S >= 8 - PD) { if constexpr (HPRE == 16) WAITV(16); else WAITV(8); }
            if constexpr (n == 24 && H7S >= 24 - PD) asm volatile("s_waitcnt vmcnt(%0)" :: "n"(nld(24 - PD) - nld(16 / E6)) : "memory");
            BAR();
            if constexpr (n >= 24 && n <= 40) STAMP(sb + 17 + n / 8);
            if constexpr (n == 8 || n == 16) STAMP(58 + 2 * layer + n / 16);
          }
          send = mfma(cs, hfr[s % 32], send); PINA(send); SCHED();
          if constexpr (n < 56) wsd[s % PD] = ldsr(sp[dbufs[n >> 3]] + (n & 7) * 4 * KB);
          SCHED();
          if constexpr (ld >= 0) hfr[ld % 32] = hld(ld);
          if constexpr (ld2 >= 0) hfr[ld2 % 32] = hld(ld2);
          if constexpr (ld3 >= 0) hfr[ld3 % 32] = hld(ld3);
          if constexpr (s < 16 / E6) { sfor<E6 / 2>([&](auto K_) AI { epi(acc[6 % 3], E6 * s + decltype(K_)::v, 6); }); }
          HOLD(cs); SCHED();
          keep = mfma(ck, hfr[s % 32], keep); PINA(keep); SCHED();
          if constexpr (n < 56) wk[s % PD] = ldsr(kb[dbufs[n >> 3]] + (n & 7) * 4 * KB);
          HOLD(ck); HOLD(hfr[s % 32]); SCHED();
          if constexpr (s < 16 / E6) { sfor<E6 / 2>([&](auto K_) AI { epi(acc[6 % 3], E6 * s + E6 / 2 + decltype(K_)::v, 6); }); }
          if constexpr (H6S < 8 - PD && s == H6S) { if constexpr (HPRE == 16) WAITV(16); else WAITV(8); hpub(6); }
          if constexpr (H7S < 24 - PD && s == H7S) { asm volatile("s_waitcnt vmcnt(%0)" :: "n"(nld(H7S + 1) - nld(16 / E6)) : "memory"); hpub(7); }
          SCHED();
        });
        {
          CW_VALS();
          send_tile(L + L_KV + w * 4096 + l16, send);
          lds_set(L, SY_EX + 4 * w, 2 * layer + 2);
          STAMP(sb + 16);
          lptr xr = L + opv(L_KV + (w ^ 2) * 4096 + l16);
          const int so = ops(A_XT + X_STEP + (ttr * 32 + 2 * ftile) * KB), sto2 = ops(A_ST + S_STEP + ttr * 4096 + ftile * 256);
          GAS v4u* od = (GAS v4u*)(p.out + (size_t)(ttr * 32 + r32) * 512 + ftile * 32 + hf * 8);
          asm volatile("" : "+v"(od));
          lds_wait(L, SY_EX + 4 * (w ^ 2), 2 * layer + 2);
          float v[16];
          finish_tile(xr, keep, res, v);
          if (layer == 0) {
            gst(rs, res[0], l16, so); gst(rs, res[1], l16 + KB, so);
            store_stats(rs, v, lane, sto2);
            STAMP(sb + 17);
            WAITV(0);
            if (lds_last(L, SY_DR, lane)) put_flag(rs, ws, F_X2, me, tag + 8u, lane);
            STAMP(sb + 18);
          } else {
            od[0] = res[0]; od[2] = res[1];
            STAMP(sb + 17);
          }
        }
      }
    }
  } else {
    constexpr int KS = 16 * 1536 * 2;
#define HW_VALS() LANE_VALS(); WG_VALS(); const int hw = wave - 4, lb = hw * 8 * KB, wc = WS_WC + m * 231 * KB + lb, wdn = WS_WD + fb * 224 * KB + lb, \
    akv = A_KV + ops(layer) * KV_LAYER, ao = A_OT + ops(layer) * O_LAYER, fl = ops(layer) * F_LAYER, hco = WS_FLAGS + ((fl + F_H) * 256 + g * 16) * FLAG_STRIDE; (void)ao; const unsigned tag = epoch * 16u + ops(layer) * 8u; \
    (void)hw; (void)lb; (void)wc; (void)wdn; (void)akv; (void)tag; (void)hco
#define RAW_VALS() const rsrc_t rw = __builtin_amdgcn_make_buffer_rsrc((void*)p.wqkv, 0, 512 * 1536 * 2, 0x00020000); \
    const int vraw = (lane >> 2) * 3072 + (lane & 3) * 16, wkv = hw * 4 * KS + 64 * m, wqq = hw * 8 * KS + 64 * m; (void)wkv
#define DMA(EXTRA, base, G, BUF) dma_chunk<EXTRA, G, BUF>(rs, base, lb, l16)
#define DMA_Q() sfor<8>([&](auto I_) AI { constexpr int i = decltype(I_)::v; dma1p<i * KS, BUF_C + i * KB>(rw, wqq, lb, vraw); })
#define STAGE(arr, base, G) do { const int o_ = ops(base) + (G); _Pragma("unroll") for (int i_ = 0; i_ < 8; ++i_) arr[i_] = gldf(rs, l16, o_, i_); } while (0)
#define ZERO(arr) do { _Pragma("unroll") for (int i_ = 0; i_ < 8; ++i_) arr[i_] = v4u{0u, 0u, 0u, 0u}; } while (0)
#define PIN(arr) asm volatile("" : "+v"(arr[0]), "+v"(arr[1]), "+v"(arr[2]), "+v"(arr[3]), "+v"(arr[4]), "+v"(arr[5]), "+v"(arr[6]), "+v"(arr[7]))
#define PUT(arr, BUF) do { if (DBG_NOPUT) { PIN(arr); break; } lptr d_ = L + opv((BUF) + lb + l16); _Pragma("unroll") for (int i_ = 0; i_ < 8; ++i_) ldsw(d_ + i_ * KB, arr[i_]); } while (0)
#define GET(arr, BUF) do { lptr d_ = L + opv((BUF) + lb + l16); _Pragma("unroll") for (int i_ = 0; i_ < 8; ++i_) arr[i_] = ldsr(d_ + i_ * KB); } while (0)
#define WARM_K() do { if (hw < 2) touch<0>(rs, akv + me * 16 * KB + hw * 8 * KB, lane * 128); } while (0)
#define WARM_N() do { if (hw < 2) touch<A_KV + KV_LAYER>(rs, me * 16 * KB + hw * 8 * KB, lane * 128); } while (0)
#define WARM_O() do { if (hw == 1) touch<0>(rs, ao + (4 * g * 32 + 2 * m) * KB, (lane >> 4) * 32 * KB + (lane & 15) * 128); } while (0)
#define WARM_X() WARM_XS(2 * layer)
#define WARM_Y() WARM_XS(1)
#define WARM_XS(k) do { if (hw == 1) touch<A_XT>(rs, (k) * X_STEP + ((4 * g + 2 * th) * 32 + 4 * fb) * KB, (lane >> 5) * 32 * KB + (lane & 31) * 128); \
    if (hw == 2) touch<A_ST>(rs, (k) * S_STEP + (4 * g + 2 * th) * 4096 + fb * 512, lane < 8 ? (lane >> 2) * 4096 + (lane & 3) * 128 : WS_BYTES); } while (0)
#define WARM_S() WARM_SS(2 * layer)
#define WARM_T() WARM_SS(1)
#define WARM_SS(k) do { if (hw == 2) touch<A_ST>(rs, (k) * S_STEP + (4 * g + 2 * th) * 4096 + fb * 512, lane < 8 ? (lane >> 2) * 4096 + (lane & 3) * 128 : WS_BYTES); } while (0)
#define WARM_A() do { if (hw == 0) touch<0>(rw, b * 6 * KB, lane < 48 ? lane * 128 : WS_BYTES); \
    if (hw == 1) { const rsrc_t rx_ = __builtin_amdgcn_make_buffer_rsrc((void*)p.x, 0, 2048 * 1024, 0x00020000); touch<0>(rx_, b * 8 * KB, lane * 128); } } while (0)
#define WARM_U() do { if (b < 168) { const bool gu_ = b < 112; const int r_ = b - 112; \
    const rsrc_t ru_ = __builtin_amdgcn_make_buffer_rsrc((void*)(gu_ ? ((hw & 2) ? p.wu : p.wg) : p.wd), 0, 1792 * 1024, 0x00020000); \
    touch<0>(ru_, gu_ ? ((b & 3) * 128 + (hw & 1) * 64) * 3584 + (b >> 2) * 128 : ((r_ % 7) * 256 + hw * 64) * 1024 + (r_ / 7) * 128, lane * (gu_ ? 3584 : 1024)); } } while (0)
#define WARM_I() do { if (hw == 2) { const rsrc_t rc_ = __builtin_amdgcn_make_buffer_rsrc((void*)pc0, 0, CODE_BYTES, 0x00020000); \
    touch<0>(rc_, (b >> 3) * 14 * 128, lane < 14 ? lane * 128 : WS_BYTES); } } while (0)
#define WARM_P() do { if (hw == 3) touch<0>(rs, (b & 63) * 128, lane * (1 << PGSH)); } while (0)
#define WARM_Q() do { if (hw == 1) { const rsrc_t rq_ = __builtin_amdgcn_make_buffer_rsrc((void*)p.out, 0, 2048 * 1024, 0x00020000); touch<0>(rq_, (4 * g + 2 * th) * 32 * KB + fb * 128, lane * KB); } } while (0)
#define WARM_H() touch<A_HT>(rs, layer * H_LAYER + ((4 * g + hw) * 112 + 7 * m) * KB, lane < 56 ? lane * 128 : WS_BYTES)
#define WARM_F() do { if (hw == 2) touch<WS_FLAGS>(rs, me * FLAG_STRIDE, lane < F_KINDS ? lane * 256 * FLAG_STRIDE : WS_BYTES); } while (0)
#define WARM_W() do { if (hw == 3) { const rsrc_t ro_ = __builtin_amdgcn_make_buffer_rsrc((void*)p.wo, 0, 512 * 512 * 2, 0x00020000); touch<0>(ro_, b * 2 * KB, lane < 16 ? lane * 128 : WS_BYTES); } } while (0)
#define SAVE(BUF, dst) do { v4u t_[8]; GET(t_, BUF); const int o_ = ops((dst) + lb); _Pragma("unroll") for (int i_ = 0; i_ < 8; ++i_) gst(rs, t_[i_], l16 + (i_ & 3) * KB, o_ + (i_ >> 2) * 4 * KB); } while (0)
#define HFLAG(nt) do { if (hw == 1) put_flag(rs, ws, fl + F_H + (nt) - 2, me, tag + (nt), lane); } while (0)
#define BARL8() asm volatile("s_waitcnt lgkmcnt(8)\n\ts_barrier" ::: "memory")
#define DBG_NOPUT 0
#define HISSUE(nt) do { if (hw == 0) hpoll_issue<nt>(rs, hco, lane); } while (0)
#define HGATE(nt, younger) do { if (hw == 0) { WAITV(younger); hgate<nt>(rs, L, hco, lane, tag, dead); } } while (0)
    v4u a0[8], a1[8];
    {
      const int layer = 0; const unsigned epoch = 0;
      HW_VALS(); RAW_VALS();
      if (layer == 0) { WARM_I(); } if (layer == 0) { WARM_A(); } if (layer == 0) { WARM_W(); }
      if (WARM0) {
        const int r = b >> 3;
        if (hw < 3) touch<0>(rw, r * 16 * 3072 + hw * 1024 + (b & 1) * 512, (lane >> 2) * 3072 + (lane & 3) * 128);
        else {
          const rsrc_t rx = __builtin_amdgcn_make_buffer_rsrc((void*)p.x, 0, 2048 * 1024, 0x00020000);
          touch<0>(rx, (((b & 7) >> 1) * 512 + r * 16) * KB, lane * 128); touch<8 * KB>(rx, (((b & 7) >> 1) * 512 + r * 16) * KB, lane * 128);
        }
        __builtin_amdgcn_s_sleep(WARM0);
      }
      sfor<16>([&](auto I_) AI { constexpr int i = decltype(I_)::v; dma1p<((i >> 3) * 16 + ((i & 7) >> 1)) * KS + ((i & 1) ? 2048 : 1024), (i >> 3) * CH + (i & 7) * KB>(rw, wkv, lb, vraw); });
      if (QAT == 0) DMA_Q();
      STAMP(0);
      if (QAT == 0) WAITV(16); else WAITV(8);
      BAR();
      if (QAT == 1) DMA_Q();
      STAMP(1);
      __builtin_amdgcn_s_setprio(HPRIO);
      const int tro = tr_off(lane);
      lptr xp = L + L_X + (hw >> 1) * 512 + (hw & 1) * 8 + l16;
      float c[4] = {0.f, 0.f, 0.f, 0.f};
      {
        lptr ta = L + opv(BUF_A + lb + tro);
#pragma unroll
        for (int i = 0; i < 4; ++i) { c[0] = sum8(trr(ta + 2 * i * KB), c[0]); c[1] = sum8(trr(ta + (2 * i + 1) * KB), c[1]); }
      }
      GET(a0, BUF_A); PIN(a0);

      if (QAT == 2) WAITV(0); else WAITV(8);
      BAR();
      if (QAT == 2) DMA_Q();
      STAMP(2);
      {
        lptr tb = L + opv(BUF_B + lb + tro);
#pragma unroll
        for (int i = 0; i < 4; ++i) { c[2] = sum8(trr(tb + 2 * i * KB), c[2]); c[3] = sum8(trr(tb + (2 * i + 1) * KB), c[3]); }
        const unsigned hk = hilo(xhalf_sum(c[0] + c[2])), hv = hilo(xhalf_sum(c[1] + c[3]));
        if (lane < 32) { *(LAS v2u*)xp = v2u{hk, hk}; *(LAS v2u*)(xp + KB) = v2u{hv, hv}; }
      }
      STAMP(5);
      if (b < 168) {
        const bool gu = b < 112;
        const int r = b - 112, pitch = gu ? 3584 : 1024;
        const rsrc_t r0 = __builtin_amdgcn_make_buffer_rsrc((void*)(gu ? p.wg : p.wd), 0, 1792 * 1024, 0x00020000);
        const rsrc_t r1 = __builtin_amdgcn_make_buffer_rsrc((void*)(gu ? p.wu : p.wd), 0, 1792 * 1024, 0x00020000);
        const int vu = (lane >> 3) * pitch + (lane & 7) * 16;
        const int s0 = gu ? ((b & 3) * 128 + 32 * hw) * 3584 + (b >> 2) * 128 : ((r % 7) * 256 + 32 * hw) * 1024 + (r / 7) * 128, s1 = gu ? s0 : s0 + 128 * 1024;
#pragma unroll
        for (int i = 0; i < 4; ++i) { if (WARMC && i == 0 && gu) touch<WS_WC + 32 * KB>(rs, ((b >> 2) * 4 + hw) * 33 * KB + ((b & 3) >> 1) * 512, lane < 4 ? lane * 128 : WS_BYTES); dma1p<0, BUF_D>(r0, s0 + i * 8 * pitch, (4 * hw + i) * KB, vu); dma1p<0, BUF_D + 16 * KB>(r1, s1 + i * 8 * pitch, (4 * hw + i) * KB, vu); }
      }
      if (b < 168) WAITV(8); else WAITV(0);
      BARL();
      STAMP(7);
      {
        lptr tc = L + opv(BUF_C + lb + tro);
        c[0] = c[1] = c[2] = c[3] = 0.f;
#pragma unroll
        for (int i = 0; i < 8; ++i) c[i & 3] = sum8(trr(tc + i * KB), c[i & 3]);
        const unsigned hq = hilo(xhalf_sum((c[0] + c[1]) + (c[2] + c[3])));
        if (lane < 32) *(LAS v2u*)(xp + 2 * KB) = v2u{hq, hq};
        lds_add(L, SY_Q, lane);
      }
      if (IMG && g == 0) SAVE(BUF_C, WS_IQ + m * 32 * KB);
      if (layer == 0) { WARM_F(); }
      GET(a1, BUF_B); PIN(a1);
      __builtin_amdgcn_s_setprio(0);
      STAMP(3);
    }
    const unsigned epoch = __builtin_amdgcn_readfirstlane(*(volatile LAS unsigned*)(L + L_SY + SY_EP));
#pragma unroll 1
    for (int layer = 0; layer < 2; ++layer) {
      const int sb = 2 + layer * 28;
      (void)sb;
      v4u d2[8], d3[8], d4[8], d5[8], d6[8];
      if (layer == 1) {
        HW_VALS();
        BARL();
      if (b == 0 && hw == 2) {
        for (unsigned n = 0; n < (1u << 16); ++n) {
          gu32* f = flag_ptr(ws, F_P2, lane);
          const unsigned v0 = __hip_atomic_load(f, RLX_AGENT), v1 = __hip_atomic_load(f + 16 * FLAG_STRIDE, RLX_AGENT);
          const unsigned v2 = __hip_atomic_load(f + 32 * FLAG_STRIDE, RLX_AGENT), v3 = __hip_atomic_load(f + 48 * FLAG_STRIDE, RLX_AGENT);
          if (__all(v0 == epoch && v1 == epoch && v2 == epoch && v3 == epoch)) break;
        }
        if (lane == 0) __hip_atomic_store((gu32*)(ws + WS_EPOCH), epoch, RLX_AGENT);
      }
        STAMP(sb + 0);
        WAITV(0);

        BAR();
        if (WO_EARLY) {
          const rsrc_t ro = __builtin_amdgcn_make_buffer_rsrc((void*)p.wo, 0, 512 * 512 * 2, 0x00020000);
          const int so_ = hw * 2 * 16 * KB + fb * 128, vo_ = (lane >> 2) * KB + (lane & 3) * 16;
          lds_wait<HWSLP>(L, SY_X0, 4);
          __builtin_amdgcn_s_sleep(WO_SLEEP);
          if (IMG) { DMA(false, WS_IO + fb * 64 * KB + lb, 0, BUF_D); DMA(false, WS_IO + fb * 64 * KB + lb, 32 * KB, BUF_A); }
          else sfor<16>([&](auto I_) AI { constexpr int i = decltype(I_)::v; dma1p<((i >> 3) * 8 + ((i & 7) >> 2)) * 16 * KB + ((i >> 1) & 1) * 256 * KB + (i & 1) * 64, (i < 8 ? BUF_D : BUF_A) + (i & 7) * KB>(ro, so_, lb, vo_); });
        }

        STAMP(sb + 1);
      }

      STAMP(sb + 2);
      BAR();
      {
        HW_VALS();

        const int tq = 3 - hw;
        const v4u Qb = ldsr(L + L_QB + tq * KB + l16);
        FILL_DEF();
        auto unit = [&]() AI {
          if (layer == 0 && b < 168) {
            const int bq = ops(b), r = bq - 112, ufb = r / 7, gi = lane >> 4, pp = lane & 3;
            const bool gu = bq < 112;
            lds_wait(L, SY_U2, 4);
            const int ku0 = gu ? 8 * (bq & 3) : 8 * (2 * (r % 7) + (hw >> 1));
            const int ubase = gu ? WS_WC + (((bq >> 2) * 4 + hw) * 33 + ku0) * KB : WS_WD + (ufb * 224 + (hw & 1)) * KB;
            float c0 = 0.f, c1 = 0.f;
            int j = ku0 / 7, t = ku0 % 7;
            lptr tp = L + BUF_D + (8 * (gi >> 1) + ((lane & 15) >> 2)) * 128 + (((pp & 1) << 1) | (pp >> 1)) * 8 +
                      (gu ? (gi & 1) * 16 * KB + hw * 32 : (hw >> 1) * 16 * KB + (hw & 1) * 64 + (gi & 1) * 32);
#pragma unroll
            for (int u = 0; u < 8; ++u) {
              const v2u lo = __builtin_bit_cast(v2u, __builtin_amdgcn_ds_read_tr16_b64_v4i16((LAS v4s*)(tp + u * 2 * KB)));
              const v2u hi = __builtin_bit_cast(v2u, __builtin_amdgcn_ds_read_tr16_b64_v4i16((LAS v4s*)(tp + u * 2 * KB + 512)));
              const v4u f = {lo.x, lo.y, hi.x, hi.y};
              gst(rs, f, l16, ubase + (gu ? u : (t * 8 + ((j - ufb) & 7)) * 4 + (j >> 3) * 2) * KB);
              if (++t == 7) { t = 0; ++j; }
              if (u & 1) c1 = sum8(f, c1); else c0 = sum8(f, c0);
            }
            if (gu) { const unsigned wd = hilo(xhalf_sum(c0 + c1)); if (lane < 32) gst2(rs, v2u{wd, wd}, l16, ubase + (32 - ku0) * KB + ((bq & 3) >> 1) * 512 + (bq & 1) * 8); }
          }
        };
        auto unit_w = [&]() AI { if (layer == 0 && ULOAD != 2) { if (hw == 3 && g >= 2) WAITV(16); else WAITV(0); lds_add(L, SY_U2, lane); } };
        if (hw == 3 && g >= 2) {
          __builtin_amdgcn_s_sleep(PD_KV + 4); poll(flag_ptr(ws, F_KV + layer, me - 32), lane == 0, tag, dead);
          const int s2 = akv + (me - 32) * 16 * KB;
          dma4<0, BUF_B>(rs, s2, 0, l16); dma4<4 * KB, BUF_B + 4 * KB>(rs, s2, 0, l16);
          dma4<8 * KB, BUF_B + 8 * KB>(rs, s2, 0, l16); dma4<12 * KB, BUF_B + 12 * KB>(rs, s2, 0, l16);
        }
        const v4u of = attn_item<1>(L, Qb, lane, tq, g, unit_w, [&]() AI {
          if (hw == 3) WAITV(0);
          if (ULOAD == 2 && layer == 0) { WAITV(0); lds_add(L, SY_U2, lane); }
          unit();
          STAMP(sb + 4);
          BARL();
          if (layer == 1) STAGE(d6, wdn, 6 * 32 * KB); else ZERO(d6);
          fill0();
        }, layer == 0, [&]() AI { if (P2HOOK) { WAITV(4); if (lds_last(L, SY_U, lane)) put_flag(rs, ws, F_P2, me, epoch, lane); } fill1(); });
        gst(rs, of, l16, ao + ((4 * g + tq) * 32 + 2 * m + 1) * KB);
        STAMP(sb + 6);
        WAITV(0);

        BAR();
      }
      {
      HW_VALS();
      if (IMG && layer == 0 && me < 8) { SAVE(BUF_D, WS_IO + fb * 64 * KB); SAVE(BUF_A, WS_IO + fb * 64 * KB + 32 * KB); }
      if (layer == 0) {
        if (hw == 0) {
          if (!P2HOOK) { put_flag(rs, ws, F_P2, me, epoch, lane); __builtin_amdgcn_s_sleep(PD_P2); }
          int qq;
          if (lane < 7) qq = 112 + 7 * fb + lane;
          else { int n = (7 * m) / 4 + ((lane - 7) >> 2); n = n > 27 ? 27 : n; qq = 4 * n + ((lane - 7) & 3); }
          poll(flag_ptr(ws, F_P2, lane < 19 ? wg_index(qq) : 0), lane < 19, epoch, dead);
          lds_set(L, SY_P2, 1);
        } else lds_wait(L, SY_P2, 1);
        DMA(true, wc, 0, BUF_C);
      }

      DMA(true, wc, 1 * CH, BUF_B); STAGE(d3, wdn, 3 * 32 * KB);
      STAMP(sb + 7);
      lds_wait<HWSLP>(L, SY_GO, 3 * layer + 1);
      __builtin_amdgcn_s_sleep(SL1);
      WAITV(8);
      lds_add(L, SY_HWL, lane);
      STAGE(d5, wdn, 5 * 32 * KB); STAGE(d4, wdn, 4 * 32 * KB);
      STAMP(sb + 9);
      lds_wait<HWSLP>(L, SY_DR, 8 * layer + 4);
      if (IMG && layer == 0 && g == 0) { WAITV(0); if (lds_last(L, SY_I, lane)) put_flag(rs, ws, F_P3, me, epoch, lane); }

      DMA(true, wc, 2 * CH, BUF_D);
      STAMP(sb + 11);
      WAITV(0); BAR();
      DMA(true, wc, 3 * CH, BUF_A); STAGE(d2, wdn, 2 * 32 * KB);
      BAR();
      STAMP(sb + 13);
      DMA(true, wc, 4 * CH, BUF_C);
      WAITV(16); BAR();
      DMA(true, wc, 5 * CH, BUF_B); if (layer == 0) STAGE(d6, wdn, 6 * 32 * KB);
      }
      {
      HW_VALS();
      WAITV(8); BAR();

      DMA(true, wc, 6 * CH, BUF_D);
      WAITV(8); BAR();
      if (PUBMODE == 0) HFLAG(3);
      DMA(false, wdn, 0 * 32 * KB, BUF_A);
      WAITV(8); BAR();
      if (PUBMODE == 0) HFLAG(4);
      HISSUE(L2);
      DMA(false, wdn, 1 * 32 * KB, BUF_C);
      HGATE(L2, 8);
      if (IMG && layer == 0 && hw == 2) poll(flag_ptr(ws, F_P3, lane == 0 ? m : fb), lane < 2, epoch, dead);
      STAMP(sb + 14);
      BAR();
      WAITV(0); PIN(d3); PIN(d5); PIN(d4); PIN(d2); PIN(d6);
      if (PUBMODE == 0) HFLAG(5);
      if (P4AT == 0) HISSUE(4);
      BAR();
      if (P4AT == 1) HISSUE(4);
      PUT(d2, BUF_B);

      if (P5AT == 0) HISSUE(5);
      BAR();
      if (H6S >= 8 - PDD) HFLAG(6);
      if (P5AT == 1) HISSUE(5);
      PUT(d3, BUF_D);
      HGATE(4, 1);
      STAMP(sb + 15);
      BARL8();
      PUT(d4, BUF_A);

      HGATE(5, 0);
      if (P6AT == 0) HISSUE(6);
      BARL8();
      if (H7S >= 24 - PDD) HFLAG(7);
      if (P6AT == 1) HISSUE(6);
      PUT(d5, BUF_C);
      HGATE(6, 0);
      if (P7AT == 0) HISSUE(7);
      BARL8();
      if (P7AT == 1) HISSUE(7);
      PUT(d6, BUF_B);

      if (P7AT == 2) HISSUE(7);
      HGATE(7, 0);
      STAMP(sb + 16);
      BARL8();
      BARL();
      if (layer == 0) {
        PUT(a0, BUF_A);
        lds_wait4(L, 2);
        PUT(a1, BUF_B);
        if (IMG) DMA(false, WS_IQ + m * 32 * KB + lb, 0, BUF_C); else { RAW_VALS(); DMA_Q(); }
      }
      }
    }
  }
}
}

extern "C" void kernel_launch(void* const* d_in, const int* in_sizes, int n_in, void* d_out, int out_size, void* d_ws, size_t ws_size,
                              hipStream_t stream) {
  Params p;
  p.x = (const u16*)d_in[0]; p.wqkv = (const u16*)d_in[1]; p.wo = (const u16*)d_in[2];
  p.wg = (const u16*)d_in[4]; p.wu = (const u16*)d_in[5]; p.wd = (const u16*)d_in[6];
  p.out = (u16*)d_out; p.ws = (unsigned char*)d_ws;
  (void)hipFuncSetAttribute((const void*)fused_layers, hipFuncAttributeMaxDynamicSharedMemorySize, LDS_BYTES);
  hipLaunchKernelGGL(fused_layers, dim3(NWG), dim3(NTHR), LDS_BYTES, stream, p);
}
```
